```python
import torch

def _softmax(x, axis=-1):
    return torch.softmax(x, dim=axis)
def _gelu(x):
    return torch.nn.functional.gelu(x, approximate="tanh")
def _silu(x):
    return torch.nn.functional.silu(x)
def _sigmoid(x):
    return torch.sigmoid(x)
def _layernorm(x, axis=-1, eps=1e-5):
    m = x.mean(dim=axis, keepdim=True)
    v = x.var(dim=axis, keepdim=True, unbiased=False)
    return (x - m) / torch.sqrt(v + eps)
def _scatter_add(tgt, idx, src, axis=0):
    return tgt.clone().index_add_(axis, idx, src)
def _segment_sum(src, seg, n):
    out = torch.zeros((n,) + src.shape[1:], dtype=src.dtype)
    return out.index_add_(0, seg, src)
def _pad(x, pw):
    flat = tuple(v for pair in reversed(pw) for v in pair)
    return torch.nn.functional.pad(x, flat)

kernel_name = 'standalone__gqa_block_cached__decode'

def setup_inputs():

    _w = (lambda a: (lambda t: t.to(torch.bfloat16) if t.is_floating_point() else t)(torch.as_tensor(a)))
    B, Sq, D = 8, 1, 512
    Hq, Hkv, Sk, Dh = 32, 8, 4096, 128
    assert Hq % Hkv == 0, (Hq, Hkv)
    return {
        "x": _w((lambda *s: torch.randn(*s, dtype=torch.bfloat16) * 0.8)(B, Sq, D)),
        "k_cache": _w((lambda *s: torch.randn(*s, dtype=torch.bfloat16) * 0.8)(B, Hkv, Sk, Dh)),
        "v_cache": _w((lambda *s: torch.randn(*s, dtype=torch.bfloat16) * 0.05)(B, Hkv, Sk, Dh)),
        "wq": _w((lambda *s: torch.randn(*s, dtype=torch.bfloat16) * 0.8)(D, Hq * Dh) * (D ** -0.5)),
        "wo": _w((lambda *s: torch.randn(*s, dtype=torch.bfloat16) * 0.05)(Hq * Dh, D) * ((Hq * Dh) ** -0.5)),
    }

def reference(x, k_cache, v_cache, wq, wo):
    B, Sq, D = x.shape
    Hq, Dh = 32, 128
    Hkv = k_cache.shape[1]
    rep = Hq // Hkv
    q = (lambda x, *d: x.permute(*d))((x @ wq).reshape(B, Sq, Hq, Dh), 0, 2, 1, 3)
    k = (lambda x, n, axis: torch.repeat_interleave(x, n, dim=axis))(k_cache, rep, axis=1)
    v = (lambda x, n, axis: torch.repeat_interleave(x, n, dim=axis))(v_cache, rep, axis=1)
    scores = torch.einsum("bhqd,bhkd->bhqk", q, k) * (Dh ** -0.5)
    p = _softmax(scores, axis=-1)
    o = torch.einsum("bhqk,bhkd->bhqd", p, v)
    return (lambda x, *d: x.permute(*d))(o, 0, 2, 1, 3).reshape(B, Sq, Hq * Dh) @ wo
```

```cpp
#include <hip/hip_runtime.h>
#include <hip/hip_bf16.h>

#define GAS __attribute__((address_space(1)))
#define LAS __attribute__((address_space(3)))
typedef unsigned u32x4 __attribute__((ext_vector_type(4)));
typedef unsigned u32x2 __attribute__((ext_vector_type(2)));
typedef float f32x4 __attribute__((ext_vector_type(4)));
typedef short s16x4 __attribute__((ext_vector_type(4)));
typedef __bf16 bf16x8 __attribute__((ext_vector_type(8)));
typedef __bf16 bf16x2 __attribute__((ext_vector_type(2)));
typedef float f32x2 __attribute__((ext_vector_type(2)));
typedef unsigned long long u64;

constexpr int NTHR = 576;
constexpr int AUX_NT = 2, AUX_SC1 = 16;
constexpr unsigned SENT = 0xFFFFFFFFu;
constexpr unsigned SPIN_CAP = 1u << 16;
constexpr int WS_QG = 4096;
constexpr int WS_P1 = WS_QG + 262144;
constexpr int WS_ST = WS_P1 + 524288;
constexpr int WS_P2 = WS_ST + 8192;
constexpr int WS_END = WS_P2 + 524288;
constexpr int L_IMG = 0;
constexpr int L_RED = 16384;
constexpr int L_Q = 32768;
constexpr int L_ML = L_Q + 1024;
constexpr int L_F = L_ML + 256;
constexpr int L_OI = L_F + 512;
constexpr int L_END = L_OI + 8 * 272;

constexpr float C2 = 0.08838834764831845f * 1.4426950408889634f;

#define STAMP(k) do { } while (0)

__device__ __forceinline__ f32x4 mfma(u32x4 a, u32x4 b, f32x4 c) {
  return __builtin_amdgcn_mfma_f32_16x16x32_bf16(__builtin_bit_cast(bf16x8, a), __builtin_bit_cast(bf16x8, b), c, 0, 0, 0);
}
template <int CTRL> __device__ __forceinline__ float dppf(float x) {
  return __builtin_bit_cast(float, __builtin_amdgcn_mov_dpp(__builtin_bit_cast(int, x), CTRL, 0xf, 0xf, true));
}
template <int CTRL> __device__ __forceinline__ unsigned dppu(unsigned x) {
  return (unsigned)__builtin_amdgcn_mov_dpp((int)x, CTRL, 0xf, 0xf, true);
}
constexpr int ROR4 = 0x124, ROR8 = 0x128, ROR12 = 0x12C, SHL4 = 0x104, SHL8 = 0x108, SHL12 = 0x10C, QID = 0xE4, XOR1 = 0xB1, XOR2 = 0x4E, HMIR = 0x141;
template <int BANKS> __device__ __forceinline__ float bank_set(float old, float y) {
  return __builtin_bit_cast(float, __builtin_amdgcn_update_dpp(__builtin_bit_cast(int, old), __builtin_bit_cast(int, y), QID, 0xf, BANKS, false));
}
__device__ __forceinline__ float x16max(float x) {
  auto s = __builtin_amdgcn_permlane16_swap(__float_as_uint(x), __float_as_uint(x), false, false);
  return fmaxf(__uint_as_float(s[0]), __uint_as_float(s[1]));
}
__device__ __forceinline__ float x32max(float x) {
  auto s = __builtin_amdgcn_permlane32_swap(__float_as_uint(x), __float_as_uint(x), false, false);
  return fmaxf(__uint_as_float(s[0]), __uint_as_float(s[1]));
}
__device__ __forceinline__ float x16sum(float x) {
  auto s = __builtin_amdgcn_permlane16_swap(__float_as_uint(x), __float_as_uint(x), false, false);
  return __uint_as_float(s[0]) + __uint_as_float(s[1]);
}
__device__ __forceinline__ float x32sum(float x) {
  auto s = __builtin_amdgcn_permlane32_swap(__float_as_uint(x), __float_as_uint(x), false, false);
  return __uint_as_float(s[0]) + __uint_as_float(s[1]);
}
__device__ __forceinline__ unsigned pack_bf16(float lo, float hi) {
  f32x2 v = {lo, hi};
  return __builtin_bit_cast(unsigned, __builtin_convertvector(v, bf16x2));
}
__device__ __forceinline__ u32x4 no_sent(f32x4 v) {
  u32x4 u = __builtin_bit_cast(u32x4, v);
  u.x = min(u.x, SENT - 1u); u.y = min(u.y, SENT - 1u); u.z = min(u.z, SENT - 1u); u.w = min(u.w, SENT - 1u);
  return u;
}
__device__ __forceinline__ bool has_sent(u32x4 u) { return max(max(u.x, u.y), max(u.z, u.w)) == SENT; }
__device__ __forceinline__ void wg_barrier() {
  asm volatile("s_waitcnt lgkmcnt(0)\n\ts_barrier" ::: "memory");
}
__device__ __forceinline__ __amdgpu_buffer_rsrc_t mk_rsrc(const void* p, int bytes) {
  return __builtin_amdgcn_make_buffer_rsrc((void*)p, 0, bytes, 0x00020000);
}
__device__ __forceinline__ u32x2 tr_read(LAS unsigned char* p) {
  return __builtin_bit_cast(u32x2, __builtin_amdgcn_ds_read_tr16_b64_v4i16((LAS s16x4*)p));
}
__device__ __forceinline__ int img_row(int k) { return (k & ~12) * 32 + ((k >> 2) & 1) * 256 + ((k >> 3) & 1) * 128; }

__global__ void __launch_bounds__(NTHR, 3)
gqa_decode_kernel(const unsigned char* __restrict__ x, const unsigned char* __restrict__ kc, const unsigned char* __restrict__ vc,
                  const unsigned char* __restrict__ wq, const unsigned char* __restrict__ wo, unsigned char* out,
                  unsigned char* ws) {
  __shared__ __attribute__((aligned(16))) unsigned char smem[L_END];
  LAS unsigned char* L = (LAS unsigned char*)smem;
  const int tid = threadIdx.x, lane = tid & 63, w = __builtin_amdgcn_readfirstlane(tid >> 6);
  const int g = blockIdx.x & 7, i = blockIdx.x >> 3;
  const int lg = lane >> 4, ln = lane & 15;
  const __amdgpu_buffer_rsrc_t rws = mk_rsrc(ws, WS_END);
  STAMP(0);

  if (w < 8) {
    const int b = i >> 2, s = i & 3;
    const size_t base = ((size_t)(b * 8 + g) * 4096 + s * 1024 + w * 128) * 256;
    const __amdgpu_buffer_rsrc_t rk = mk_rsrc(kc + base, 32768), rv = mk_rsrc(vc + base, 32768);
    const int koff = ((lane >> 2) & 3) * 256 + lg * 64 + (lane & 3) * 16;
    const int voff = lane * 16;
    u32x4 kr[32], vr[32];
#define LDK(u) kr[u] = __builtin_amdgcn_raw_buffer_load_b128(rk, koff + ((u) & 3) * 1024, ((u) >> 2) * 4096, AUX_NT)
#define LDV(u) vr[u] = __builtin_amdgcn_raw_buffer_load_b128(rv, voff + ((u) & 3) * 1024, ((u) >> 2) * 4096, AUX_NT)
#pragma unroll
    for (int u = 0; u < 28; ++u) {
      if (u >= 8 && (u & 3) == 0) __builtin_amdgcn_s_waitcnt(0x0F74);
      LDK(u);
      __builtin_amdgcn_sched_barrier(0);
    }
    STAMP(1);
    wg_barrier();
    STAMP(2);
    const u32x4 qf = *(LAS u32x4*)(L + L_Q + (lane & 3) * 256 + (4 * lg + ((lane >> 2) & 3)) * 16);
    float sc[8];
#pragma unroll
    for (int u0 = 0; u0 < 32; u0 += 4) {
      const int t = u0 >> 3;
      f32x4 d[4];
      float y[4];
#pragma unroll
      for (int k = 0; k < 4; ++k) d[k] = mfma(kr[u0 + k], qf, f32x4{0.f, 0.f, 0.f, 0.f});
      __builtin_amdgcn_sched_barrier(0);
#pragma unroll
      for (int k = 0; k < 4; ++k) y[k] = d[k][t] + dppf<ROR12>(d[k][(t + 1) & 3]);
      __builtin_amdgcn_sched_barrier(0);
#pragma unroll
      for (int k = 0; k < 4; ++k) y[k] += dppf<ROR8>(d[k][(t + 2) & 3]);
      __builtin_amdgcn_sched_barrier(0);
#pragma unroll
      for (int k = 0; k < 4; ++k) y[k] += dppf<ROR4>(d[k][(t + 3) & 3]);
      __builtin_amdgcn_sched_barrier(0);
#pragma unroll
      for (int k = 0; k < 4; ++k) {
        const int T = (u0 + k) & 7;
        sc[T] = t == 0 ? y[k] : t == 1 ? bank_set<2>(sc[T], y[k]) : t == 2 ? bank_set<4>(sc[T], y[k]) : bank_set<8>(sc[T], y[k]);
      }
      __builtin_amdgcn_sched_barrier(0);
#pragma unroll
      for (int k = 0; k < 4; ++k) {
        if (u0 == 0) LDK(28 + k);
        else if (u0 < 28) LDV(u0 - 4 + k);
      }
      __builtin_amdgcn_sched_barrier(0);
    }
    float m = fmaxf(fmaxf(fmaxf(sc[0], sc[1]), fmaxf(sc[2], sc[3])), fmaxf(fmaxf(sc[4], sc[5]), fmaxf(sc[6], sc[7])));
    m = fmaxf(m, dppf<ROR4>(m)); m = fmaxf(m, dppf<ROR8>(m)); m = x32max(x16max(m));
    m *= C2;
    float ls = 0.f;
#pragma unroll
    for (int T = 0; T < 8; ++T) { sc[T] = __builtin_amdgcn_exp2f(fmaf(sc[T], C2, -m)); ls += sc[T]; }
    ls += dppf<ROR4>(ls); ls += dppf<ROR8>(ls); ls = x32sum(x16sum(ls));
    if (lane < 4) { *(LAS float*)(L + L_ML + lane * 32 + w * 4) = m; *(LAS float*)(L + L_ML + 128 + lane * 32 + w * 4) = ls; }
    STAMP(3);
    wg_barrier();
    const f32x4 m0 = *(LAS f32x4*)(L + L_ML + (lane & 3) * 32), m1 = *(LAS f32x4*)(L + L_ML + (lane & 3) * 32 + 16);
    const float M = fmaxf(fmaxf(fmaxf(m0.x, m0.y), fmaxf(m0.z, m0.w)), fmaxf(fmaxf(m1.x, m1.y), fmaxf(m1.z, m1.w)));
    const float al = __builtin_amdgcn_exp2f(m - M);
    u32x4 pa;
#pragma unroll
    for (int k = 0; k < 4; ++k) pa[k] = pack_bf16(sc[2 * k] * al, sc[2 * k + 1] * al);
    f32x4 acc[8];
#pragma unroll
    for (int j = 0; j < 8; ++j) acc[j] = f32x4{0.f, 0.f, 0.f, 0.f};
#pragma unroll
    for (int t = 0; t < 4; ++t) {
      u32x4 a;
#pragma unroll
      for (int k = 0; k < 4; ++k) a[k] = t == 0 ? pa[k] : t == 1 ? dppu<SHL4>(pa[k]) : t == 2 ? dppu<SHL8>(pa[k]) : dppu<SHL12>(pa[k]);
#pragma unroll
      for (int j = 0; j < 8; ++j) {
        u32x4 bb;
#pragma unroll
        for (int k = 0; k < 4; ++k) bb[k] = __builtin_amdgcn_perm(vr[8 * t + 2 * k + 1][j >> 1], vr[8 * t + 2 * k][j >> 1], (j & 1) ? 0x07060302u : 0x05040100u);
        acc[j] = mfma(a, bb, acc[j]);
      }
      __builtin_amdgcn_sched_barrier(0);
      if (t == 0) {
#pragma unroll
        for (int u = 24; u < 32; ++u) LDV(u);
        __builtin_amdgcn_sched_barrier(0);
      }
    }
    if (lane < 16) {
#pragma unroll
      for (int j = 0; j < 8; ++j) *(LAS f32x4*)(L + L_RED + w * 2048 + j * 256 + lane * 16) = acc[j];
    }
    STAMP(4);
    wg_barrier();
    STAMP(5);
    if (w < 2) {
      f32x4 v = *(LAS f32x4*)(L + L_RED + tid * 16);
#pragma unroll
      for (int ww = 1; ww < 8; ++ww) v += *(LAS f32x4*)(L + L_RED + ww * 2048 + tid * 16);
      __builtin_amdgcn_raw_buffer_store_b128(no_sent(v), rws, WS_P1 + g * 65536 + (tid >> 5) * 16384 + i * 512 + (tid & 31) * 16, 0, AUX_SC1);
    }
    STAMP(6);
    const int rb = i >> 3, cb = i & 7;
    u32x4 wa[4];
    {
      LAS unsigned char* ip = L + L_IMG + (w & 3) * 4096 + (lg >> 1) * 512 + (lg & 1) * 128 + ln * 8;
#pragma unroll
      for (int ks = 0; ks < 4; ++ks) { const u32x2 lo = tr_read(ip + ks * 1024), hi = tr_read(ip + ks * 1024 + 256); wa[ks] = u32x4{lo.x, lo.y, hi.x, hi.y}; }
    }
    const f32x4 f0 = *(LAS f32x4*)(L + L_F + w * 64 + (lane >> 5) * 16), f1 = *(LAS f32x4*)(L + L_F + w * 64 + 32 + (lane >> 5) * 16);
    {
      const int off = WS_P1 + g * 65536 + rb * 16384 + w * 2048 + lane * 16;
      u32x4 p0, p1;
      for (unsigned spins = 0; spins < SPIN_CAP; ++spins) {
        asm volatile("" ::: "memory");
        p0 = __builtin_amdgcn_raw_buffer_load_b128(rws, off, 0, AUX_SC1);
        p1 = __builtin_amdgcn_raw_buffer_load_b128(rws, off + 1024, 0, AUX_SC1);
        if (!__any(has_sent(p0) || has_sent(p1))) break;
      }
      f32x4 o = __builtin_bit_cast(f32x4, p0) * f0 + __builtin_bit_cast(f32x4, p1) * f1;
      o.x = x32sum(o.x); o.y = x32sum(o.y); o.z = x32sum(o.z); o.w = x32sum(o.w);
      if (lane < 32) *(LAS u32x2*)(L + L_OI + w * 272 + lane * 8) = u32x2{pack_bf16(o.x, o.y), pack_bf16(o.z, o.w)};
    }
    STAMP(7);
    wg_barrier();
    if (w < 4) {
      f32x4 d = {0.f, 0.f, 0.f, 0.f};
#pragma unroll
      for (int ks = 0; ks < 4; ++ks) d = mfma(wa[ks], *(LAS u32x4*)(L + L_OI + (ln & 7) * 272 + ks * 64 + lg * 16), d);
      if (ln < 8) __builtin_amdgcn_raw_buffer_store_b128(no_sent(d), rws, WS_P2 + ln * 65536 + (cb * 4 + w) * 2048 + (g * 4 + rb) * 64 + lg * 16, 0, AUX_SC1);
    }
    STAMP(8);
  } else {
    const int gq = i & 7, sl = g * 4 + (i >> 3);
    const u64 epoch = __hip_atomic_load((GAS u64*)ws, __ATOMIC_RELAXED, __HIP_MEMORY_SCOPE_AGENT);
    u32x4 wv[16], xv[16];
    {
      const __amdgpu_buffer_rsrc_t rq = mk_rsrc(wq + gq * 1024 + sl * 32, 512 * 8192 - 8192 + 32), rx = mk_rsrc(x, 8192);
#pragma unroll
      for (int t = 0; t < 16; ++t) wv[t] = __builtin_amdgcn_raw_buffer_load_b128(rq, (lane >> 1) * 8192 + (lane & 1) * 16, t * 262144, 0);
#pragma unroll
      for (int t = 0; t < 16; ++t) xv[t] = __builtin_amdgcn_raw_buffer_load_b128(rx, (ln & 7) * 1024 + lg * 16 + t * 64, 0, 0);
      const u32x4 sv = {SENT, SENT, SENT, SENT};
      __builtin_amdgcn_raw_buffer_store_b128(sv, rws, WS_P1 + gq * 65536 + sl * 2048 + lane * 16, 0, AUX_SC1);
      __builtin_amdgcn_raw_buffer_store_b128(sv, rws, WS_P1 + gq * 65536 + sl * 2048 + 1024 + lane * 16, 0, AUX_SC1);
      if (lane < 2) __builtin_amdgcn_raw_buffer_store_b128(sv, rws, WS_ST + gq * 1024 + sl * 32 + lane * 16, 0, AUX_SC1);
      const int o2 = WS_P2 + (lane >> 4) * 65536 + (gq * 4 + ((lane >> 2) & 3)) * 2048 + sl * 64 + (lane & 3) * 16;
      __builtin_amdgcn_raw_buffer_store_b128(sv, rws, o2, 0, AUX_SC1);
      __builtin_amdgcn_raw_buffer_store_b128(sv, rws, o2 + 4 * 65536, 0, AUX_SC1);
    }
    __builtin_amdgcn_sched_barrier(0);
#pragma unroll
    for (int t = 0; t < 16; ++t) *(LAS u32x4*)(L + L_IMG + t * 1024 + img_row(lane >> 1) + (lane & 1) * 16) = wv[t];
    STAMP(1);
    f32x4 d0 = {0.f, 0.f, 0.f, 0.f}, d1 = {0.f, 0.f, 0.f, 0.f};
    {
      LAS unsigned char* ip = L + L_IMG + (lg >> 1) * 512 + (lg & 1) * 128 + ln * 8;
#pragma unroll
      for (int ks = 0; ks < 16; ks += 2) {
        const u32x2 a0 = tr_read(ip + ks * 1024), a1 = tr_read(ip + ks * 1024 + 256), a2 = tr_read(ip + ks * 1024 + 1024), a3 = tr_read(ip + ks * 1024 + 1280);
        d0 = mfma(u32x4{a0.x, a0.y, a1.x, a1.y}, xv[ks], d0);
        d1 = mfma(u32x4{a2.x, a2.y, a3.x, a3.y}, xv[ks + 1], d1);
      }
    }
    d0 += d1;
    const u64 tag = epoch + 1ull;
    const unsigned tlo = (unsigned)tag << 16, thi = (unsigned)(tag >> 16);
    asm volatile("s_waitcnt vmcnt(0)" ::: "memory");
    {
      const unsigned q01 = pack_bf16(d0.x, d0.y), q23 = pack_bf16(d0.z, d0.w);
      const int off = WS_QG + gq * 32768 + ln * 4096 + sl * 128 + lg * 32;
      if (ln < 8) {
        __builtin_amdgcn_raw_buffer_store_b128(u32x4{tlo | (q01 & 0xFFFFu), thi, tlo | (q01 >> 16), thi}, rws, off, 0, AUX_SC1);
        __builtin_amdgcn_raw_buffer_store_b128(u32x4{tlo | (q23 & 0xFFFFu), thi, tlo | (q23 >> 16), thi}, rws, off + 16, 0, AUX_SC1);
      }
    }
    STAMP(2);
    {
      const int off = WS_QG + g * 32768 + (i >> 2) * 4096 + lane * 16;
      u32x4 qg[4];
      for (unsigned spins = 0; spins < SPIN_CAP; ++spins) {
        asm volatile("" ::: "memory");
        bool bad = false;
#pragma unroll
        for (int h = 0; h < 4; ++h) qg[h] = __builtin_amdgcn_raw_buffer_load_b128(rws, off + h * 1024, 0, AUX_SC1);
#pragma unroll
        for (int h = 0; h < 4; ++h) bad |= (((qg[h].x ^ tlo) | (qg[h].z ^ tlo)) >> 16) != 0u || qg[h].y != thi || qg[h].w != thi;
        if (!__any(bad)) break;
      }
#pragma unroll
      for (int h = 0; h < 4; ++h) *(LAS unsigned*)(L + L_Q + h * 256 + lane * 4) = (qg[h].x & 0xFFFFu) | (qg[h].z << 16);
    }
    STAMP(3);
    const int rb = i >> 3, cb = i & 7;
    {
      const __amdgpu_buffer_rsrc_t ro = mk_rsrc(wo + g * 524288 + cb * 128, 524288 - 1024 + 128);
      const int kk = lane >> 3;
      const int voff = (kk & 3) * 131072 + (kk >> 2) * 8192 + rb * 2048 + (lane & 7) * 16;
#pragma unroll
      for (int t = 0; t < 16; ++t) wv[t] = __builtin_amdgcn_raw_buffer_load_b128(ro, voff, (t & 7) * 16384 + (t >> 3) * 1024, AUX_NT);
    }
    __builtin_amdgcn_sched_barrier(0);
    wg_barrier();
#pragma unroll
    for (int t = 0; t < 16; ++t) *(LAS u32x4*)(L + L_IMG + ((lane & 7) >> 1) * 4096 + img_row(8 * t + (lane >> 3)) + (lane & 1) * 16) = wv[t];
    wg_barrier();
    STAMP(4);
    {
      const f32x4 m0 = *(LAS f32x4*)(L + L_ML + (lane & 3) * 32), m1 = *(LAS f32x4*)(L + L_ML + (lane & 3) * 32 + 16);
      const f32x4 l0 = *(LAS f32x4*)(L + L_ML + 128 + (lane & 3) * 32), l1 = *(LAS f32x4*)(L + L_ML + 128 + (lane & 3) * 32 + 16);
      const float M = fmaxf(fmaxf(fmaxf(m0.x, m0.y), fmaxf(m0.z, m0.w)), fmaxf(fmaxf(m1.x, m1.y), fmaxf(m1.z, m1.w)));
      float Ls = 0.f;
#pragma unroll
      for (int k = 0; k < 4; ++k) Ls += l0[k] * __builtin_amdgcn_exp2f(m0[k] - M) + l1[k] * __builtin_amdgcn_exp2f(m1[k] - M);
      const unsigned val = min(__float_as_uint((lane & 4) ? Ls : M), SENT - 1u);
      if (lane < 8) __builtin_amdgcn_raw_buffer_store_b32(val, rws, WS_ST + g * 1024 + i * 32 + lane * 4, 0, AUX_SC1);
    }
    {
      u32x4 st;
      for (unsigned spins = 0; spins < SPIN_CAP; ++spins) {
        asm volatile("" ::: "memory");
        st = __builtin_amdgcn_raw_buffer_load_b128(rws, WS_ST + g * 1024 + lane * 16, 0, AUX_SC1);
        if (!__any(has_sent(st))) break;
        __builtin_amdgcn_s_sleep(8);
      }
      f32x4 f;
#pragma unroll
      for (int h = 0; h < 4; ++h) {
        const float v = __uint_as_float(st[h]), o = dppf<XOR1>(v);
        const float Ms = (lane & 1) ? o : v, Lv = (lane & 1) ? v : o;
        float mx = fmaxf(Ms, dppf<XOR2>(Ms)); mx = fmaxf(mx, dppf<HMIR>(mx));
        const float e = __builtin_amdgcn_exp2f(Ms - mx);
        float den = e * Lv; den += dppf<XOR2>(den); den += dppf<HMIR>(den);
        f[h] = e / den;
      }
      if (!(lane & 1)) *(LAS f32x4*)(L + L_F + (lane >> 1) * 16) = f;
    }
    STAMP(5);
    wg_barrier();
    wg_barrier();
    STAMP(6);
    {
      const int off = WS_P2 + (i >> 2) * 65536 + (4 * g + (i & 3)) * 2048 + lane * 16;
      u32x4 p0, p1;
      for (unsigned spins = 0; spins < SPIN_CAP; ++spins) {
        asm volatile("" ::: "memory");
        p0 = __builtin_amdgcn_raw_buffer_load_b128(rws, off, 0, AUX_SC1);
        p1 = __builtin_amdgcn_raw_buffer_load_b128(rws, off + 1024, 0, AUX_SC1);
        if (!__any(has_sent(p0) || has_sent(p1))) break;
      }
      STAMP(7);
      f32x4 o = __builtin_bit_cast(f32x4, p0) + __builtin_bit_cast(f32x4, p1);
#pragma unroll
      for (int k = 0; k < 4; ++k) { float v = o[k]; v += dppf<ROR4>(v); v += dppf<ROR8>(v); o[k] = x32sum(x16sum(v)); }
      if (lane < 4) *(GAS u32x2*)(out + (i >> 2) * 1024 + (4 * g + (i & 3)) * 32 + lane * 8) = u32x2{pack_bf16(o.x, o.y), pack_bf16(o.z, o.w)};
      if (blockIdx.x == 0 && lane == 0) __hip_atomic_store((GAS u64*)ws, tag, __ATOMIC_RELAXED, __HIP_MEMORY_SCOPE_AGENT);
    }
    STAMP(8);
  }
}

extern "C" void kernel_launch(void* const* d_in, const int* in_sizes, int n_in, void* d_out, int out_size, void* d_ws, size_t ws_size,
                              hipStream_t stream) {
  gqa_decode_kernel<<<256, NTHR, 0, stream>>>((const unsigned char*)d_in[0], (const unsigned char*)d_in[1], (const unsigned char*)d_in[2],
                                              (const unsigned char*)d_in[3], (const unsigned char*)d_in[4], (unsigned char*)d_out,
                                              (unsigned char*)d_ws);
}
```

```cpp
#include <hip/hip_runtime.h>
#include <hip/hip_bf16.h>

#define GAS __attribute__((address_space(1)))
#define LAS __attribute__((address_space(3)))
typedef unsigned u32x4 __attribute__((ext_vector_type(4)));
typedef unsigned u32x2 __attribute__((ext_vector_type(2)));
typedef float f32x4 __attribute__((ext_vector_type(4)));
typedef short s16x4 __attribute__((ext_vector_type(4)));
typedef __bf16 bf16x8 __attribute__((ext_vector_type(8)));
typedef __bf16 bf16x2 __attribute__((ext_vector_type(2)));
typedef float f32x2 __attribute__((ext_vector_type(2)));
typedef unsigned long long u64;

constexpr int NTHR = 576;
#define DK 4
constexpr int AUX_NT = 2, AUX_SC1 = 16;
constexpr unsigned SENT = 0xFFFFFFFFu;
constexpr unsigned SPIN_CAP = 1u << 16;
constexpr int WS_QG = 4096;
constexpr int WS_P1 = WS_QG + 262144;
constexpr int WS_ST = WS_P1 + 524288;
constexpr int WS_P2 = WS_ST + 8192;
constexpr int WS_END = WS_P2 + 524288;
constexpr int L_IMG = 0;
constexpr int L_RED = 16384;
constexpr int L_Q = 32768;
constexpr int L_ML = L_Q + 1024;
constexpr int L_F = L_ML + 256;
constexpr int L_OI = L_F + 512;
constexpr int L_END = L_OI + 8 * 272;

constexpr float C2 = 0.08838834764831845f * 1.4426950408889634f;

#define STAMP(k) do { } while (0)
#define STAMP_DECL do { } while (0)
#define STAMP_OUT do { } while (0)

__device__ __forceinline__ f32x4 mfma(u32x4 a, u32x4 b, f32x4 c) {
  return __builtin_amdgcn_mfma_f32_16x16x32_bf16(__builtin_bit_cast(bf16x8, a), __builtin_bit_cast(bf16x8, b), c, 0, 0, 0);
}
template <int CTRL> __device__ __forceinline__ float dppf(float x) {
  return __builtin_bit_cast(float, __builtin_amdgcn_mov_dpp(__builtin_bit_cast(int, x), CTRL, 0xf, 0xf, true));
}
template <int CTRL> __device__ __forceinline__ unsigned dppu(unsigned x) {
  return (unsigned)__builtin_amdgcn_mov_dpp((int)x, CTRL, 0xf, 0xf, true);
}
constexpr int ROR4 = 0x124, ROR8 = 0x128, ROR12 = 0x12C, SHL4 = 0x104, SHL8 = 0x108, SHL12 = 0x10C, QID = 0xE4, XOR1 = 0xB1, XOR2 = 0x4E, HMIR = 0x141;
template <int BANKS> __device__ __forceinline__ float bank_set(float old, float y) {
  return __builtin_bit_cast(float, __builtin_amdgcn_update_dpp(__builtin_bit_cast(int, old), __builtin_bit_cast(int, y), QID, 0xf, BANKS, false));
}
__device__ __forceinline__ float x16max(float x) {
  auto s = __builtin_amdgcn_permlane16_swap(__float_as_uint(x), __float_as_uint(x), false, false);
  return fmaxf(__uint_as_float(s[0]), __uint_as_float(s[1]));
}
__device__ __forceinline__ float x32max(float x) {
  auto s = __builtin_amdgcn_permlane32_swap(__float_as_uint(x), __float_as_uint(x), false, false);
  return fmaxf(__uint_as_float(s[0]), __uint_as_float(s[1]));
}
__device__ __forceinline__ float x16sum(float x) {
  auto s = __builtin_amdgcn_permlane16_swap(__float_as_uint(x), __float_as_uint(x), false, false);
  return __uint_as_float(s[0]) + __uint_as_float(s[1]);
}
__device__ __forceinline__ float x32sum(float x) {
  auto s = __builtin_amdgcn_permlane32_swap(__float_as_uint(x), __float_as_uint(x), false, false);
  return __uint_as_float(s[0]) + __uint_as_float(s[1]);
}
__device__ __forceinline__ unsigned pack_bf16(float lo, float hi) {
  f32x2 v = {lo, hi};
  return __builtin_bit_cast(unsigned, __builtin_convertvector(v, bf16x2));
}
__device__ __forceinline__ u32x4 no_sent(f32x4 v) {
  u32x4 u = __builtin_bit_cast(u32x4, v);
  u.x = min(u.x, SENT - 1u); u.y = min(u.y, SENT - 1u); u.z = min(u.z, SENT - 1u); u.w = min(u.w, SENT - 1u);
  return u;
}
__device__ __forceinline__ bool has_sent(u32x4 u) { return max(max(u.x, u.y), max(u.z, u.w)) == SENT; }
__device__ __forceinline__ void wg_barrier() {
  asm volatile("s_waitcnt lgkmcnt(0)\n\ts_barrier" ::: "memory");
}
__device__ __forceinline__ __amdgpu_buffer_rsrc_t mk_rsrc(const void* p, int bytes) {
  return __builtin_amdgcn_make_buffer_rsrc((void*)p, 0, bytes, 0x00020000);
}
__device__ __forceinline__ u32x2 tr_read(LAS unsigned char* p) {
  return __builtin_bit_cast(u32x2, __builtin_amdgcn_ds_read_tr16_b64_v4i16((LAS s16x4*)p));
}
__device__ __forceinline__ int img_row(int k) { return (k & ~12) * 32 + ((k >> 2) & 1) * 256 + ((k >> 3) & 1) * 128; }

__global__ void __launch_bounds__(NTHR, 3)
gqa_decode_kernel(const unsigned char* __restrict__ x, const unsigned char* __restrict__ kc, const unsigned char* __restrict__ vc,
                  const unsigned char* __restrict__ wq, const unsigned char* __restrict__ wo, unsigned char* out,
                  unsigned char* ws) {
  __shared__ __attribute__((aligned(16))) unsigned char smem[L_END];
  LAS unsigned char* L = (LAS unsigned char*)smem;
  const int tid = threadIdx.x, lane = tid & 63, w = __builtin_amdgcn_readfirstlane(tid >> 6);
  const int g = blockIdx.x & 7, i = blockIdx.x >> 3;
  const int lg = lane >> 4, ln = lane & 15;
  const __amdgpu_buffer_rsrc_t rws = mk_rsrc(ws, WS_END);
  STAMP_DECL;
  STAMP(0);

  if (w < 8) {
    const int b = i >> 2, s = i & 3;
    const size_t base = ((size_t)(b * 8 + g) * 4096 + s * 1024 + w * 128) * 256;
    const __amdgpu_buffer_rsrc_t rk = mk_rsrc(kc + base, 32768), rv = mk_rsrc(vc + base, 32768);
    const int koff = ((lane >> 2) & 3) * 256 + lg * 64 + (lane & 3) * 16;
    const int voff = lane * 16;
    u32x4 kr[32], vr[32];
#define LDK(u) kr[u] = __builtin_amdgcn_raw_buffer_load_b128(rk, koff + ((u) & 3) * 1024, ((u) >> 2) * 4096, AUX_NT)
#define LDV(u) vr[u] = __builtin_amdgcn_raw_buffer_load_b128(rv, voff + ((u) & 3) * 1024, ((u) >> 2) * 4096, AUX_NT)
    wg_barrier();
#pragma unroll
    for (int u = 0; u < 28; ++u) {
      if (u >= DK) __builtin_amdgcn_s_waitcnt(0x0F70 | (DK - 1));
      LDK(u);
      __builtin_amdgcn_sched_barrier(0);
    }
    STAMP(1);
    wg_barrier();
    STAMP(2);
    const u32x4 qf = *(LAS u32x4*)(L + L_Q + (lane & 3) * 256 + (4 * lg + ((lane >> 2) & 3)) * 16);
    float sc[8];
#pragma unroll
    for (int u0 = 0; u0 < 32; u0 += 4) {
      const int t = u0 >> 3;
      f32x4 d[4];
      float y[4];
#pragma unroll
      for (int k = 0; k < 4; ++k) d[k] = mfma(kr[u0 + k], qf, f32x4{0.f, 0.f, 0.f, 0.f});
      __builtin_amdgcn_sched_barrier(0);
#pragma unroll
      for (int k = 0; k < 4; ++k) y[k] = d[k][t] + dppf<ROR12>(d[k][(t + 1) & 3]);
      __builtin_amdgcn_sched_barrier(0);
#pragma unroll
      for (int k = 0; k < 4; ++k) y[k] += dppf<ROR8>(d[k][(t + 2) & 3]);
      __builtin_amdgcn_sched_barrier(0);
#pragma unroll
      for (int k = 0; k < 4; ++k) y[k] += dppf<ROR4>(d[k][(t + 3) & 3]);
      __builtin_amdgcn_sched_barrier(0);
#pragma unroll
      for (int k = 0; k < 4; ++k) {
        const int T = (u0 + k) & 7;
        sc[T] = t == 0 ? y[k] : t == 1 ? bank_set<2>(sc[T], y[k]) : t == 2 ? bank_set<4>(sc[T], y[k]) : bank_set<8>(sc[T], y[k]);
      }
      __builtin_amdgcn_sched_barrier(0);
#pragma unroll
      for (int k = 0; k < 4; ++k) {
        if (u0 == 0) LDK(28 + k);
        else if (u0 == 4) LDV(k);
        else if (u0 == 8) LDV(4 + k);
        else if (u0 == 24) LDV(8 + k);
        else if (u0 == 28) LDV(12 + k);
      }
      __builtin_amdgcn_sched_barrier(0);
    }
    float m = fmaxf(fmaxf(fmaxf(sc[0], sc[1]), fmaxf(sc[2], sc[3])), fmaxf(fmaxf(sc[4], sc[5]), fmaxf(sc[6], sc[7])));
    m = fmaxf(m, dppf<ROR4>(m)); m = fmaxf(m, dppf<ROR8>(m)); m = x32max(x16max(m));
    m *= C2;
    float ls = 0.f;
#pragma unroll
    for (int T = 0; T < 8; ++T) { sc[T] = __builtin_amdgcn_exp2f(fmaf(sc[T], C2, -m)); ls += sc[T]; }
    ls += dppf<ROR4>(ls); ls += dppf<ROR8>(ls); ls = x32sum(x16sum(ls));
    if (lane < 4) { *(LAS float*)(L + L_ML + lane * 32 + w * 4) = m; *(LAS float*)(L + L_ML + 128 + lane * 32 + w * 4) = ls; }
    STAMP(3);
    wg_barrier();
    const f32x4 m0 = *(LAS f32x4*)(L + L_ML + (lane & 3) * 32), m1 = *(LAS f32x4*)(L + L_ML + (lane & 3) * 32 + 16);
    const float M = fmaxf(fmaxf(fmaxf(m0.x, m0.y), fmaxf(m0.z, m0.w)), fmaxf(fmaxf(m1.x, m1.y), fmaxf(m1.z, m1.w)));
    const float al = __builtin_amdgcn_exp2f(m - M);
    u32x4 pa;
#pragma unroll
    for (int k = 0; k < 4; ++k) pa[k] = pack_bf16(sc[2 * k] * al, sc[2 * k + 1] * al);
    f32x4 acc[8];
#pragma unroll
    for (int j = 0; j < 8; ++j) acc[j] = f32x4{0.f, 0.f, 0.f, 0.f};
#pragma unroll
    for (int t = 0; t < 4; ++t) {
      u32x4 a;
#pragma unroll
      for (int k = 0; k < 4; ++k) a[k] = t == 0 ? pa[k] : t == 1 ? dppu<SHL4>(pa[k]) : t == 2 ? dppu<SHL8>(pa[k]) : dppu<SHL12>(pa[k]);
#pragma unroll
      for (int j = 0; j < 8; ++j) {
        u32x4 bb;
#pragma unroll
        for (int k = 0; k < 4; ++k) bb[k] = __builtin_amdgcn_perm(vr[8 * t + 2 * k + 1][j >> 1], vr[8 * t + 2 * k][j >> 1], (j & 1) ? 0x07060302u : 0x05040100u);
        acc[j] = mfma(a, bb, acc[j]);
      }
      __builtin_amdgcn_sched_barrier(0);
      if (t < 2) {
#pragma unroll
        for (int u = 16; u < 24; ++u) LDV(u + 8 * t);
        __builtin_amdgcn_sched_barrier(0);
      }
    }
    if (lane < 16) {
#pragma unroll
      for (int j = 0; j < 8; ++j) *(LAS f32x4*)(L + L_RED + w * 2048 + j * 256 + lane * 16) = acc[j];
    }
    STAMP(4);
    wg_barrier();
    STAMP(5);
    if (w < 2) {
      f32x4 v = *(LAS f32x4*)(L + L_RED + tid * 16);
#pragma unroll
      for (int ww = 1; ww < 8; ++ww) v += *(LAS f32x4*)(L + L_RED + ww * 2048 + tid * 16);
      __builtin_amdgcn_raw_buffer_store_b128(no_sent(v), rws, WS_P1 + g * 65536 + (tid >> 5) * 16384 + i * 512 + (tid & 31) * 16, 0, AUX_SC1);
    }
    STAMP(6);
    const int rb = i >> 3, cb = i & 7;
    u32x4 wa[4];
    {
      LAS unsigned char* ip = L + L_IMG + (w & 3) * 4096 + (lg >> 1) * 512 + (lg & 1) * 128 + ln * 8;
#pragma unroll
      for (int ks = 0; ks < 4; ++ks) { const u32x2 lo = tr_read(ip + ks * 1024), hi = tr_read(ip + ks * 1024 + 256); wa[ks] = u32x4{lo.x, lo.y, hi.x, hi.y}; }
    }
    const f32x4 f0 = *(LAS f32x4*)(L + L_F + w * 64 + (lane >> 5) * 16), f1 = *(LAS f32x4*)(L + L_F + w * 64 + 32 + (lane >> 5) * 16);
    {
      const int off = WS_P1 + g * 65536 + rb * 16384 + w * 2048 + lane * 16;
      u32x4 p0, p1;
      for (unsigned spins = 0; spins < SPIN_CAP; ++spins) {
        asm volatile("" ::: "memory");
        p0 = __builtin_amdgcn_raw_buffer_load_b128(rws, off, 0, AUX_SC1);
        p1 = __builtin_amdgcn_raw_buffer_load_b128(rws, off + 1024, 0, AUX_SC1);
        if (!__any(has_sent(p0) || has_sent(p1))) break;
      }
      f32x4 o = __builtin_bit_cast(f32x4, p0) * f0 + __builtin_bit_cast(f32x4, p1) * f1;
      o.x = x32sum(o.x); o.y = x32sum(o.y); o.z = x32sum(o.z); o.w = x32sum(o.w);
      if (lane < 32) *(LAS u32x2*)(L + L_OI + w * 272 + lane * 8) = u32x2{pack_bf16(o.x, o.y), pack_bf16(o.z, o.w)};
    }
    STAMP(7);
    wg_barrier();
    if (w < 4) {
      f32x4 d = {0.f, 0.f, 0.f, 0.f};
#pragma unroll
      for (int ks = 0; ks < 4; ++ks) d = mfma(wa[ks], *(LAS u32x4*)(L + L_OI + (ln & 7) * 272 + ks * 64 + lg * 16), d);
      if (ln < 8) __builtin_amdgcn_raw_buffer_store_b128(no_sent(d), rws, WS_P2 + ln * 65536 + (cb * 4 + w) * 2048 + (g * 4 + rb) * 64 + lg * 16, 0, AUX_SC1);
    }
    STAMP(8);
  } else {
    __builtin_amdgcn_s_setprio(3);
    const int gq = i & 7, sl = g * 4 + (i >> 3);
    const u64 epoch = __hip_atomic_load((GAS u64*)ws, __ATOMIC_RELAXED, __HIP_MEMORY_SCOPE_AGENT);
    u32x4 wv[16], xv[16];
    {
      const __amdgpu_buffer_rsrc_t rq = mk_rsrc(wq + gq * 1024 + sl * 32, 512 * 8192 - 8192 + 32), rx = mk_rsrc(x, 8192);
#pragma unroll
      for (int t = 0; t < 16; ++t) wv[t] = __builtin_amdgcn_raw_buffer_load_b128(rq, (lane >> 1) * 8192 + (lane & 1) * 16, t * 262144, 0);
#pragma unroll
      for (int t = 0; t < 16; ++t) xv[t] = __builtin_amdgcn_raw_buffer_load_b128(rx, (ln & 7) * 1024 + lg * 16 + t * 64, 0, 0);
      const u32x4 sv = {SENT, SENT, SENT, SENT};
      __builtin_amdgcn_raw_buffer_store_b128(sv, rws, WS_P1 + gq * 65536 + sl * 2048 + lane * 16, 0, AUX_SC1);
      __builtin_amdgcn_raw_buffer_store_b128(sv, rws, WS_P1 + gq * 65536 + sl * 2048 + 1024 + lane * 16, 0, AUX_SC1);
      if (lane < 2) __builtin_amdgcn_raw_buffer_store_b128(sv, rws, WS_ST + gq * 1024 + sl * 32 + lane * 16, 0, AUX_SC1);
      const int o2 = WS_P2 + (lane >> 4) * 65536 + (gq * 4 + ((lane >> 2) & 3)) * 2048 + sl * 64 + (lane & 3) * 16;
      __builtin_amdgcn_raw_buffer_store_b128(sv, rws, o2, 0, AUX_SC1);
      __builtin_amdgcn_raw_buffer_store_b128(sv, rws, o2 + 4 * 65536, 0, AUX_SC1);
    }
    __builtin_amdgcn_sched_barrier(0);
    wg_barrier();
#pragma unroll
    for (int t = 0; t < 16; ++t) *(LAS u32x4*)(L + L_IMG + t * 1024 + img_row(lane >> 1) + (lane & 1) * 16) = wv[t];
    STAMP(1);
    f32x4 d0 = {0.f, 0.f, 0.f, 0.f}, d1 = {0.f, 0.f, 0.f, 0.f};
    {
      LAS unsigned char* ip = L + L_IMG + (lg >> 1) * 512 + (lg & 1) * 128 + ln * 8;
#pragma unroll
      for (int ks = 0; ks < 16; ks += 2) {
        const u32x2 a0 = tr_read(ip + ks * 1024), a1 = tr_read(ip + ks * 1024 + 256), a2 = tr_read(ip + ks * 1024 + 1024), a3 = tr_read(ip + ks * 1024 + 1280);
        d0 = mfma(u32x4{a0.x, a0.y, a1.x, a1.y}, xv[ks], d0);
        d1 = mfma(u32x4{a2.x, a2.y, a3.x, a3.y}, xv[ks + 1], d1);
      }
    }
    d0 += d1;
    const u64 tag = epoch + 1ull;
    const unsigned tlo = (unsigned)tag << 16, thi = (unsigned)(tag >> 16);
    asm volatile("s_waitcnt vmcnt(0)" ::: "memory");
    {
      const unsigned q01 = pack_bf16(d0.x, d0.y), q23 = pack_bf16(d0.z, d0.w);
      const int off = WS_QG + gq * 32768 + ln * 4096 + sl * 128 + lg * 32;
      if (ln < 8) {
        __builtin_amdgcn_raw_buffer_store_b128(u32x4{tlo | (q01 & 0xFFFFu), thi, tlo | (q01 >> 16), thi}, rws, off, 0, AUX_SC1);
        __builtin_amdgcn_raw_buffer_store_b128(u32x4{tlo | (q23 & 0xFFFFu), thi, tlo | (q23 >> 16), thi}, rws, off + 16, 0, AUX_SC1);
      }
    }
    STAMP(2);
    {
      const int off = WS_QG + g * 32768 + (i >> 2) * 4096 + lane * 16;
      u32x4 qg[4];
      for (unsigned spins = 0; spins < SPIN_CAP; ++spins) {
        asm volatile("" ::: "memory");
        bool bad = false;
#pragma unroll
        for (int h = 0; h < 4; ++h) qg[h] = __builtin_amdgcn_raw_buffer_load_b128(rws, off + h * 1024, 0, AUX_SC1);
#pragma unroll
        for (int h = 0; h < 4; ++h) bad |= (((qg[h].x ^ tlo) | (qg[h].z ^ tlo)) >> 16) != 0u || qg[h].y != thi || qg[h].w != thi;
        if (!__any(bad)) break;
      }
#pragma unroll
      for (int h = 0; h < 4; ++h) *(LAS unsigned*)(L + L_Q + h * 256 + lane * 4) = (qg[h].x & 0xFFFFu) | (qg[h].z << 16);
    }
    STAMP(3);
    const int rb = i >> 3, cb = i & 7;
    {
      const __amdgpu_buffer_rsrc_t ro = mk_rsrc(wo + g * 524288 + cb * 128, 524288 - 1024 + 128);
      const int kk = lane >> 3;
      const int voff = (kk & 3) * 131072 + (kk >> 2) * 8192 + rb * 2048 + (lane & 7) * 16;
#pragma unroll
      for (int t = 0; t < 16; ++t) wv[t] = __builtin_amdgcn_raw_buffer_load_b128(ro, voff, (t & 7) * 16384 + (t >> 3) * 1024, AUX_NT);
    }
    __builtin_amdgcn_sched_barrier(0);
    wg_barrier();
#pragma unroll
    for (int t = 0; t < 16; ++t) *(LAS u32x4*)(L + L_IMG + ((lane & 7) >> 1) * 4096 + img_row(8 * t + (lane >> 3)) + (lane & 1) * 16) = wv[t];
    wg_barrier();
    STAMP(4);
    {
      const f32x4 m0 = *(LAS f32x4*)(L + L_ML + (lane & 3) * 32), m1 = *(LAS f32x4*)(L + L_ML + (lane & 3) * 32 + 16);
      const f32x4 l0 = *(LAS f32x4*)(L + L_ML + 128 + (lane & 3) * 32), l1 = *(LAS f32x4*)(L + L_ML + 128 + (lane & 3) * 32 + 16);
      const float M = fmaxf(fmaxf(fmaxf(m0.x, m0.y), fmaxf(m0.z, m0.w)), fmaxf(fmaxf(m1.x, m1.y), fmaxf(m1.z, m1.w)));
      float Ls = 0.f;
#pragma unroll
      for (int k = 0; k < 4; ++k) Ls += l0[k] * __builtin_amdgcn_exp2f(m0[k] - M) + l1[k] * __builtin_amdgcn_exp2f(m1[k] - M);
      const unsigned val = min(__float_as_uint((lane & 4) ? Ls : M), SENT - 1u);
      if (lane < 8) __builtin_amdgcn_raw_buffer_store_b32(val, rws, WS_ST + g * 1024 + i * 32 + lane * 4, 0, AUX_SC1);
    }
    {
      u32x4 st;
      for (unsigned spins = 0; spins < SPIN_CAP; ++spins) {
        asm volatile("" ::: "memory");
        st = __builtin_amdgcn_raw_buffer_load_b128(rws, WS_ST + g * 1024 + lane * 16, 0, AUX_SC1);
        if (!__any(has_sent(st))) break;
        __builtin_amdgcn_s_sleep(8);
      }
      f32x4 f;
#pragma unroll
      for (int h = 0; h < 4; ++h) {
        const float v = __uint_as_float(st[h]), o = dppf<XOR1>(v);
        const float Ms = (lane & 1) ? o : v, Lv = (lane & 1) ? v : o;
        float mx = fmaxf(Ms, dppf<XOR2>(Ms)); mx = fmaxf(mx, dppf<HMIR>(mx));
        const float e = __builtin_amdgcn_exp2f(Ms - mx);
        float den = e * Lv; den += dppf<XOR2>(den); den += dppf<HMIR>(den);
        f[h] = e / den;
      }
      if (!(lane & 1)) *(LAS f32x4*)(L + L_F + (lane >> 1) * 16) = f;
    }
    STAMP(5);
    wg_barrier();
    wg_barrier();
    STAMP(6);
    {
      const int off = WS_P2 + (i >> 2) * 65536 + (4 * g + (i & 3)) * 2048 + lane * 16;
      u32x4 p0, p1;
      for (unsigned spins = 0; spins < SPIN_CAP; ++spins) {
        asm volatile("" ::: "memory");
        p0 = __builtin_amdgcn_raw_buffer_load_b128(rws, off, 0, AUX_SC1);
        p1 = __builtin_amdgcn_raw_buffer_load_b128(rws, off + 1024, 0, AUX_SC1);
        if (!__any(has_sent(p0) || has_sent(p1))) break;
      }
      STAMP(7);
      f32x4 o = __builtin_bit_cast(f32x4, p0) + __builtin_bit_cast(f32x4, p1);
#pragma unroll
      for (int k = 0; k < 4; ++k) { float v = o[k]; v += dppf<ROR4>(v); v += dppf<ROR8>(v); o[k] = x32sum(x16sum(v)); }
      if (lane < 4) *(GAS u32x2*)(out + (i >> 2) * 1024 + (4 * g + (i & 3)) * 32 + lane * 8) = u32x2{pack_bf16(o.x, o.y), pack_bf16(o.z, o.w)};
      if (blockIdx.x == 0 && lane == 0) __hip_atomic_store((GAS u64*)ws, tag, __ATOMIC_RELAXED, __HIP_MEMORY_SCOPE_AGENT);
    }
    STAMP(8);
  }
  STAMP_OUT;
}

extern "C" void kernel_launch(void* const* d_in, const int* in_sizes, int n_in, void* d_out, int out_size, void* d_ws, size_t ws_size,
                              hipStream_t stream) {
  gqa_decode_kernel<<<256, NTHR, 0, stream>>>((const unsigned char*)d_in[0], (const unsigned char*)d_in[1], (const unsigned char*)d_in[2],
                                              (const unsigned char*)d_in[3], (const unsigned char*)d_in[4], (unsigned char*)d_out,
                                              (unsigned char*)d_ws);
}
```

```cpp
#define PROBE 0
#include <hip/hip_runtime.h>
#include <hip/hip_bf16.h>

#define GAS __attribute__((address_space(1)))
#define LAS __attribute__((address_space(3)))
typedef unsigned u32x4 __attribute__((ext_vector_type(4)));
typedef unsigned u32x2 __attribute__((ext_vector_type(2)));
typedef float f32x4 __attribute__((ext_vector_type(4)));
typedef short s16x4 __attribute__((ext_vector_type(4)));
typedef __bf16 bf16x8 __attribute__((ext_vector_type(8)));
typedef __bf16 bf16x2 __attribute__((ext_vector_type(2)));
typedef float f32x2 __attribute__((ext_vector_type(2)));
typedef unsigned long long u64;

constexpr int NTHR = 576;
#define DK 4
constexpr int AUX_NT = 2, AUX_SC1 = 16;
constexpr unsigned SENT = 0xFFFFFFFFu;
constexpr unsigned SPIN_CAP = 1u << 16;
constexpr int WS_QG = 4096;
constexpr int WS_P1 = WS_QG + 262144;
constexpr int WS_ST = WS_P1 + 524288;
constexpr int WS_P2 = WS_ST + 8192;
constexpr int WS_END = WS_P2 + 524288;
constexpr int L_IMG = 0;
constexpr int L_RED = 16384;
constexpr int L_Q = 32768;
constexpr int L_ML = L_Q + 1024;
constexpr int L_F = L_ML + 256;
constexpr int L_OI = L_F + 512;
constexpr int L_END = L_OI + 8 * 272;

constexpr float C2 = 0.08838834764831845f * 1.4426950408889634f;

#define STAMP(k) do { __builtin_amdgcn_sched_barrier(0); asm volatile("s_memrealtime %0\n\ts_waitcnt lgkmcnt(0)" : "=s"(stamps_[k]) :: "memory"); __builtin_amdgcn_sched_barrier(0); } while (0)
#define STAMP_DECL u64 stamps_[12] = {0, 0, 0, 0, 0, 0, 0, 0, 0, 0, 0, 0}
#define STAMP_OUT do { if (w == 8) { const u64 until_ = stamps_[8] + (stamps_[PROBE] - stamps_[0]) * 2; while (__builtin_amdgcn_s_memrealtime() < until_) __builtin_amdgcn_s_sleep(1); } } while (0)

__device__ __forceinline__ f32x4 mfma(u32x4 a, u32x4 b, f32x4 c) {
  return __builtin_amdgcn_mfma_f32_16x16x32_bf16(__builtin_bit_cast(bf16x8, a), __builtin_bit_cast(bf16x8, b), c, 0, 0, 0);
}
template <int CTRL> __device__ __forceinline__ float dppf(float x) {
  return __builtin_bit_cast(float, __builtin_amdgcn_mov_dpp(__builtin_bit_cast(int, x), CTRL, 0xf, 0xf, true));
}
template <int CTRL> __device__ __forceinline__ unsigned dppu(unsigned x) {
  return (unsigned)__builtin_amdgcn_mov_dpp((int)x, CTRL, 0xf, 0xf, true);
}
constexpr int ROR4 = 0x124, ROR8 = 0x128, ROR12 = 0x12C, SHL4 = 0x104, SHL8 = 0x108, SHL12 = 0x10C, QID = 0xE4, XOR1 = 0xB1, XOR2 = 0x4E, HMIR = 0x141;
template <int BANKS> __device__ __forceinline__ float bank_set(float old, float y) {
  return __builtin_bit_cast(float, __builtin_amdgcn_update_dpp(__builtin_bit_cast(int, old), __builtin_bit_cast(int, y), QID, 0xf, BANKS, false));
}
__device__ __forceinline__ float x16max(float x) {
  auto s = __builtin_amdgcn_permlane16_swap(__float_as_uint(x), __float_as_uint(x), false, false);
  return fmaxf(__uint_as_float(s[0]), __uint_as_float(s[1]));
}
__device__ __forceinline__ float x32max(float x) {
  auto s = __builtin_amdgcn_permlane32_swap(__float_as_uint(x), __float_as_uint(x), false, false);
  return fmaxf(__uint_as_float(s[0]), __uint_as_float(s[1]));
}
__device__ __forceinline__ float x16sum(float x) {
  auto s = __builtin_amdgcn_permlane16_swap(__float_as_uint(x), __float_as_uint(x), false, false);
  return __uint_as_float(s[0]) + __uint_as_float(s[1]);
}
__device__ __forceinline__ float x32sum(float x) {
  auto s = __builtin_amdgcn_permlane32_swap(__float_as_uint(x), __float_as_uint(x), false, false);
  return __uint_as_float(s[0]) + __uint_as_float(s[1]);
}
__device__ __forceinline__ unsigned pack_bf16(float lo, float hi) {
  f32x2 v = {lo, hi};
  return __builtin_bit_cast(unsigned, __builtin_convertvector(v, bf16x2));
}
__device__ __forceinline__ u32x4 no_sent(f32x4 v) {
  u32x4 u = __builtin_bit_cast(u32x4, v);
  u.x = min(u.x, SENT - 1u); u.y = min(u.y, SENT - 1u); u.z = min(u.z, SENT - 1u); u.w = min(u.w, SENT - 1u);
  return u;
}
__device__ __forceinline__ bool has_sent(u32x4 u) { return max(max(u.x, u.y), max(u.z, u.w)) == SENT; }
__device__ __forceinline__ void wg_barrier() {
  asm volatile("s_waitcnt lgkmcnt(0)\n\ts_barrier" ::: "memory");
}
__device__ __forceinline__ __amdgpu_buffer_rsrc_t mk_rsrc(const void* p, int bytes) {
  return __builtin_amdgcn_make_buffer_rsrc((void*)p, 0, bytes, 0x00020000);
}
__device__ __forceinline__ u32x2 tr_read(LAS unsigned char* p) {
  return __builtin_bit_cast(u32x2, __builtin_amdgcn_ds_read_tr16_b64_v4i16((LAS s16x4*)p));
}
__device__ __forceinline__ int img_row(int k) { return (k & ~12) * 32 + ((k >> 2) & 1) * 256 + ((k >> 3) & 1) * 128; }

__global__ void __launch_bounds__(NTHR, 3)
gqa_decode_kernel(const unsigned char* __restrict__ x, const unsigned char* __restrict__ kc, const unsigned char* __restrict__ vc,
                  const unsigned char* __restrict__ wq, const unsigned char* __restrict__ wo, unsigned char* out,
                  unsigned char* ws) {
  __shared__ __attribute__((aligned(16))) unsigned char smem[L_END];
  LAS unsigned char* L = (LAS unsigned char*)smem;
  const int tid = threadIdx.x, lane = tid & 63, w = __builtin_amdgcn_readfirstlane(tid >> 6);
  const int g = blockIdx.x & 7, i = blockIdx.x >> 3;
  const int lg = lane >> 4, ln = lane & 15;
  const __amdgpu_buffer_rsrc_t rws = mk_rsrc(ws, WS_END);
  STAMP_DECL;
  STAMP(0);

  if (w < 8) {
    const int b = i >> 2, s = i & 3;
    const size_t base = ((size_t)(b * 8 + g) * 4096 + s * 1024 + w * 128) * 256;
    const __amdgpu_buffer_rsrc_t rk = mk_rsrc(kc + base, 32768), rv = mk_rsrc(vc + base, 32768);
    const int koff = ((lane >> 2) & 3) * 256 + lg * 64 + (lane & 3) * 16;
    const int voff = lane * 16;
    u32x4 kr[32], vr[32];
#define LDK(u) kr[u] = __builtin_amdgcn_raw_buffer_load_b128(rk, koff + ((u) & 3) * 1024, ((u) >> 2) * 4096, AUX_NT)
#define LDV(u) vr[u] = __builtin_amdgcn_raw_buffer_load_b128(rv, voff + ((u) & 3) * 1024, ((u) >> 2) * 4096, AUX_NT)
    wg_barrier();
#pragma unroll
    for (int u = 0; u < 28; ++u) {
      if (u >= DK) __builtin_amdgcn_s_waitcnt(0x0F70 | (DK - 1));
      LDK(u);
      __builtin_amdgcn_sched_barrier(0);
    }
    STAMP(1);
    wg_barrier();
    STAMP(2);
    const u32x4 qf = *(LAS u32x4*)(L + L_Q + (lane & 3) * 256 + (4 * lg + ((lane >> 2) & 3)) * 16);
    float sc[8];
#pragma unroll
    for (int u0 = 0; u0 < 32; u0 += 4) {
      const int t = u0 >> 3;
      f32x4 d[4];
      float y[4];
#pragma unroll
      for (int k = 0; k < 4; ++k) d[k] = mfma(kr[u0 + k], qf, f32x4{0.f, 0.f, 0.f, 0.f});
      __builtin_amdgcn_sched_barrier(0);
#pragma unroll
      for (int k = 0; k < 4; ++k) y[k] = d[k][t] + dppf<ROR12>(d[k][(t + 1) & 3]);
      __builtin_amdgcn_sched_barrier(0);
#pragma unroll
      for (int k = 0; k < 4; ++k) y[k] += dppf<ROR8>(d[k][(t + 2) & 3]);
      __builtin_amdgcn_sched_barrier(0);
#pragma unroll
      for (int k = 0; k < 4; ++k) y[k] += dppf<ROR4>(d[k][(t + 3) & 3]);
      __builtin_amdgcn_sched_barrier(0);
#pragma unroll
      for (int k = 0; k < 4; ++k) {
        const int T = (u0 + k) & 7;
        sc[T] = t == 0 ? y[k] : t == 1 ? bank_set<2>(sc[T], y[k]) : t == 2 ? bank_set<4>(sc[T], y[k]) : bank_set<8>(sc[T], y[k]);
      }
      __builtin_amdgcn_sched_barrier(0);
#pragma unroll
      for (int k = 0; k < 4; ++k) {
        if (u0 == 0) LDK(28 + k);
        else if (u0 == 4) LDV(k);
        else if (u0 == 8) LDV(4 + k);
        else if (u0 == 24) LDV(8 + k);
        else if (u0 == 28) LDV(12 + k);
      }
      __builtin_amdgcn_sched_barrier(0);
    }
    float m = fmaxf(fmaxf(fmaxf(sc[0], sc[1]), fmaxf(sc[2], sc[3])), fmaxf(fmaxf(sc[4], sc[5]), fmaxf(sc[6], sc[7])));
    m = fmaxf(m, dppf<ROR4>(m)); m = fmaxf(m, dppf<ROR8>(m)); m = x32max(x16max(m));
    m *= C2;
    float ls = 0.f;
#pragma unroll
    for (int T = 0; T < 8; ++T) { sc[T] = __builtin_amdgcn_exp2f(fmaf(sc[T], C2, -m)); ls += sc[T]; }
    ls += dppf<ROR4>(ls); ls += dppf<ROR8>(ls); ls = x32sum(x16sum(ls));
    if (lane < 4) { *(LAS float*)(L + L_ML + lane * 32 + w * 4) = m; *(LAS float*)(L + L_ML + 128 + lane * 32 + w * 4) = ls; }
    STAMP(3);
    wg_barrier();
    const f32x4 m0 = *(LAS f32x4*)(L + L_ML + (lane & 3) * 32), m1 = *(LAS f32x4*)(L + L_ML + (lane & 3) * 32 + 16);
    const float M = fmaxf(fmaxf(fmaxf(m0.x, m0.y), fmaxf(m0.z, m0.w)), fmaxf(fmaxf(m1.x, m1.y), fmaxf(m1.z, m1.w)));
    const float al = __builtin_amdgcn_exp2f(m - M);
    u32x4 pa;
#pragma unroll
    for (int k = 0; k < 4; ++k) pa[k] = pack_bf16(sc[2 * k] * al, sc[2 * k + 1] * al);
    f32x4 acc[8];
#pragma unroll
    for (int j = 0; j < 8; ++j) acc[j] = f32x4{0.f, 0.f, 0.f, 0.f};
#pragma unroll
    for (int t = 0; t < 4; ++t) {
      u32x4 a;
#pragma unroll
      for (int k = 0; k < 4; ++k) a[k] = t == 0 ? pa[k] : t == 1 ? dppu<SHL4>(pa[k]) : t == 2 ? dppu<SHL8>(pa[k]) : dppu<SHL12>(pa[k]);
#pragma unroll
      for (int j = 0; j < 8; ++j) {
        u32x4 bb;
#pragma unroll
        for (int k = 0; k < 4; ++k) bb[k] = __builtin_amdgcn_perm(vr[8 * t + 2 * k + 1][j >> 1], vr[8 * t + 2 * k][j >> 1], (j & 1) ? 0x07060302u : 0x05040100u);
        acc[j] = mfma(a, bb, acc[j]);
      }
      __builtin_amdgcn_sched_barrier(0);
      if (t < 2) {
#pragma unroll
        for (int u = 16; u < 24; ++u) LDV(u + 8 * t);
        __builtin_amdgcn_sched_barrier(0);
      }
    }
    if (lane < 16) {
#pragma unroll
      for (int j = 0; j < 8; ++j) *(LAS f32x4*)(L + L_RED + w * 2048 + j * 256 + lane * 16) = acc[j];
    }
    STAMP(4);
    wg_barrier();
    STAMP(5);
    if (w < 2) {
      f32x4 v = *(LAS f32x4*)(L + L_RED + tid * 16);
#pragma unroll
      for (int ww = 1; ww < 8; ++ww) v += *(LAS f32x4*)(L + L_RED + ww * 2048 + tid * 16);
      __builtin_amdgcn_raw_buffer_store_b128(no_sent(v), rws, WS_P1 + g * 65536 + (tid >> 5) * 16384 + i * 512 + (tid & 31) * 16, 0, AUX_SC1);
    }
    STAMP(6);
    const int rb = i >> 3, cb = i & 7;
    u32x4 wa[4];
    {
      LAS unsigned char* ip = L + L_IMG + (w & 3) * 4096 + (lg >> 1) * 512 + (lg & 1) * 128 + ln * 8;
#pragma unroll
      for (int ks = 0; ks < 4; ++ks) { const u32x2 lo = tr_read(ip + ks * 1024), hi = tr_read(ip + ks * 1024 + 256); wa[ks] = u32x4{lo.x, lo.y, hi.x, hi.y}; }
    }
    const f32x4 f0 = *(LAS f32x4*)(L + L_F + w * 64 + (lane >> 5) * 16), f1 = *(LAS f32x4*)(L + L_F + w * 64 + 32 + (lane >> 5) * 16);
    {
      const int off = WS_P1 + g * 65536 + rb * 16384 + w * 2048 + lane * 16;
      u32x4 p0, p1;
      for (unsigned spins = 0; spins < SPIN_CAP; ++spins) {
        asm volatile("" ::: "memory");
        p0 = __builtin_amdgcn_raw_buffer_load_b128(rws, off, 0, AUX_SC1);
        p1 = __builtin_amdgcn_raw_buffer_load_b128(rws, off + 1024, 0, AUX_SC1);
        if (!__any(has_sent(p0) || has_sent(p1))) break;
      }
      f32x4 o = __builtin_bit_cast(f32x4, p0) * f0 + __builtin_bit_cast(f32x4, p1) * f1;
      o.x = x32sum(o.x); o.y = x32sum(o.y); o.z = x32sum(o.z); o.w = x32sum(o.w);
      if (lane < 32) *(LAS u32x2*)(L + L_OI + w * 272 + lane * 8) = u32x2{pack_bf16(o.x, o.y), pack_bf16(o.z, o.w)};
    }
    STAMP(7);
    wg_barrier();
    if (w < 4) {
      f32x4 d = {0.f, 0.f, 0.f, 0.f};
#pragma unroll
      for (int ks = 0; ks < 4; ++ks) d = mfma(wa[ks], *(LAS u32x4*)(L + L_OI + (ln & 7) * 272 + ks * 64 + lg * 16), d);
      if (ln < 8) __builtin_amdgcn_raw_buffer_store_b128(no_sent(d), rws, WS_P2 + ln * 65536 + (cb * 4 + w) * 2048 + (g * 4 + rb) * 64 + lg * 16, 0, AUX_SC1);
    }
    STAMP(8);
  } else {
    __builtin_amdgcn_s_setprio(3);
    const int gq = i & 7, sl = g * 4 + (i >> 3);
    const u64 epoch = __hip_atomic_load((GAS u64*)ws, __ATOMIC_RELAXED, __HIP_MEMORY_SCOPE_AGENT);
    u32x4 wv[16], xv[16];
    {
      const __amdgpu_buffer_rsrc_t rq = mk_rsrc(wq + gq * 1024 + sl * 32, 512 * 8192 - 8192 + 32), rx = mk_rsrc(x, 8192);
#pragma unroll
      for (int t = 0; t < 16; ++t) wv[t] = __builtin_amdgcn_raw_buffer_load_b128(rq, (lane >> 1) * 8192 + (lane & 1) * 16, t * 262144, 0);
#pragma unroll
      for (int t = 0; t < 16; ++t) xv[t] = __builtin_amdgcn_raw_buffer_load_b128(rx, (ln & 7) * 1024 + lg * 16 + t * 64, 0, 0);
      const u32x4 sv = {SENT, SENT, SENT, SENT};
      __builtin_amdgcn_raw_buffer_store_b128(sv, rws, WS_P1 + gq * 65536 + sl * 2048 + lane * 16, 0, AUX_SC1);
      __builtin_amdgcn_raw_buffer_store_b128(sv, rws, WS_P1 + gq * 65536 + sl * 2048 + 1024 + lane * 16, 0, AUX_SC1);
      if (lane < 2) __builtin_amdgcn_raw_buffer_store_b128(sv, rws, WS_ST + gq * 1024 + sl * 32 + lane * 16, 0, AUX_SC1);
      const int o2 = WS_P2 + (lane >> 4) * 65536 + (gq * 4 + ((lane >> 2) & 3)) * 2048 + sl * 64 + (lane & 3) * 16;
      __builtin_amdgcn_raw_buffer_store_b128(sv, rws, o2, 0, AUX_SC1);
      __builtin_amdgcn_raw_buffer_store_b128(sv, rws, o2 + 4 * 65536, 0, AUX_SC1);
    }
    __builtin_amdgcn_sched_barrier(0);
    wg_barrier();
#pragma unroll
    for (int t = 0; t < 16; ++t) *(LAS u32x4*)(L + L_IMG + t * 1024 + img_row(lane >> 1) + (lane & 1) * 16) = wv[t];
    STAMP(1);
    f32x4 d0 = {0.f, 0.f, 0.f, 0.f}, d1 = {0.f, 0.f, 0.f, 0.f};
    {
      LAS unsigned char* ip = L + L_IMG + (lg >> 1) * 512 + (lg & 1) * 128 + ln * 8;
#pragma unroll
      for (int ks = 0; ks < 16; ks += 2) {
        const u32x2 a0 = tr_read(ip + ks * 1024), a1 = tr_read(ip + ks * 1024 + 256), a2 = tr_read(ip + ks * 1024 + 1024), a3 = tr_read(ip + ks * 1024 + 1280);
        d0 = mfma(u32x4{a0.x, a0.y, a1.x, a1.y}, xv[ks], d0);
        d1 = mfma(u32x4{a2.x, a2.y, a3.x, a3.y}, xv[ks + 1], d1);
      }
    }
    d0 += d1;
    const u64 tag = epoch + 1ull;
    const unsigned tlo = (unsigned)tag << 16, thi = (unsigned)(tag >> 16);
    asm volatile("s_waitcnt vmcnt(0)" ::: "memory");
    {
      const unsigned q01 = pack_bf16(d0.x, d0.y), q23 = pack_bf16(d0.z, d0.w);
      const int off = WS_QG + gq * 32768 + ln * 4096 + sl * 128 + lg * 32;
      if (ln < 8) {
        __builtin_amdgcn_raw_buffer_store_b128(u32x4{tlo | (q01 & 0xFFFFu), thi, tlo | (q01 >> 16), thi}, rws, off, 0, AUX_SC1);
        __builtin_amdgcn_raw_buffer_store_b128(u32x4{tlo | (q23 & 0xFFFFu), thi, tlo | (q23 >> 16), thi}, rws, off + 16, 0, AUX_SC1);
      }
    }
    STAMP(2);
    {
      const int off = WS_QG + g * 32768 + (i >> 2) * 4096 + lane * 16;
      u32x4 qg[4];
      for (unsigned spins = 0; spins < SPIN_CAP; ++spins) {
        asm volatile("" ::: "memory");
        bool bad = false;
#pragma unroll
        for (int h = 0; h < 4; ++h) qg[h] = __builtin_amdgcn_raw_buffer_load_b128(rws, off + h * 1024, 0, AUX_SC1);
#pragma unroll
        for (int h = 0; h < 4; ++h) bad |= (((qg[h].x ^ tlo) | (qg[h].z ^ tlo)) >> 16) != 0u || qg[h].y != thi || qg[h].w != thi;
        if (!__any(bad)) break;
      }
#pragma unroll
      for (int h = 0; h < 4; ++h) *(LAS unsigned*)(L + L_Q + h * 256 + lane * 4) = (qg[h].x & 0xFFFFu) | (qg[h].z << 16);
    }
    STAMP(3);
    const int rb = i >> 3, cb = i & 7;
    {
      const __amdgpu_buffer_rsrc_t ro = mk_rsrc(wo + g * 524288 + cb * 128, 524288 - 1024 + 128);
      const int kk = lane >> 3;
      const int voff = (kk & 3) * 131072 + (kk >> 2) * 8192 + rb * 2048 + (lane & 7) * 16;
#pragma unroll
      for (int t = 0; t < 16; ++t) wv[t] = __builtin_amdgcn_raw_buffer_load_b128(ro, voff, (t & 7) * 16384 + (t >> 3) * 1024, AUX_NT);
    }
    __builtin_amdgcn_sched_barrier(0);
    wg_barrier();
#pragma unroll
    for (int t = 0; t < 16; ++t) *(LAS u32x4*)(L + L_IMG + ((lane & 7) >> 1) * 4096 + img_row(8 * t + (lane >> 3)) + (lane & 1) * 16) = wv[t];
    wg_barrier();
    STAMP(4);
    {
      const f32x4 m0 = *(LAS f32x4*)(L + L_ML + (lane & 3) * 32), m1 = *(LAS f32x4*)(L + L_ML + (lane & 3) * 32 + 16);
      const f32x4 l0 = *(LAS f32x4*)(L + L_ML + 128 + (lane & 3) * 32), l1 = *(LAS f32x4*)(L + L_ML + 128 + (lane & 3) * 32 + 16);
      const float M = fmaxf(fmaxf(fmaxf(m0.x, m0.y), fmaxf(m0.z, m0.w)), fmaxf(fmaxf(m1.x, m1.y), fmaxf(m1.z, m1.w)));
      float Ls = 0.f;
#pragma unroll
      for (int k = 0; k < 4; ++k) Ls += l0[k] * __builtin_amdgcn_exp2f(m0[k] - M) + l1[k] * __builtin_amdgcn_exp2f(m1[k] - M);
      const unsigned val = min(__float_as_uint((lane & 4) ? Ls : M), SENT - 1u);
      if (lane < 8) __builtin_amdgcn_raw_buffer_store_b32(val, rws, WS_ST + g * 1024 + i * 32 + lane * 4, 0, AUX_SC1);
    }
    {
      u32x4 st;
      for (unsigned spins = 0; spins < SPIN_CAP; ++spins) {
        asm volatile("" ::: "memory");
        st = __builtin_amdgcn_raw_buffer_load_b128(rws, WS_ST + g * 1024 + lane * 16, 0, AUX_SC1);
        if (!__any(has_sent(st))) break;
        __builtin_amdgcn_s_sleep(8);
      }
      f32x4 f;
#pragma unroll
      for (int h = 0; h < 4; ++h) {
        const float v = __uint_as_float(st[h]), o = dppf<XOR1>(v);
        const float Ms = (lane & 1) ? o : v, Lv = (lane & 1) ? v : o;
        float mx = fmaxf(Ms, dppf<XOR2>(Ms)); mx = fmaxf(mx, dppf<HMIR>(mx));
        const float e = __builtin_amdgcn_exp2f(Ms - mx);
        float den = e * Lv; den += dppf<XOR2>(den); den += dppf<HMIR>(den);
        f[h] = e / den;
      }
      if (!(lane & 1)) *(LAS f32x4*)(L + L_F + (lane >> 1) * 16) = f;
    }
    STAMP(5);
    wg_barrier();
    wg_barrier();
    STAMP(6);
    {
      const int off = WS_P2 + (i >> 2) * 65536 + (4 * g + (i & 3)) * 2048 + lane * 16;
      u32x4 p0, p1;
      for (unsigned spins = 0; spins < SPIN_CAP; ++spins) {
        asm volatile("" ::: "memory");
        p0 = __builtin_amdgcn_raw_buffer_load_b128(rws, off, 0, AUX_SC1);
        p1 = __builtin_amdgcn_raw_buffer_load_b128(rws, off + 1024, 0, AUX_SC1);
        if (!__any(has_sent(p0) || has_sent(p1))) break;
      }
      STAMP(7);
      f32x4 o = __builtin_bit_cast(f32x4, p0) + __builtin_bit_cast(f32x4, p1);
#pragma unroll
      for (int k = 0; k < 4; ++k) { float v = o[k]; v += dppf<ROR4>(v); v += dppf<ROR8>(v); o[k] = x32sum(x16sum(v)); }
      if (lane < 4) *(GAS u32x2*)(out + (i >> 2) * 1024 + (4 * g + (i & 3)) * 32 + lane * 8) = u32x2{pack_bf16(o.x, o.y), pack_bf16(o.z, o.w)};
      if (blockIdx.x == 0 && lane == 0) __hip_atomic_store((GAS u64*)ws, tag, __ATOMIC_RELAXED, __HIP_MEMORY_SCOPE_AGENT);
    }
    STAMP(8);
  }
  STAMP_OUT;
}

extern "C" void kernel_launch(void* const* d_in, const int* in_sizes, int n_in, void* d_out, int out_size, void* d_ws, size_t ws_size,
                              hipStream_t stream) {
  gqa_decode_kernel<<<256, NTHR, 0, stream>>>((const unsigned char*)d_in[0], (const unsigned char*)d_in[1], (const unsigned char*)d_in[2],
                                              (const unsigned char*)d_in[3], (const unsigned char*)d_in[4], (unsigned char*)d_out,
                                              (unsigned char*)d_ws);
}
```

```cpp
#define DLO 3
#define DHI 6
#define TOFF 200
#define TAU 27
#define SLPO 0
#define AUX_Q1 0
#include <hip/hip_runtime.h>
#include <hip/hip_bf16.h>

#define GAS __attribute__((address_space(1)))
#define LAS __attribute__((address_space(3)))
typedef unsigned u32x4 __attribute__((ext_vector_type(4)));
typedef unsigned u32x2 __attribute__((ext_vector_type(2)));
typedef float f32x4 __attribute__((ext_vector_type(4)));
typedef short s16x4 __attribute__((ext_vector_type(4)));
typedef __bf16 bf16x8 __attribute__((ext_vector_type(8)));
typedef __bf16 bf16x2 __attribute__((ext_vector_type(2)));
typedef float f32x2 __attribute__((ext_vector_type(2)));
typedef unsigned long long u64;

constexpr int NTHR = 576;
#define DK 4
#define DV DHI
#define PARK 36
#define PRE 0
#define AUX_WQ 2
#define ROT 1
#define TRIGQ 20
#define TRIGS 58
constexpr int AUX_NT = 2, AUX_SC1 = 16;
constexpr unsigned SENT = 0xFFFFFFFFu;
constexpr unsigned SPIN_CAP = 1u << 16;
constexpr int WS_QG = 4096;
constexpr int WS_P1 = WS_QG + 1048576;
constexpr int WS_ST = WS_P1 + 524288;
constexpr int WS_P2 = WS_ST + 8192;
constexpr int WS_END = WS_P2 + 524288;
constexpr int L_IMG = 0;
constexpr int L_RED = 16384;
constexpr int L_Q = 32768;
constexpr int L_ML = L_Q + 1024;
constexpr int L_F = L_ML + 256;
constexpr int L_OI = L_F + 512;
constexpr int L_AL = L_OI + 8 * 272;
constexpr int L_FLAG = L_AL + 128;
constexpr int L_X = L_FLAG + 16;
constexpr int L_END = L_X + 8 * 272;

constexpr float C2 = 0.08838834764831845f * 1.4426950408889634f;

#define STAMP(k) do { } while (0)
#define STAMP_DECL do { } while (0)
#define STAMP_OUT do { } while (0)

__device__ __forceinline__ f32x4 mfma(u32x4 a, u32x4 b, f32x4 c) {
  return __builtin_amdgcn_mfma_f32_16x16x32_bf16(__builtin_bit_cast(bf16x8, a), __builtin_bit_cast(bf16x8, b), c, 0, 0, 0);
}
template <int CTRL> __device__ __forceinline__ float dppf(float x) {
  return __builtin_bit_cast(float, __builtin_amdgcn_mov_dpp(__builtin_bit_cast(int, x), CTRL, 0xf, 0xf, true));
}
template <int CTRL> __device__ __forceinline__ unsigned dppu(unsigned x) {
  return (unsigned)__builtin_amdgcn_mov_dpp((int)x, CTRL, 0xf, 0xf, true);
}
constexpr int ROR4 = 0x124, ROR8 = 0x128, ROR12 = 0x12C, SHL4 = 0x104, SHL8 = 0x108, SHL12 = 0x10C, QID = 0xE4, XOR1 = 0xB1, XOR2 = 0x4E, HMIR = 0x141;
template <int BANKS> __device__ __forceinline__ float bank_set(float old, float y) {
  return __builtin_bit_cast(float, __builtin_amdgcn_update_dpp(__builtin_bit_cast(int, old), __builtin_bit_cast(int, y), QID, 0xf, BANKS, false));
}
__device__ __forceinline__ float x16max(float x) {
  auto s = __builtin_amdgcn_permlane16_swap(__float_as_uint(x), __float_as_uint(x), false, false);
  return fmaxf(__uint_as_float(s[0]), __uint_as_float(s[1]));
}
__device__ __forceinline__ float x32max(float x) {
  auto s = __builtin_amdgcn_permlane32_swap(__float_as_uint(x), __float_as_uint(x), false, false);
  return fmaxf(__uint_as_float(s[0]), __uint_as_float(s[1]));
}
__device__ __forceinline__ float x16sum(float x) {
  auto s = __builtin_amdgcn_permlane16_swap(__float_as_uint(x), __float_as_uint(x), false, false);
  return __uint_as_float(s[0]) + __uint_as_float(s[1]);
}
__device__ __forceinline__ float x32sum(float x) {
  auto s = __builtin_amdgcn_permlane32_swap(__float_as_uint(x), __float_as_uint(x), false, false);
  return __uint_as_float(s[0]) + __uint_as_float(s[1]);
}
__device__ __forceinline__ unsigned pack_bf16(float lo, float hi) {
  f32x2 v = {lo, hi};
  return __builtin_bit_cast(unsigned, __builtin_convertvector(v, bf16x2));
}
__device__ __forceinline__ u32x4 no_sent(f32x4 v) {
  u32x4 u = __builtin_bit_cast(u32x4, v);
  u.x = min(u.x, SENT - 1u); u.y = min(u.y, SENT - 1u); u.z = min(u.z, SENT - 1u); u.w = min(u.w, SENT - 1u);
  return u;
}
__device__ __forceinline__ bool has_sent(u32x4 u) { return max(max(u.x, u.y), max(u.z, u.w)) == SENT; }
__device__ __forceinline__ void wg_barrier() {
  asm volatile("s_waitcnt lgkmcnt(0)\n\ts_barrier" ::: "memory");
}
__device__ __forceinline__ __amdgpu_buffer_rsrc_t mk_rsrc(const void* p, int bytes) {
  return __builtin_amdgcn_make_buffer_rsrc((void*)p, 0, bytes, 0x00020000);
}
__device__ __forceinline__ void poll2(__amdgpu_buffer_rsrc_t rws, int off, u32x4& p0, u32x4& p1) {
  asm volatile("" ::: "memory");
  p0 = __builtin_amdgcn_raw_buffer_load_b128(rws, off, 0, AUX_SC1);
  p1 = __builtin_amdgcn_raw_buffer_load_b128(rws, off + 1024, 0, AUX_SC1);
  for (unsigned spins = 0; spins < SPIN_CAP; ++spins) {
    const bool n0 = has_sent(p0), n1 = has_sent(p1);
    if (!__any(n0 || n1)) break;
    asm volatile("" ::: "memory");
    const u32x4 t0 = __builtin_amdgcn_raw_buffer_load_b128(rws, n0 ? off : 0x7FFF0000, 0, AUX_SC1);
    const u32x4 t1 = __builtin_amdgcn_raw_buffer_load_b128(rws, n1 ? off + 1024 : 0x7FFF0000, 0, AUX_SC1);
    p0 = n0 ? t0 : p0; p1 = n1 ? t1 : p1;
  }
}
__device__ __forceinline__ u32x2 tr_read(LAS unsigned char* p) {
  return __builtin_bit_cast(u32x2, __builtin_amdgcn_ds_read_tr16_b64_v4i16((LAS s16x4*)p));
}
__device__ __forceinline__ int img_row(int k) { return (k & ~12) * 32 + ((k >> 2) & 1) * 256 + ((k >> 3) & 1) * 128; }

__global__ void __launch_bounds__(NTHR, 3)
gqa_decode_kernel(const unsigned char* __restrict__ x, const unsigned char* __restrict__ kc, const unsigned char* __restrict__ vc,
                  const unsigned char* __restrict__ wq, const unsigned char* __restrict__ wo, unsigned char* out,
                  unsigned char* ws) {
  __shared__ __attribute__((aligned(16))) unsigned char smem[L_END];
  LAS unsigned char* L = (LAS unsigned char*)smem;
  const int tid = threadIdx.x, lane = tid & 63, w = __builtin_amdgcn_readfirstlane(tid >> 6);
  const int g = blockIdx.x & 7, i = blockIdx.x >> 3;
  const int lg = lane >> 4, ln = lane & 15;
  const __amdgpu_buffer_rsrc_t rws = mk_rsrc(ws, WS_END);
  STAMP_DECL;
  STAMP(0);

  if (w < 8) {
    const int b = i >> 2, s = i & 3;
    const size_t base = ((size_t)(b * 8 + g) * 4096 + s * 1024) * 256;
    const __amdgpu_buffer_rsrc_t rk = mk_rsrc(kc + base, 262144), rv = mk_rsrc(vc + base, 262144);
#define CHUNK(c) ((c) * 32768 + ((w + ROT * (c)) & 7) * 4096)
    const int koff = ((lane >> 2) & 3) * 256 + lg * 64 + (lane & 3) * 16;
    const int voff = lane * 16;
    u32x4 kr[32], vr[32];
    const unsigned t0 = (unsigned)__builtin_amdgcn_s_memrealtime();
    bool late = false;
#define ISSUE(n, D) do { \
      if ((n) >= DHI) __builtin_amdgcn_s_waitcnt(0x0F70 | (DHI - 1)); \
      if (DLO != DHI && (n) >= DLO && !late) __builtin_amdgcn_s_waitcnt(0x0F70 | (DLO - 1)); \
      if (DLO != DHI && ((n) & 3) == 0 && (n) >= 4) late = (unsigned)__builtin_amdgcn_s_memrealtime() - t0 > (unsigned)(TOFF + (n) * TAU); \
      if ((n) < 32) kr[(n) & 31] = __builtin_amdgcn_raw_buffer_load_b128(rk, koff + ((n) & 3) * 1024, CHUNK(((n) & 31) >> 2), AUX_NT); \
      else vr[(n) & 31] = __builtin_amdgcn_raw_buffer_load_b128(rv, voff + ((n) & 3) * 1024, CHUNK(((n) & 31) >> 2), AUX_NT); \
      if ((n) == TRIGQ || (n) == TRIGS) *(volatile LAS unsigned*)(L + L_FLAG + 8) = (n); \
      __builtin_amdgcn_sched_barrier(0); } while (0)
#pragma unroll
    for (int n = 0; n < PARK; ++n) {
      if (n == PRE) wg_barrier();
      ISSUE(n, DK);
    }
    STAMP(1);
    for (unsigned spins = 0; spins < (SPIN_CAP << 6); ++spins) {
      if (__builtin_amdgcn_readfirstlane(*(volatile LAS unsigned*)(L + L_FLAG)) != 0u) break;
      __builtin_amdgcn_s_sleep(1);
    }
    asm volatile("" ::: "memory");
    STAMP(2);
    const u32x4 qf = *(LAS u32x4*)(L + L_Q + (lane & 3) * 256 + (4 * lg + ((lane >> 2) & 3)) * 16);
    float sc[8];
    constexpr int NQ = PARK + 4;
#pragma unroll
    for (int u0 = 0; u0 < 32; u0 += 4) {
      const int t = u0 >> 3;
      f32x4 d[4];
      float y[4];
#pragma unroll
      for (int k = 0; k < 4; ++k) d[k] = mfma(kr[u0 + k], qf, f32x4{0.f, 0.f, 0.f, 0.f});
      __builtin_amdgcn_sched_barrier(0);
#pragma unroll
      for (int k = 0; k < 4; ++k) y[k] = d[k][t] + dppf<ROR12>(d[k][(t + 1) & 3]);
      __builtin_amdgcn_sched_barrier(0);
#pragma unroll
      for (int k = 0; k < 4; ++k) y[k] += dppf<ROR8>(d[k][(t + 2) & 3]);
      __builtin_amdgcn_sched_barrier(0);
#pragma unroll
      for (int k = 0; k < 4; ++k) y[k] += dppf<ROR4>(d[k][(t + 3) & 3]);
      __builtin_amdgcn_sched_barrier(0);
#pragma unroll
      for (int k = 0; k < 4; ++k) {
        const int T = (u0 + k) & 7;
        sc[T] = t == 0 ? y[k] : t == 1 ? bank_set<2>(sc[T], y[k]) : t == 2 ? bank_set<4>(sc[T], y[k]) : bank_set<8>(sc[T], y[k]);
      }
      __builtin_amdgcn_sched_barrier(0);
      if ((u0 & 4) == 0) ISSUE(PARK + (u0 >> 3), DV);
    }
    float m = fmaxf(fmaxf(fmaxf(sc[0], sc[1]), fmaxf(sc[2], sc[3])), fmaxf(fmaxf(sc[4], sc[5]), fmaxf(sc[6], sc[7])));
    m = fmaxf(m, dppf<ROR4>(m)); m = fmaxf(m, dppf<ROR8>(m)); m = x32max(x16max(m));
    m *= C2;
    float ls = 0.f;
#pragma unroll
    for (int T = 0; T < 8; ++T) { sc[T] = __builtin_amdgcn_exp2f(fmaf(sc[T], C2, -m)); ls += sc[T]; }
    ls += dppf<ROR4>(ls); ls += dppf<ROR8>(ls); ls = x32sum(x16sum(ls));
    if (lane < 4) { *(LAS float*)(L + L_ML + lane * 32 + w * 4) = m; *(LAS float*)(L + L_ML + 128 + lane * 32 + w * 4) = ls; }
    asm volatile("s_waitcnt lgkmcnt(0)" ::: "memory");
    if (lane == 0) __hip_atomic_fetch_add((LAS unsigned*)(L + L_FLAG + 4), 1u, __ATOMIC_RELAXED, __HIP_MEMORY_SCOPE_WORKGROUP);
    STAMP(3);
    u32x4 pa;
#pragma unroll
    for (int k = 0; k < 4; ++k) pa[k] = pack_bf16(sc[2 * k], sc[2 * k + 1]);
    f32x4 acc[8];
#pragma unroll
    for (int j = 0; j < 8; ++j) acc[j] = f32x4{0.f, 0.f, 0.f, 0.f};
    __builtin_amdgcn_sched_barrier(0);
#pragma unroll
    for (int t = 0; t < 4; ++t) {
#pragma unroll
      for (int n = NQ; n < 64; ++n) if (n > 31 + 8 * t + DV && n <= 39 + 8 * t + DV || (t == 0 && n <= 31 + DV)) ISSUE(n, DV);
      u32x4 a;
#pragma unroll
      for (int k = 0; k < 4; ++k) a[k] = t == 0 ? pa[k] : t == 1 ? dppu<SHL4>(pa[k]) : t == 2 ? dppu<SHL8>(pa[k]) : dppu<SHL12>(pa[k]);
#pragma unroll
      for (int j = 0; j < 8; ++j) {
        u32x4 bb;
#pragma unroll
        for (int k = 0; k < 4; ++k) bb[k] = __builtin_amdgcn_perm(vr[8 * t + 2 * k + 1][j >> 1], vr[8 * t + 2 * k][j >> 1], (j & 1) ? 0x07060302u : 0x05040100u);
        acc[j] = mfma(a, bb, acc[j]);
      }
      __builtin_amdgcn_sched_barrier(0);
    }
    if (lane < 16) {
#pragma unroll
      for (int j = 0; j < 8; ++j) *(LAS f32x4*)(L + L_RED + w * 2048 + j * 256 + lane * 16) = acc[j];
    }
    STAMP(4);
    wg_barrier();
    STAMP(5);
    if (w < 2) {
      f32x4 v = *(LAS f32x4*)(L + L_RED + tid * 16) * *(LAS f32x4*)(L + L_AL);
#pragma unroll
      for (int ww = 1; ww < 8; ++ww) v += *(LAS f32x4*)(L + L_RED + ww * 2048 + tid * 16) * *(LAS f32x4*)(L + L_AL + ww * 16);
      __builtin_amdgcn_raw_buffer_store_b128(no_sent(v), rws, WS_P1 + g * 65536 + (tid >> 5) * 16384 + i * 512 + (tid & 31) * 16, 0, AUX_SC1);
    }
    STAMP(6);
    const int rb = i >> 3, cb = i & 7;
    u32x4 wa[4];
    {
      LAS unsigned char* ip = L + L_IMG + (w & 3) * 4096 + (lg >> 1) * 512 + (lg & 1) * 128 + ln * 8;
#pragma unroll
      for (int ks = 0; ks < 4; ++ks) { const u32x2 lo = tr_read(ip + ks * 1024), hi = tr_read(ip + ks * 1024 + 256); wa[ks] = u32x4{lo.x, lo.y, hi.x, hi.y}; }
    }
    const f32x4 f0 = *(LAS f32x4*)(L + L_F + w * 64 + (lane >> 5) * 16), f1 = *(LAS f32x4*)(L + L_F + w * 64 + 32 + (lane >> 5) * 16);
    {
      const int off = WS_P1 + g * 65536 + rb * 16384 + w * 2048 + lane * 16;
      u32x4 p0, p1;
      poll2(rws, off, p0, p1);
      f32x4 o = __builtin_bit_cast(f32x4, p0) * f0 + __builtin_bit_cast(f32x4, p1) * f1;
      o.x = x32sum(o.x); o.y = x32sum(o.y); o.z = x32sum(o.z); o.w = x32sum(o.w);
      if (lane < 32) *(LAS u32x2*)(L + L_OI + w * 272 + lane * 8) = u32x2{pack_bf16(o.x, o.y), pack_bf16(o.z, o.w)};
    }
    STAMP(7);
    wg_barrier();
    if (w < 4) {
      f32x4 d = {0.f, 0.f, 0.f, 0.f};
#pragma unroll
      for (int ks = 0; ks < 4; ++ks) d = mfma(wa[ks], *(LAS u32x4*)(L + L_OI + (ln & 7) * 272 + ks * 64 + lg * 16), d);
      if (ln < 8) __builtin_amdgcn_raw_buffer_store_b128(no_sent(d), rws, WS_P2 + ln * 65536 + (cb * 4 + w) * 2048 + (g * 4 + rb) * 64 + lg * 16, 0, AUX_SC1);
    }
    STAMP(8);
  } else {
    __builtin_amdgcn_s_setprio(3);
    const int gq = i & 7, kq = i >> 3, cq = (g + gq) & 7, sl = cq * 4 + kq;
    const u64 epoch = __hip_atomic_load((GAS u64*)ws, __ATOMIC_RELAXED, __HIP_MEMORY_SCOPE_AGENT);
    u32x4 wv[16], xv[2];
    if (lane < 4) *(LAS unsigned*)(L + L_FLAG + lane * 4) = 0u;
    {
      const __amdgpu_buffer_rsrc_t rq = mk_rsrc(wq + kq * 1048576 + gq * 1024 + cq * 128, 1048576 - 8192 + 128), rx = mk_rsrc(x, 8192);
#pragma unroll
      for (int t = 0; t < 2; ++t) xv[t] = __builtin_amdgcn_raw_buffer_load_b128(rx, lg * 1024 + kq * 256 + ln * 16, t * 4096, 0);
#pragma unroll
      for (int t = 0; t < 16; ++t) wv[t] = __builtin_amdgcn_raw_buffer_load_b128(rq, (lane >> 3) * 8192 + (lane & 7) * 16, t * 65536, AUX_NT);
      const u32x4 sv = {SENT, SENT, SENT, SENT};
      __builtin_amdgcn_raw_buffer_store_b128(sv, rws, WS_P1 + gq * 65536 + sl * 2048 + lane * 16, 0, AUX_SC1);
      __builtin_amdgcn_raw_buffer_store_b128(sv, rws, WS_P1 + gq * 65536 + sl * 2048 + 1024 + lane * 16, 0, AUX_SC1);
      if (lane < 2) __builtin_amdgcn_raw_buffer_store_b128(sv, rws, WS_ST + gq * 1024 + sl * 32 + lane * 16, 0, AUX_SC1);
      const int o2 = WS_P2 + (lane >> 4) * 65536 + (gq * 4 + ((lane >> 2) & 3)) * 2048 + (g * 4 + (i >> 3)) * 64 + (lane & 3) * 16;
      __builtin_amdgcn_raw_buffer_store_b128(sv, rws, o2, 0, AUX_SC1);
      __builtin_amdgcn_raw_buffer_store_b128(sv, rws, o2 + 4 * 65536, 0, AUX_SC1);
    }
    __builtin_amdgcn_sched_barrier(0);
    wg_barrier();
    const int img_w = L_IMG + ((lane & 7) >> 1) * 4096 + img_row(lane >> 3) + (lane & 1) * 16;
#pragma unroll
    for (int t = 0; t < 2; ++t) *(LAS u32x4*)(L + L_X + (4 * t + lg) * 272 + ln * 16) = xv[t];
#pragma unroll
    for (int t = 0; t < 16; ++t) *(LAS u32x4*)(L + img_w + img_row(8 * t)) = wv[t];
    STAMP(1);
    const u64 tag = epoch + 1ull;
    const unsigned tlo = (unsigned)tag & 0xFFu, thi = (unsigned)(tag >> 8);
    {
      LAS unsigned char* ip = L + L_IMG + (lg >> 1) * 512 + (lg & 1) * 128 + ln * 8;
      LAS unsigned char* xp = L + L_X + (ln & 7) * 272 + lg * 16;
      f32x4 d[4];
#pragma unroll
      for (int ct = 0; ct < 4; ++ct) d[ct] = f32x4{0.f, 0.f, 0.f, 0.f};
#pragma unroll
      for (int ks = 0; ks < 4; ++ks) {
        const u32x4 xb = *(LAS u32x4*)(xp + ks * 64);
#pragma unroll
        for (int ct = 0; ct < 4; ++ct) {
          const u32x2 a0 = tr_read(ip + ct * 4096 + ks * 1024), a1 = tr_read(ip + ct * 4096 + ks * 1024 + 256);
          d[ct] = mfma(u32x4{a0.x, a0.y, a1.x, a1.y}, xb, d[ct]);
        }
      }
      asm volatile("s_waitcnt vmcnt(0)" ::: "memory");
      const int off = WS_QG + gq * 131072 + (ln & 7) * 16384 + kq * 4096 + cq * 512 + lg * 32 + (ln >> 3) * 16;
#pragma unroll
      for (int ct = 0; ct < 4; ++ct) {
        const unsigned v0 = __float_as_uint(ln < 8 ? d[ct][0] : d[ct][2]), v1 = __float_as_uint(ln < 8 ? d[ct][1] : d[ct][3]);
        __builtin_amdgcn_raw_buffer_store_b128(u32x4{((v0 + 0x80u) & ~0xFFu) | tlo, thi, ((v1 + 0x80u) & ~0xFFu) | tlo, thi}, rws, off + ct * 128, 0, AUX_SC1);
      }
    }
    STAMP(2);
    const int rb = i >> 3, cb = i & 7;
    {
      const __amdgpu_buffer_rsrc_t ro = mk_rsrc(wo + g * 524288 + cb * 128, 524288 - 1024 + 128);
      const int kk = lane >> 3;
      const int voff = (kk & 3) * 131072 + (kk >> 2) * 8192 + rb * 2048 + (lane & 7) * 16;
#pragma unroll
      for (int t = 0; t < 16; ++t) {
        wv[t] = __builtin_amdgcn_raw_buffer_load_b128(ro, voff, (t & 7) * 16384 + (t >> 3) * 1024, AUX_NT);
        __builtin_amdgcn_sched_barrier(0);
        if (t < 15 && SLPO) __builtin_amdgcn_s_sleep(SLPO);
        __builtin_amdgcn_sched_barrier(0);
      }
    }
#pragma unroll
    for (int t = 0; t < 16; ++t) *(LAS u32x4*)(L + img_w + img_row(8 * t)) = wv[t];
    for (unsigned spins = 0; spins < (SPIN_CAP << 6); ++spins) {
      if (__builtin_amdgcn_readfirstlane(*(volatile LAS unsigned*)(L + L_FLAG + 8)) >= (unsigned)TRIGQ) break;
      __builtin_amdgcn_s_sleep(4);
    }
    STAMP(9);
    {
      const int off = WS_QG + g * 131072 + (i >> 2) * 16384 + lane * 16;
      for (unsigned spins = 0; spins < SPIN_CAP; ++spins) {
        asm volatile("" ::: "memory");
        unsigned bad = 0u;
        if (AUX_Q1 != AUX_SC1 && spins == 0) {
#pragma unroll
          for (int t = 0; t < 16; ++t) wv[t] = __builtin_amdgcn_raw_buffer_load_b128(rws, off + (t & 3) * 1024, (t >> 2) * 4096, AUX_Q1);
        } else {
#pragma unroll
          for (int t = 0; t < 16; ++t) wv[t] = __builtin_amdgcn_raw_buffer_load_b128(rws, off + (t & 3) * 1024, (t >> 2) * 4096, AUX_SC1);
        }
#pragma unroll
        for (int t = 0; t < 16; ++t) bad |= (((wv[t].x ^ tlo) | (wv[t].z ^ tlo)) & 0xFFu) | (wv[t].y ^ thi) | (wv[t].w ^ thi);
        if (!__any(bad != 0u)) break;
      }
#pragma unroll
      for (int h = 0; h < 4; ++h) {
        const float q0 = (__uint_as_float(wv[h].x & ~0xFFu) + __uint_as_float(wv[4 + h].x & ~0xFFu)) + (__uint_as_float(wv[8 + h].x & ~0xFFu) + __uint_as_float(wv[12 + h].x & ~0xFFu));
        const float q1 = (__uint_as_float(wv[h].z & ~0xFFu) + __uint_as_float(wv[4 + h].z & ~0xFFu)) + (__uint_as_float(wv[8 + h].z & ~0xFFu) + __uint_as_float(wv[12 + h].z & ~0xFFu));
        *(LAS unsigned*)(L + L_Q + h * 256 + lane * 4) = pack_bf16(q0, q1);
      }
      asm volatile("s_waitcnt lgkmcnt(0)" ::: "memory");
      *(volatile LAS unsigned*)(L + L_FLAG) = 1u;
    }
    STAMP(3);
    for (unsigned spins = 0; spins < (SPIN_CAP << 6); ++spins) {
      if (__builtin_amdgcn_readfirstlane(*(volatile LAS unsigned*)(L + L_FLAG + 4)) == 8u) break;
      __builtin_amdgcn_s_sleep(4);
    }
    asm volatile("" ::: "memory");
    STAMP(4);
    {
      const f32x4 m0 = *(LAS f32x4*)(L + L_ML + (lane & 3) * 32), m1 = *(LAS f32x4*)(L + L_ML + (lane & 3) * 32 + 16);
      const f32x4 l0 = *(LAS f32x4*)(L + L_ML + 128 + (lane & 3) * 32), l1 = *(LAS f32x4*)(L + L_ML + 128 + (lane & 3) * 32 + 16);
      const float M = fmaxf(fmaxf(fmaxf(m0.x, m0.y), fmaxf(m0.z, m0.w)), fmaxf(fmaxf(m1.x, m1.y), fmaxf(m1.z, m1.w)));
      float Ls = 0.f;
#pragma unroll
      for (int k = 0; k < 4; ++k) {
        const float a0 = __builtin_amdgcn_exp2f(m0[k] - M), a1 = __builtin_amdgcn_exp2f(m1[k] - M);
        Ls += l0[k] * a0 + l1[k] * a1;
        if (lane < 4) { *(LAS float*)(L + L_AL + k * 16 + lane * 4) = a0; *(LAS float*)(L + L_AL + 64 + k * 16 + lane * 4) = a1; }
      }
      const unsigned val = min(__float_as_uint((lane & 4) ? Ls : M), SENT - 1u);
      if (lane < 8) __builtin_amdgcn_raw_buffer_store_b32(val, rws, WS_ST + g * 1024 + i * 32 + lane * 4, 0, AUX_SC1);
    }
    for (unsigned spins = 0; spins < (SPIN_CAP << 6); ++spins) {
      if (__builtin_amdgcn_readfirstlane(*(volatile LAS unsigned*)(L + L_FLAG + 8)) >= (unsigned)TRIGS) break;
      __builtin_amdgcn_s_sleep(4);
    }
    STAMP(10);
    {
      u32x4 st;
      for (unsigned spins = 0; spins < SPIN_CAP; ++spins) {
        asm volatile("" ::: "memory");
        st = __builtin_amdgcn_raw_buffer_load_b128(rws, WS_ST + g * 1024 + lane * 16, 0, AUX_SC1);
        if (!__any(has_sent(st))) break;
        __builtin_amdgcn_s_sleep(8);
      }
      f32x4 f;
#pragma unroll
      for (int h = 0; h < 4; ++h) {
        const float v = __uint_as_float(st[h]), o = dppf<XOR1>(v);
        const float Ms = (lane & 1) ? o : v, Lv = (lane & 1) ? v : o;
        float mx = fmaxf(Ms, dppf<XOR2>(Ms)); mx = fmaxf(mx, dppf<HMIR>(mx));
        const float e = __builtin_amdgcn_exp2f(Ms - mx);
        float den = e * Lv; den += dppf<XOR2>(den); den += dppf<HMIR>(den);
        f[h] = e / den;
      }
      if (!(lane & 1)) *(LAS f32x4*)(L + L_F + (lane >> 1) * 16) = f;
    }
    STAMP(5);
    wg_barrier();
    wg_barrier();
    STAMP(6);
    {
      const int off = WS_P2 + (i >> 2) * 65536 + (4 * g + (i & 3)) * 2048 + lane * 16;
      u32x4 p0, p1;
      poll2(rws, off, p0, p1);
      STAMP(7);
      f32x4 o = __builtin_bit_cast(f32x4, p0) + __builtin_bit_cast(f32x4, p1);
#pragma unroll
      for (int k = 0; k < 4; ++k) { float v = o[k]; v += dppf<ROR4>(v); v += dppf<ROR8>(v); o[k] = x32sum(x16sum(v)); }
      if (lane < 4) *(GAS u32x2*)(out + (i >> 2) * 1024 + (4 * g + (i & 3)) * 32 + lane * 8) = u32x2{pack_bf16(o.x, o.y), pack_bf16(o.z, o.w)};
      if (blockIdx.x == 0 && lane == 0) __hip_atomic_store((GAS u64*)ws, tag, __ATOMIC_RELAXED, __HIP_MEMORY_SCOPE_AGENT);
    }
    STAMP(8);
  }
  STAMP_OUT;
}

extern "C" void kernel_launch(void* const* d_in, const int* in_sizes, int n_in, void* d_out, int out_size, void* d_ws, size_t ws_size,
                              hipStream_t stream) {
  gqa_decode_kernel<<<256, NTHR, 0, stream>>>((const unsigned char*)d_in[0], (const unsigned char*)d_in[1], (const unsigned char*)d_in[2],
                                              (const unsigned char*)d_in[3], (const unsigned char*)d_in[4], (unsigned char*)d_out,
                                              (unsigned char*)d_ws);
}
```

```cpp
#define TAU 290
#include <hip/hip_runtime.h>
#include <hip/hip_bf16.h>

#define GAS __attribute__((address_space(1)))
#define LAS __attribute__((address_space(3)))
typedef unsigned u32x4 __attribute__((ext_vector_type(4)));
typedef unsigned u32x2 __attribute__((ext_vector_type(2)));
typedef float f32x4 __attribute__((ext_vector_type(4)));
typedef short s16x4 __attribute__((ext_vector_type(4)));
typedef __bf16 bf16x8 __attribute__((ext_vector_type(8)));
typedef __bf16 bf16x2 __attribute__((ext_vector_type(2)));
typedef float f32x2 __attribute__((ext_vector_type(2)));
typedef unsigned long long u64;

constexpr int NTHR = 576;
#define DLO 2
#define DHI 6
#define DMID 3
#define ELO -30
#define EHI 30
#define EVAL 2
#define DINIT 6
#define TOFF 10
#define PARK 36
#define TRIGQ 20
#define TRIGS 54
#define AUX_Q1 0
#define SLPI 6
constexpr int AUX_NT = 2, AUX_SC1 = 16;
constexpr unsigned SENT = 0xFFFFFFFFu;
constexpr unsigned SPIN_CAP = 1u << 16;
constexpr int WS_QG = 4096;
constexpr int WS_P1 = WS_QG + 1048576;
constexpr int WS_ST = WS_P1 + 262144;
constexpr int WS_P2 = WS_ST + 8192;
constexpr int WS_END = WS_P2 + 524288;
constexpr int L_IMG = 0;
constexpr int L_RED = 16384;
constexpr int L_Q = 32768;
constexpr int L_ML = L_Q + 1024;
constexpr int L_OI = L_ML + 256;
constexpr int L_AL = L_OI + 8 * 272;
constexpr int L_FLAG = L_AL + 128;
constexpr int L_X = L_FLAG + 16;
constexpr int L_END = L_X + 8 * 272;

constexpr float C2 = 0.08838834764831845f * 1.4426950408889634f;

#define STAMP(k) do { } while (0)
#define STAMP_DECL do { } while (0)
#define STAMP_OUT do { } while (0)

__device__ __forceinline__ f32x4 mfma(u32x4 a, u32x4 b, f32x4 c) {
  return __builtin_amdgcn_mfma_f32_16x16x32_bf16(__builtin_bit_cast(bf16x8, a), __builtin_bit_cast(bf16x8, b), c, 0, 0, 0);
}
template <int CTRL> __device__ __forceinline__ float dppf(float x) {
  return __builtin_bit_cast(float, __builtin_amdgcn_mov_dpp(__builtin_bit_cast(int, x), CTRL, 0xf, 0xf, true));
}
template <int CTRL> __device__ __forceinline__ unsigned dppu(unsigned x) {
  return (unsigned)__builtin_amdgcn_mov_dpp((int)x, CTRL, 0xf, 0xf, true);
}
constexpr int ROR4 = 0x124, ROR8 = 0x128, ROR12 = 0x12C, SHL4 = 0x104, SHL8 = 0x108, SHL12 = 0x10C, QID = 0xE4, XOR1 = 0xB1, XOR2 = 0x4E, HMIR = 0x141;
template <int BANKS> __device__ __forceinline__ float bank_set(float old, float y) {
  return __builtin_bit_cast(float, __builtin_amdgcn_update_dpp(__builtin_bit_cast(int, old), __builtin_bit_cast(int, y), QID, 0xf, BANKS, false));
}
__device__ __forceinline__ float x16max(float x) {
  auto s = __builtin_amdgcn_permlane16_swap(__float_as_uint(x), __float_as_uint(x), false, false);
  return fmaxf(__uint_as_float(s[0]), __uint_as_float(s[1]));
}
__device__ __forceinline__ float x32max(float x) {
  auto s = __builtin_amdgcn_permlane32_swap(__float_as_uint(x), __float_as_uint(x), false, false);
  return fmaxf(__uint_as_float(s[0]), __uint_as_float(s[1]));
}
__device__ __forceinline__ float x16sum(float x) {
  auto s = __builtin_amdgcn_permlane16_swap(__float_as_uint(x), __float_as_uint(x), false, false);
  return __uint_as_float(s[0]) + __uint_as_float(s[1]);
}
__device__ __forceinline__ float x32sum(float x) {
  auto s = __builtin_amdgcn_permlane32_swap(__float_as_uint(x), __float_as_uint(x), false, false);
  return __uint_as_float(s[0]) + __uint_as_float(s[1]);
}
__device__ __forceinline__ float tsum16(float a, float b) {
  auto s = __builtin_amdgcn_permlane16_swap(__float_as_uint(a), __float_as_uint(b), false, false);
  return __uint_as_float(s[0]) + __uint_as_float(s[1]);
}
__device__ __forceinline__ float tsum32(float a, float b) {
  auto s = __builtin_amdgcn_permlane32_swap(__float_as_uint(a), __float_as_uint(b), false, false);
  return __uint_as_float(s[0]) + __uint_as_float(s[1]);
}
__device__ __forceinline__ unsigned pack_bf16(float lo, float hi) {
  f32x2 v = {lo, hi};
  return __builtin_bit_cast(unsigned, __builtin_convertvector(v, bf16x2));
}
__device__ __forceinline__ u32x4 no_sent(f32x4 v) {
  u32x4 u = __builtin_bit_cast(u32x4, v);
  u.x = min(u.x, SENT - 1u); u.y = min(u.y, SENT - 1u); u.z = min(u.z, SENT - 1u); u.w = min(u.w, SENT - 1u);
  return u;
}
__device__ __forceinline__ bool has_sent(u32x4 u) { return max(max(u.x, u.y), max(u.z, u.w)) == SENT; }
__device__ __forceinline__ void wg_barrier() {
  asm volatile("s_waitcnt lgkmcnt(0)\n\ts_barrier" ::: "memory");
}
__device__ __forceinline__ __amdgpu_buffer_rsrc_t mk_rsrc(const void* p, int bytes) {
  return __builtin_amdgcn_make_buffer_rsrc((void*)p, 0, bytes, 0x00020000);
}
__device__ __forceinline__ u32x4 poll1(__amdgpu_buffer_rsrc_t rws, int off) {
  asm volatile("" ::: "memory");
  u32x4 p0 = __builtin_amdgcn_raw_buffer_load_b128(rws, off, 0, AUX_SC1);
#pragma unroll 1
  for (unsigned spins = 0; spins < SPIN_CAP; ++spins) {
    const bool n0 = has_sent(p0);
    if (!__any(n0)) break;
    asm volatile("" ::: "memory");
    const u32x4 t0 = __builtin_amdgcn_raw_buffer_load_b128(rws, n0 ? off : 0x7FFF0000, 0, AUX_SC1);
    p0 = n0 ? t0 : p0;
  }
  return p0;
}
__device__ __forceinline__ void poll2(__amdgpu_buffer_rsrc_t rws, int off, u32x4& p0, u32x4& p1) {
  asm volatile("" ::: "memory");
  p0 = __builtin_amdgcn_raw_buffer_load_b128(rws, off, 0, AUX_SC1);
  p1 = __builtin_amdgcn_raw_buffer_load_b128(rws, off + 1024, 0, AUX_SC1);
#pragma unroll 1
  for (unsigned spins = 0; spins < SPIN_CAP; ++spins) {
    const bool n0 = has_sent(p0), n1 = has_sent(p1);
    if (!__any(n0 || n1)) break;
    asm volatile("" ::: "memory");
    const u32x4 t0 = __builtin_amdgcn_raw_buffer_load_b128(rws, n0 ? off : 0x7FFF0000, 0, AUX_SC1);
    const u32x4 t1 = __builtin_amdgcn_raw_buffer_load_b128(rws, n1 ? off + 1024 : 0x7FFF0000, 0, AUX_SC1);
    p0 = n0 ? t0 : p0; p1 = n1 ? t1 : p1;
  }
}
__device__ __forceinline__ u32x2 tr_read(LAS unsigned char* p) {
  return __builtin_bit_cast(u32x2, __builtin_amdgcn_ds_read_tr16_b64_v4i16((LAS s16x4*)p));
}
__device__ __forceinline__ int img_row(int k) { return (k & ~12) * 32 + ((k >> 2) & 1) * 256 + ((k >> 3) & 1) * 128; }

template <int SLP> __device__ __forceinline__ void lds_wait_ge(LAS unsigned char* p, unsigned want) {
#pragma unroll 1
  for (unsigned spins = 0; spins < (SPIN_CAP << 6); ++spins) {
    if ((unsigned)__builtin_amdgcn_readfirstlane(*(volatile LAS unsigned*)p) >= want) break;
    __builtin_amdgcn_s_sleep(SLP);
  }
  asm volatile("" ::: "memory");
}
typedef __attribute__((address_space(4))) const u64 karg_t;
__device__ __forceinline__ karg_t* kargs() { karg_t* p = (karg_t*)__builtin_amdgcn_kernarg_segment_ptr(); asm volatile("" : "+s"(p)); return p; }

__global__ void __launch_bounds__(NTHR, 3)
gqa_decode_kernel(const void* kc_, const void* vc_, void* ws_, const void* wq_, const void* x_, const void* wo_, void* out_) {
  __shared__ __attribute__((aligned(16))) unsigned char smem[L_END];
  LAS unsigned char* L = (LAS unsigned char*)smem;
  const int tid = threadIdx.x, lane = tid & 63, w = __builtin_amdgcn_readfirstlane(tid >> 6);
  const int g = blockIdx.x & 7, i = blockIdx.x >> 3;
  const int lg = lane >> 4, ln = lane & 15;
  const int rb = i >> 3, cb = i & 7;
  STAMP_DECL;
  STAMP(0);

  if (__builtin_expect(w < 8, 1)) {
    karg_t* ka = kargs();
    const size_t base = ((size_t)((i >> 2) * 8 + g) * 4096 + (i & 3) * 1024) * 256;
    const __amdgpu_buffer_rsrc_t rk = mk_rsrc((const unsigned char*)ka[0] + base, 262144), rv = mk_rsrc((const unsigned char*)ka[1] + base, 262144);
    const __amdgpu_buffer_rsrc_t rws = mk_rsrc((const void*)ka[2], WS_END);
#define CHUNK(c) ((c) * 32768 + ((w + (c)) & 7) * 4096)
    const int koff = ((lane >> 2) & 3) * 256 + lg * 64 + (lane & 3) * 16;
    const int voff = lane * 16;
    u32x4 kr[32], vr[32];
#pragma unroll
    for (int n = 0; n < DINIT; ++n) kr[n] = __builtin_amdgcn_raw_buffer_load_b128(rk, koff + (n & 3) * 1024, CHUNK(n >> 2), AUX_NT);
    __builtin_amdgcn_sched_barrier(0);
    STAMP(8);
    const unsigned t0 = (unsigned)__builtin_amdgcn_s_memrealtime();
    wg_barrier();
    int lag = 0;
#define ISSUE(n) do { \
      __builtin_amdgcn_s_waitcnt(0x0F70 | (DHI - 1)); \
      if (lag < EHI) { __builtin_amdgcn_s_waitcnt(0x0F70 | (DMID - 1)); if (lag < ELO) __builtin_amdgcn_s_waitcnt(0x0F70 | (DLO - 1)); } \
      if (((n) & (EVAL - 1)) == 0) lag = (int)((unsigned)__builtin_amdgcn_s_memrealtime() - t0) - (TOFF + ((n) * TAU) / 10); \
      if ((n) < 32) kr[(n) & 31] = __builtin_amdgcn_raw_buffer_load_b128(rk, koff + ((n) & 3) * 1024, CHUNK(((n) & 31) >> 2), AUX_NT); \
      else vr[(n) & 31] = __builtin_amdgcn_raw_buffer_load_b128(rv, voff + ((n) & 3) * 1024, CHUNK(((n) & 31) >> 2), AUX_NT); \
      if ((n) == TRIGQ || (n) == TRIGS) *(volatile LAS unsigned*)(L + L_FLAG + 8) = (n); \
      if ((n) == 63 && lane == 0) __hip_atomic_fetch_add((LAS unsigned*)(L + L_FLAG + 12), 1u, __ATOMIC_RELAXED, __HIP_MEMORY_SCOPE_WORKGROUP); \
      __builtin_amdgcn_sched_barrier(0); } while (0)
#pragma unroll
    for (int n = DINIT; n < PARK; ++n) {
      ISSUE(n);
      if (n == 8) STAMP(10);
      if (n == 14) STAMP(11);
    }
    STAMP(1);
    lds_wait_ge<1>(L + L_FLAG, 1u);
    STAMP(2);
    const u32x4 qf = *(LAS u32x4*)(L + L_Q + (lane & 3) * 256 + (4 * lg + ((lane >> 2) & 3)) * 16);
    float sc[8];
    constexpr int NQ = PARK + 4;
#pragma unroll
    for (int u0 = 0; u0 < 32; u0 += 4) {
      const int t = u0 >> 3;
      f32x4 d[4];
      float y[4];
#pragma unroll
      for (int k = 0; k < 4; ++k) d[k] = mfma(kr[u0 + k], qf, f32x4{0.f, 0.f, 0.f, 0.f});
      __builtin_amdgcn_sched_barrier(0);
#pragma unroll
      for (int k = 0; k < 4; ++k) y[k] = d[k][t] + dppf<ROR12>(d[k][(t + 1) & 3]);
      __builtin_amdgcn_sched_barrier(0);
#pragma unroll
      for (int k = 0; k < 4; ++k) y[k] += dppf<ROR8>(d[k][(t + 2) & 3]);
      __builtin_amdgcn_sched_barrier(0);
#pragma unroll
      for (int k = 0; k < 4; ++k) y[k] += dppf<ROR4>(d[k][(t + 3) & 3]);
      __builtin_amdgcn_sched_barrier(0);
#pragma unroll
      for (int k = 0; k < 4; ++k) {
        const int T = (u0 + k) & 7;
        sc[T] = t == 0 ? y[k] : t == 1 ? bank_set<2>(sc[T], y[k]) : t == 2 ? bank_set<4>(sc[T], y[k]) : bank_set<8>(sc[T], y[k]);
      }
      __builtin_amdgcn_sched_barrier(0);
      if ((u0 & 4) == 0) ISSUE(PARK + (u0 >> 3));
    }
    float m = fmaxf(fmaxf(fmaxf(sc[0], sc[1]), fmaxf(sc[2], sc[3])), fmaxf(fmaxf(sc[4], sc[5]), fmaxf(sc[6], sc[7])));
    m = fmaxf(m, dppf<ROR4>(m)); m = fmaxf(m, dppf<ROR8>(m)); m = x32max(x16max(m));
    m *= C2;
    float ls = 0.f;
#pragma unroll
    for (int T = 0; T < 8; ++T) { sc[T] = __builtin_amdgcn_exp2f(fmaf(sc[T], C2, -m)); ls += sc[T]; }
    ls += dppf<ROR4>(ls); ls += dppf<ROR8>(ls); ls = x32sum(x16sum(ls));
    if (lane < 4) { *(LAS float*)(L + L_ML + lane * 32 + w * 4) = m; *(LAS float*)(L + L_ML + 128 + lane * 32 + w * 4) = ls; }
    asm volatile("s_waitcnt lgkmcnt(0)" ::: "memory");
    if (lane == 0) __hip_atomic_fetch_add((LAS unsigned*)(L + L_FLAG + 4), 1u, __ATOMIC_RELAXED, __HIP_MEMORY_SCOPE_WORKGROUP);
    STAMP(3);
    u32x4 pa;
#pragma unroll
    for (int k = 0; k < 4; ++k) pa[k] = pack_bf16(sc[2 * k], sc[2 * k + 1]);
    f32x4 acc[8];
#pragma unroll
    for (int j = 0; j < 8; ++j) acc[j] = f32x4{0.f, 0.f, 0.f, 0.f};
    __builtin_amdgcn_sched_barrier(0);
#pragma unroll
    for (int t = 0; t < 4; ++t) {
#pragma unroll
      for (int n = NQ; n < 64; ++n) if ((n > 31 + 8 * t + DHI && n <= 39 + 8 * t + DHI) || (t == 0 && n <= 31 + DHI)) ISSUE(n);
      u32x4 a, bb[8];
#pragma unroll
      for (int k = 0; k < 4; ++k) a[k] = t == 0 ? pa[k] : t == 1 ? dppu<SHL4>(pa[k]) : t == 2 ? dppu<SHL8>(pa[k]) : dppu<SHL12>(pa[k]);
#pragma unroll
      for (int k = 0; k < 4; ++k) {
#pragma unroll
        for (int j = 0; j < 8; ++j) bb[j][k] = __builtin_amdgcn_perm(vr[8 * t + 2 * k + 1][j >> 1], vr[8 * t + 2 * k][j >> 1], (j & 1) ? 0x07060302u : 0x05040100u);
        if (t == 3 && k < 3) __builtin_amdgcn_sched_barrier(0);
      }
#pragma unroll
      for (int j = 0; j < 8; ++j) acc[j] = mfma(a, bb[j], acc[j]);
      __builtin_amdgcn_sched_barrier(0);
    }
    if (lane < 16) {
#pragma unroll
      for (int j = 0; j < 8; ++j) *(LAS f32x4*)(L + L_RED + w * 2048 + j * 256 + lane * 16) = acc[j];
    }
    STAMP(4);
    wg_barrier();
    STAMP(5);
    if (w < 2) {
      f32x4 v = *(LAS f32x4*)(L + L_RED + tid * 16) * *(LAS f32x4*)(L + L_AL);
#pragma unroll
      for (int ww = 1; ww < 8; ++ww) v += *(LAS f32x4*)(L + L_RED + ww * 2048 + tid * 16) * *(LAS f32x4*)(L + L_AL + ww * 16);
      const unsigned h0 = min(pack_bf16(v.x, v.y), SENT - 1u), h1 = min(pack_bf16(v.z, v.w), SENT - 1u);
      const u32x4 pk = {h0, h1, dppu<XOR1>(h0), dppu<XOR1>(h1)};
      if (!(lane & 1)) __builtin_amdgcn_raw_buffer_store_b128(pk, rws, WS_P1 + g * 32768 + (tid >> 5) * 8192 + i * 256 + (tid & 31) * 8, 0, AUX_SC1);
    }
    STAMP(6);
    {
      const u32x4 p = poll1(rws, WS_P1 + g * 32768 + rb * 8192 + w * 1024 + lane * 16);
      const float a0 = tsum16(__uint_as_float(p.x << 16), __uint_as_float(p.y << 16)), a1 = tsum16(__uint_as_float(p.x & 0xFFFF0000u), __uint_as_float(p.y & 0xFFFF0000u));
      const float a2 = tsum16(__uint_as_float(p.z << 16), __uint_as_float(p.w << 16)), a3 = tsum16(__uint_as_float(p.z & 0xFFFF0000u), __uint_as_float(p.w & 0xFFFF0000u));
      *(LAS unsigned*)(L + L_OI + w * 272 + ln * 16 + lg * 4) = pack_bf16(tsum32(a0, a2), tsum32(a1, a3));
    }
    STAMP(7);
    wg_barrier();
    if (w < 4) {
      LAS unsigned char* ip = L + L_IMG + w * 4096 + (lg >> 1) * 512 + (lg & 1) * 128 + ln * 8;
      f32x4 d0 = {0.f, 0.f, 0.f, 0.f}, d1 = {0.f, 0.f, 0.f, 0.f};
#pragma unroll
      for (int ks = 0; ks < 4; ks += 2) {
        const u32x2 a0 = tr_read(ip + ks * 1024), a1 = tr_read(ip + ks * 1024 + 256), a2 = tr_read(ip + ks * 1024 + 1024), a3 = tr_read(ip + ks * 1024 + 1280);
        d0 = mfma(u32x4{a0.x, a0.y, a1.x, a1.y}, *(LAS u32x4*)(L + L_OI + (ln & 7) * 272 + ks * 64 + lg * 16), d0);
        d1 = mfma(u32x4{a2.x, a2.y, a3.x, a3.y}, *(LAS u32x4*)(L + L_OI + (ln & 7) * 272 + ks * 64 + 64 + lg * 16), d1);
      }
      if (ln < 8) __builtin_amdgcn_raw_buffer_store_b128(no_sent(d0 + d1), rws, WS_P2 + ln * 65536 + (cb * 4 + w) * 2048 + (g * 4 + rb) * 64 + lg * 16, 0, AUX_SC1);
    }
    STAMP(9);
  } else {
    karg_t* ka = kargs();
    const u64 a_ws = ka[2], a_wq = ka[3], a_x = ka[4];
    if (lane < 4) *(LAS unsigned*)(L + L_FLAG + lane * 4) = 0u;
    wg_barrier();
    __builtin_amdgcn_s_setprio(3);
    const int gq = i & 7, kq = i >> 3, cq = (g + gq) & 7;
    const __amdgpu_buffer_rsrc_t rws = mk_rsrc((const void*)a_ws, WS_END);
    u32x4 wv[16], xv[2];
    const u32x2 ep = __builtin_amdgcn_raw_buffer_load_b64(rws, 0, 0, AUX_SC1);
    {
      const __amdgpu_buffer_rsrc_t rq = mk_rsrc((const unsigned char*)a_wq + kq * 1048576 + gq * 1024 + cq * 128, 1048576 - 8192 + 128), rx = mk_rsrc((const void*)a_x, 8192);
#pragma unroll
      for (int t = 0; t < 2; ++t) xv[t] = __builtin_amdgcn_raw_buffer_load_b128(rx, lg * 1024 + kq * 256 + ln * 16, t * 4096, 0);
#pragma unroll
      for (int t = 0; t < 16; ++t) wv[t] = __builtin_amdgcn_raw_buffer_load_b128(rq, (lane >> 3) * 8192 + (lane & 7) * 16, t * 65536, AUX_NT);
      const int sl = cq * 4 + kq;
      const u32x4 sv = {SENT, SENT, SENT, SENT};
      __builtin_amdgcn_raw_buffer_store_b128(sv, rws, WS_P1 + gq * 32768 + sl * 1024 + lane * 16, 0, AUX_SC1);
      if (lane < 2) __builtin_amdgcn_raw_buffer_store_b128(sv, rws, WS_ST + gq * 1024 + sl * 32 + lane * 16, 0, AUX_SC1);
      const int o2 = WS_P2 + lg * 65536 + (cb * 4 + ((lane >> 2) & 3)) * 2048 + (g * 4 + rb) * 64 + (lane & 3) * 16;
      __builtin_amdgcn_raw_buffer_store_b128(sv, rws, o2, 0, AUX_SC1);
      __builtin_amdgcn_raw_buffer_store_b128(sv, rws, o2 + 4 * 65536, 0, AUX_SC1);
    }
    __builtin_amdgcn_sched_barrier(0);
    const int img_w = L_IMG + ((lane & 7) >> 1) * 4096 + img_row(lane >> 3) + (lane & 1) * 16;
#pragma unroll
    for (int t = 0; t < 2; ++t) *(LAS u32x4*)(L + L_X + (4 * t + lg) * 272 + ln * 16) = xv[t];
#pragma unroll
    for (int t = 0; t < 16; ++t) *(LAS u32x4*)(L + img_w + img_row(8 * t)) = wv[t];
    STAMP(1);
    const u64 tag = (((u64)ep.y << 32) | ep.x) + 1ull;
    const unsigned tlo = (unsigned)tag & 0xFFu, thi = (unsigned)(tag >> 8);
    {
      LAS unsigned char* ip = L + L_IMG + (lg >> 1) * 512 + (lg & 1) * 128 + ln * 8;
      LAS unsigned char* xp = L + L_X + (ln & 7) * 272 + lg * 16;
      f32x4 d[4];
#pragma unroll
      for (int ct = 0; ct < 4; ++ct) d[ct] = f32x4{0.f, 0.f, 0.f, 0.f};
#pragma unroll
      for (int ks = 0; ks < 4; ++ks) {
        const u32x4 xb = *(LAS u32x4*)(xp + ks * 64);
#pragma unroll
        for (int ct = 0; ct < 4; ++ct) {
          const u32x2 a0 = tr_read(ip + ct * 4096 + ks * 1024), a1 = tr_read(ip + ct * 4096 + ks * 1024 + 256);
          d[ct] = mfma(u32x4{a0.x, a0.y, a1.x, a1.y}, xb, d[ct]);
        }
      }
      asm volatile("s_waitcnt vmcnt(0)" ::: "memory");
      const int off = WS_QG + gq * 131072 + (ln & 7) * 16384 + kq * 4096 + cq * 512 + lg * 32 + (ln >> 3) * 16;
#pragma unroll
      for (int ct = 0; ct < 4; ++ct) {
        const unsigned v0 = __float_as_uint(ln < 8 ? d[ct][0] : d[ct][2]), v1 = __float_as_uint(ln < 8 ? d[ct][1] : d[ct][3]);
        __builtin_amdgcn_raw_buffer_store_b128(u32x4{((v0 + 0x80u) & ~0xFFu) | tlo, thi, ((v1 + 0x80u) & ~0xFFu) | tlo, thi}, rws, off + ct * 128, 0, AUX_SC1);
      }
    }
    STAMP(2);
    lds_wait_ge<4>(L + L_FLAG + 8, TRIGQ);
    STAMP(9);
    {
      const int off = WS_QG + g * 131072 + (i >> 2) * 16384 + lane * 16;
#pragma unroll
      for (int t = 0; t < 16; ++t) wv[t] = __builtin_amdgcn_raw_buffer_load_b128(rws, off + (t & 3) * 1024, (t >> 2) * 4096, AUX_Q1);
#pragma unroll 1
      for (unsigned spins = 0; spins < SPIN_CAP; ++spins) {
        unsigned bad = 0u;
#pragma unroll
        for (int t = 0; t < 16; ++t) bad |= (((wv[t].x ^ tlo) | (wv[t].z ^ tlo)) & 0xFFu) | (wv[t].y ^ thi) | (wv[t].w ^ thi);
        if (!__any(bad != 0u)) break;
        asm volatile("" ::: "memory");
#pragma unroll
        for (int t = 0; t < 16; ++t) wv[t] = __builtin_amdgcn_raw_buffer_load_b128(rws, off + (t & 3) * 1024, (t >> 2) * 4096, AUX_SC1);
      }
#pragma unroll
      for (int h = 0; h < 4; ++h) {
        const float q0 = (__uint_as_float(wv[h].x & ~0xFFu) + __uint_as_float(wv[4 + h].x & ~0xFFu)) + (__uint_as_float(wv[8 + h].x & ~0xFFu) + __uint_as_float(wv[12 + h].x & ~0xFFu));
        const float q1 = (__uint_as_float(wv[h].z & ~0xFFu) + __uint_as_float(wv[4 + h].z & ~0xFFu)) + (__uint_as_float(wv[8 + h].z & ~0xFFu) + __uint_as_float(wv[12 + h].z & ~0xFFu));
        *(LAS unsigned*)(L + L_Q + h * 256 + lane * 4) = pack_bf16(q0, q1);
      }
      asm volatile("s_waitcnt lgkmcnt(0)" ::: "memory");
      *(volatile LAS unsigned*)(L + L_FLAG) = 1u;
    }
    STAMP(3);
    lds_wait_ge<4>(L + L_FLAG + 4, 8u);
    STAMP(4);
    const f32x4 m0 = *(LAS f32x4*)(L + L_ML + (lane & 3) * 32), m1 = *(LAS f32x4*)(L + L_ML + (lane & 3) * 32 + 16);
    {
      const f32x4 l0 = *(LAS f32x4*)(L + L_ML + 128 + (lane & 3) * 32), l1 = *(LAS f32x4*)(L + L_ML + 128 + (lane & 3) * 32 + 16);
      const float M = fmaxf(fmaxf(fmaxf(m0.x, m0.y), fmaxf(m0.z, m0.w)), fmaxf(fmaxf(m1.x, m1.y), fmaxf(m1.z, m1.w)));
      float Ls = 0.f;
#pragma unroll
      for (int k = 0; k < 4; ++k) Ls += l0[k] * __builtin_amdgcn_exp2f(m0[k] - M) + l1[k] * __builtin_amdgcn_exp2f(m1[k] - M);
      if (lane < 4) __builtin_amdgcn_raw_buffer_store_b64(u32x2{min(__float_as_uint(M), SENT - 1u), min(__float_as_uint(Ls), SENT - 1u)}, rws, WS_ST + g * 1024 + i * 32 + lane * 8, 0, AUX_SC1);
    }
    lds_wait_ge<4>(L + L_FLAG + 8, TRIGS);
    STAMP(10);
    {
      u32x2 st;
#pragma unroll 1
      for (unsigned spins = 0; spins < SPIN_CAP; ++spins) {
        asm volatile("" ::: "memory");
        st = __builtin_amdgcn_raw_buffer_load_b64(rws, WS_ST + g * 1024 + (i >> 2) * 128 + ln * 8, 0, AUX_SC1);
        if (!__any(max(st.x, st.y) == SENT)) break;
        __builtin_amdgcn_s_sleep(8);
      }
      const float Ms = __uint_as_float(st.x);
      float Mg = fmaxf(Ms, dppf<ROR4>(Ms)); Mg = fmaxf(Mg, dppf<ROR8>(Mg));
      float Lg = __uint_as_float(st.y) * __builtin_amdgcn_exp2f(Ms - Mg);
      Lg += dppf<ROR4>(Lg); Lg += dppf<ROR8>(Lg);
      const float inv = 1.0f / Lg;
      if (lane < 4) {
#pragma unroll
        for (int k = 0; k < 4; ++k) {
          *(LAS float*)(L + L_AL + k * 16 + lane * 4) = __builtin_amdgcn_exp2f(m0[k] - Mg) * inv;
          *(LAS float*)(L + L_AL + 64 + k * 16 + lane * 4) = __builtin_amdgcn_exp2f(m1[k] - Mg) * inv;
        }
      }
    }
    STAMP(5);
    ka = kargs();
    const __amdgpu_buffer_rsrc_t ro = mk_rsrc((const unsigned char*)ka[5] + g * 524288 + cb * 128, 524288 - 1024 + 128);
    unsigned char* out = (unsigned char*)ka[6];
    lds_wait_ge<2>(L + L_FLAG + 12, 8u);
    {
      const int kk = lane >> 3;
      const int woff = (kk & 3) * 131072 + (kk >> 2) * 8192 + rb * 2048 + (lane & 7) * 16;
#pragma unroll
      for (int t = 0; t < 16; ++t) wv[t] = __builtin_amdgcn_raw_buffer_load_b128(ro, woff, (t & 7) * 16384 + (t >> 3) * 1024, AUX_NT);
    }
    __builtin_amdgcn_sched_barrier(0);
    __builtin_amdgcn_s_setprio(0);
    wg_barrier();
    __builtin_amdgcn_s_sleep(SLPI);
#pragma unroll
    for (int t = 0; t < 16; ++t) *(LAS u32x4*)(L + img_w + img_row(8 * t)) = wv[t];
    STAMP(11);
    wg_barrier();
    STAMP(6);
    {
      const int off = WS_P2 + (i >> 2) * 65536 + (4 * g + (i & 3)) * 2048 + lane * 16;
      u32x4 p0, p1;
      poll2(rws, off, p0, p1);
      STAMP(7);
      const f32x4 o = __builtin_bit_cast(f32x4, p0) + __builtin_bit_cast(f32x4, p1);
      float v = tsum32(tsum16(o.x, o.y), tsum16(o.z, o.w));
      v += dppf<ROR4>(v); v += dppf<ROR8>(v);
      if (ln < 4) *(GAS unsigned short*)(out + (i >> 2) * 1024 + (4 * g + (i & 3)) * 32 + ln * 8 + lg * 2) = (unsigned short)pack_bf16(v, v);
      if (blockIdx.x == 0 && lane == 0) __builtin_amdgcn_raw_buffer_store_b64(u32x2{(unsigned)tag, (unsigned)(tag >> 32)}, rws, 0, 0, AUX_SC1);
    }
    STAMP(8);
  }
  STAMP_OUT;
}

extern "C" void kernel_launch(void* const* d_in, const int* in_sizes, int n_in, void* d_out, int out_size, void* d_ws, size_t ws_size,
                              hipStream_t stream) {
  gqa_decode_kernel<<<256, NTHR, 0, stream>>>(d_in[1], d_in[2], d_ws, d_in[3], d_in[0], d_in[4], d_out);
}
```

```cpp
#define TAU 285
#define TOFF 110
#include <hip/hip_runtime.h>
#include <hip/hip_bf16.h>

#define GAS __attribute__((address_space(1)))
#define LAS __attribute__((address_space(3)))
typedef unsigned u32x4 __attribute__((ext_vector_type(4)));
typedef unsigned u32x2 __attribute__((ext_vector_type(2)));
typedef float f32x4 __attribute__((ext_vector_type(4)));
typedef short s16x4 __attribute__((ext_vector_type(4)));
typedef __bf16 bf16x8 __attribute__((ext_vector_type(8)));
typedef __bf16 bf16x2 __attribute__((ext_vector_type(2)));
typedef float f32x2 __attribute__((ext_vector_type(2)));
typedef unsigned long long u64;

constexpr int NTHR = 576;
#define DLO 1
#define DHI 6
#define DMID 3
#define ELO -40
#define EHI 30
#define EVAL 2
#define PD 1
#define PSLP 5
#define H1S0 24
#define H1S1 0
#define H2S0 24
#define H2S1 0
#define NTH 16
#define H1NEAR 0
#define NEARFL "nt"
#define DINIT 4
#define PARK 36
#define TRIGQ 20
#define TRIGS 54
#define AUX_Q1 0
#define SLPI 6
constexpr int AUX_NT = 2, AUX_SC1 = 16;
constexpr unsigned SENT = 0xFFFFFFFFu;
constexpr unsigned SPIN_CAP = 1u << 16;
constexpr int WS_HI = 1024;
constexpr int WS_QG = 4096;
constexpr int WS_P1 = WS_QG + 524288;
constexpr int WS_ST = WS_P1 + 262144;
constexpr int WS_P2 = WS_ST + 8192;
constexpr int WS_END = WS_P2 + 524288;
constexpr int L_IMG = 0;
constexpr int L_RED = 16384;
constexpr int L_Q = 32768;
constexpr int L_ML = L_Q + 1024;
constexpr int L_OI = L_ML + 256;
constexpr int L_AL = L_OI + 8 * 272;
constexpr int L_FLAG = L_AL + 128;
constexpr int L_X = L_FLAG + 32;
constexpr int L_END = L_X + 8 * 272;

constexpr float C2 = 0.08838834764831845f * 1.4426950408889634f;

#define STAMP(k) do { } while (0)
#define STAMP_DECL do { } while (0)
#define STAMP_OUT do { } while (0)

#define TEND do { } while (0)
__device__ __forceinline__ f32x4 mfma(u32x4 a, u32x4 b, f32x4 c) {
  return __builtin_amdgcn_mfma_f32_16x16x32_bf16(__builtin_bit_cast(bf16x8, a), __builtin_bit_cast(bf16x8, b), c, 0, 0, 0);
}
template <int CTRL> __device__ __forceinline__ float dppf(float x) {
  return __builtin_bit_cast(float, __builtin_amdgcn_mov_dpp(__builtin_bit_cast(int, x), CTRL, 0xf, 0xf, true));
}
template <int CTRL> __device__ __forceinline__ unsigned dppu(unsigned x) {
  return (unsigned)__builtin_amdgcn_mov_dpp((int)x, CTRL, 0xf, 0xf, true);
}
constexpr int ROR4 = 0x124, ROR8 = 0x128, ROR12 = 0x12C, SHL4 = 0x104, SHL8 = 0x108, SHL12 = 0x10C, QID = 0xE4, XOR1 = 0xB1, XOR2 = 0x4E, HMIR = 0x141;
template <int BANKS> __device__ __forceinline__ float bank_set(float old, float y) {
  return __builtin_bit_cast(float, __builtin_amdgcn_update_dpp(__builtin_bit_cast(int, old), __builtin_bit_cast(int, y), QID, 0xf, BANKS, false));
}
__device__ __forceinline__ float x16max(float x) {
  auto s = __builtin_amdgcn_permlane16_swap(__float_as_uint(x), __float_as_uint(x), false, false);
  return fmaxf(__uint_as_float(s[0]), __uint_as_float(s[1]));
}
__device__ __forceinline__ float x32max(float x) {
  auto s = __builtin_amdgcn_permlane32_swap(__float_as_uint(x), __float_as_uint(x), false, false);
  return fmaxf(__uint_as_float(s[0]), __uint_as_float(s[1]));
}
__device__ __forceinline__ float x16sum(float x) {
  auto s = __builtin_amdgcn_permlane16_swap(__float_as_uint(x), __float_as_uint(x), false, false);
  return __uint_as_float(s[0]) + __uint_as_float(s[1]);
}
__device__ __forceinline__ float x32sum(float x) {
  auto s = __builtin_amdgcn_permlane32_swap(__float_as_uint(x), __float_as_uint(x), false, false);
  return __uint_as_float(s[0]) + __uint_as_float(s[1]);
}
__device__ __forceinline__ float tsum16(float a, float b) {
  auto s = __builtin_amdgcn_permlane16_swap(__float_as_uint(a), __float_as_uint(b), false, false);
  return __uint_as_float(s[0]) + __uint_as_float(s[1]);
}
__device__ __forceinline__ float tsum32(float a, float b) {
  auto s = __builtin_amdgcn_permlane32_swap(__float_as_uint(a), __float_as_uint(b), false, false);
  return __uint_as_float(s[0]) + __uint_as_float(s[1]);
}
__device__ __forceinline__ unsigned pack_bf16(float lo, float hi) {
  f32x2 v = {lo, hi};
  return __builtin_bit_cast(unsigned, __builtin_convertvector(v, bf16x2));
}
__device__ __forceinline__ u32x4 no_sent(f32x4 v) {
  u32x4 u = __builtin_bit_cast(u32x4, v);
  u.x = min(u.x, SENT - 1u); u.y = min(u.y, SENT - 1u); u.z = min(u.z, SENT - 1u); u.w = min(u.w, SENT - 1u);
  return u;
}
__device__ __forceinline__ bool has_sent(u32x4 u) { return max(max(u.x, u.y), max(u.z, u.w)) == SENT; }
__device__ __forceinline__ void wg_barrier() {
  asm volatile("s_waitcnt lgkmcnt(0)\n\ts_barrier" ::: "memory");
}
__device__ __forceinline__ __amdgpu_buffer_rsrc_t mk_rsrc(const void* p, int bytes) {
  return __builtin_amdgcn_make_buffer_rsrc((void*)p, 0, bytes, 0x00020000);
}
template <int K, bool NEAR> __device__ __forceinline__ void poll_issue(u32x4& r, int off, u64 mask, u32x4 srd) {
  if (NEAR) asm volatile("s_mov_b64 exec, %2\n\tbuffer_load_dwordx4 %0, %1, %3, 0 offen offset:%4 " NEARFL "\n\ts_mov_b64 exec, -1"
               : "+v"(r) : "v"(off), "s"(mask), "s"(srd), "i"(K * 1024) : "memory");
  else asm volatile("s_mov_b64 exec, %2\n\tbuffer_load_dwordx4 %0, %1, %3, 0 offen offset:%4 sc1\n\ts_mov_b64 exec, -1"
               : "+v"(r) : "v"(off), "s"(mask), "s"(srd), "i"(K * 1024) : "memory");
}
template <int N, int S0, int S1, int NEARP> __device__ __forceinline__ void poll_words(u32x4 srd, int off, u32x4 (&r)[2], u32x4 (&res)[2]) {
  r[0] = u32x4{SENT, SENT, SENT, SENT}; r[1] = r[0];
  if (S0) __builtin_amdgcn_s_sleep(S0);
  u64 m0 = ~0ull, m1 = ~0ull;
#pragma unroll 1
  for (unsigned spins = 1; spins < SPIN_CAP; ++spins) {
    if (NEARP && (spins % (NEARP ? NEARP : 1)) != 0) {
      poll_issue<0, true>(r[0], off, m0 | 1ull, srd);
      if (N > 1) poll_issue<1, true>(r[1], off, m1 | 1ull, srd);
    } else {
      poll_issue<0, false>(r[0], off, m0 | 1ull, srd);
      if (N > 1) poll_issue<1, false>(r[1], off, m1 | 1ull, srd);
    }
    if (N > 1) asm volatile("s_waitcnt vmcnt(0)" : "+v"(r[0]), "+v"(r[1]) :: "memory");
    else asm volatile("s_waitcnt vmcnt(0)" : "+v"(r[0]) :: "memory");
    m0 = __builtin_amdgcn_ballot_w64(has_sent(r[0]));
    m1 = N > 1 ? __builtin_amdgcn_ballot_w64(has_sent(r[1])) : 0ull;
    if ((m0 | m1) == 0ull) break;
    if (S1) __builtin_amdgcn_s_sleep(S1);
  }
  res[0] = r[0]; res[1] = r[1];
}
#define POLL_KEEP(r) asm volatile("" :: "v"(r) : "memory")
__device__ __forceinline__ u32x4 mk_srd(u64 p, int bytes) { return u32x4{(unsigned)p, (unsigned)(p >> 32) & 0xFFFFu, (unsigned)bytes, 0x00020000u}; }
__device__ __forceinline__ u32x2 tr_read(LAS unsigned char* p) {
  return __builtin_bit_cast(u32x2, __builtin_amdgcn_ds_read_tr16_b64_v4i16((LAS s16x4*)p));
}
__device__ __forceinline__ int img_row(int k) { return (k & ~12) * 32 + ((k >> 2) & 1) * 256 + ((k >> 3) & 1) * 128; }

template <int SLP> __device__ __forceinline__ void lds_wait_ge(LAS unsigned char* p, unsigned want) {
#pragma unroll 1
  for (unsigned spins = 0; spins < (SPIN_CAP << 6); ++spins) {
    if ((unsigned)__builtin_amdgcn_readfirstlane(*(volatile LAS unsigned*)p) >= want) break;
    __builtin_amdgcn_s_sleep(SLP);
  }
  asm volatile("" ::: "memory");
}
typedef __attribute__((address_space(4))) const u64 karg_t;
__device__ __forceinline__ karg_t* kargs() { karg_t* p = (karg_t*)__builtin_amdgcn_kernarg_segment_ptr(); asm volatile("" : "+s"(p)); return p; }

__global__ void __launch_bounds__(NTHR, 3)
gqa_decode_kernel(const void* kc_, const void* vc_, void* ws_, const void* wq_, const void* x_, const void* wo_, void* out_) {
  karg_t* ka = kargs();
  const u64 a_k = ka[0], a_v = ka[1], a_ws = ka[2], a_wq = ka[3], a_x = ka[4];
  const unsigned t0 = (unsigned)__builtin_amdgcn_s_memrealtime();
  __builtin_amdgcn_sched_barrier(0);
  __shared__ __attribute__((aligned(16))) unsigned char smem[L_END];
  LAS unsigned char* L = (LAS unsigned char*)smem;
  const int tid = threadIdx.x, lane = tid & 63, w = __builtin_amdgcn_readfirstlane(tid >> 6);
  const int g = blockIdx.x & 7, i = blockIdx.x >> 3;
  const int lg = lane >> 4, ln = lane & 15;
  const int rb = i >> 3, cb = i & 7;
  const int gq = i & 7, kq = i >> 3, cq = (g + gq) & 7;
  const int img_w = L_IMG + ((lane & 7) >> 1) * 4096 + img_row(lane >> 3) + (lane & 1) * 16;
  STAMP_DECL;
  STAMP(0);
  int koff = ((lane >> 2) & 3) * 256 + lg * 64 + (lane & 3) * 16;
  int qoff = (lane >> 3) * 8192 + (lane & 7) * 16, xoff = kq * 256 + ln * 16;
  asm volatile("" : "+v"(koff), "+v"(qoff), "+v"(xoff));
  if (tid < 8) *(LAS unsigned*)(L + L_FLAG + tid * 4) = 0u;
  wg_barrier();

  if (__builtin_expect(w >= 8, 0)) {
    __builtin_amdgcn_s_setprio(3);
    const __amdgpu_buffer_rsrc_t rws = mk_rsrc((const void*)a_ws, WS_END);
    u32x4 wv[16];
    const u32x2 ep = __builtin_amdgcn_raw_buffer_load_b64(rws, 0, 0, AUX_SC1);
    {
      const int sl = cq * 4 + kq;
      const u32x4 sv = {SENT, SENT, SENT, SENT};
      __builtin_amdgcn_raw_buffer_store_b128(sv, rws, WS_P1 + gq * 32768 + sl * 1024 + lane * 16, 0, AUX_SC1);
      if (lane < 2) __builtin_amdgcn_raw_buffer_store_b128(sv, rws, WS_ST + gq * 1024 + sl * 32 + lane * 16, 0, AUX_SC1);
      const int o2 = WS_P2 + lg * 65536 + (cb * 4 + ((lane >> 2) & 3)) * 2048 + (g * 4 + rb) * 64 + (lane & 3) * 16;
      __builtin_amdgcn_raw_buffer_store_b128(sv, rws, o2, 0, AUX_SC1);
      __builtin_amdgcn_raw_buffer_store_b128(sv, rws, o2 + 4 * 65536, 0, AUX_SC1);
    }
    __builtin_amdgcn_sched_barrier(0);
    lds_wait_ge<1>(L + L_FLAG + 16, 8u);
    STAMP(1);
    const u64 tag = (((u64)ep.y << 32) | ep.x) + 1ull;
    {
      LAS unsigned char* ip = L + L_IMG + (lg >> 1) * 512 + (lg & 1) * 128 + ln * 8;
      LAS unsigned char* xp = L + L_X + (ln & 7) * 272 + lg * 16;
      f32x4 d[4];
#pragma unroll
      for (int ct = 0; ct < 4; ++ct) d[ct] = f32x4{0.f, 0.f, 0.f, 0.f};
#pragma unroll
      for (int ks = 0; ks < 4; ++ks) {
        const u32x4 xb = *(LAS u32x4*)(xp + ks * 64);
#pragma unroll
        for (int ct = 0; ct < 4; ++ct) {
          const u32x2 a0 = tr_read(ip + ct * 4096 + ks * 1024), a1 = tr_read(ip + ct * 4096 + ks * 1024 + 256);
          d[ct] = mfma(u32x4{a0.x, a0.y, a1.x, a1.y}, xb, d[ct]);
        }
      }
      const int off = WS_QG + gq * 65536 + (ln & 7) * 8192 + kq * 2048 + cq * 256 + (ln >> 3) * 128 + lg * 16;
      __builtin_amdgcn_raw_buffer_store_b128(__builtin_bit_cast(u32x4, ln < 8 ? d[0] : d[2]), rws, off, 0, AUX_SC1);
      __builtin_amdgcn_raw_buffer_store_b128(__builtin_bit_cast(u32x4, ln < 8 ? d[1] : d[3]), rws, off + 64, 0, AUX_SC1);
      asm volatile("s_waitcnt vmcnt(0)" ::: "memory");
      if (lane == 0) __builtin_amdgcn_raw_buffer_store_b64(u32x2{(unsigned)tag, (unsigned)(tag >> 32)}, rws, WS_HI + gq * 256 + (cq * 4 + kq) * 8, 0, AUX_SC1);
    }
    STAMP(2);
    lds_wait_ge<4>(L + L_FLAG + 8, TRIGQ);
    STAMP(9);
    {
#pragma unroll 1
      for (unsigned spins = 0; spins < SPIN_CAP; ++spins) {
        asm volatile("" ::: "memory");
        const u32x2 hi = __builtin_amdgcn_raw_buffer_load_b64(rws, WS_HI + g * 256 + (lane & 31) * 8, 0, AUX_SC1);
        if (!__any(((hi.x ^ (unsigned)tag) | (hi.y ^ (unsigned)(tag >> 32))) != 0u)) break;
        __builtin_amdgcn_s_sleep(8);
      }
      asm volatile("" ::: "memory");
      const int off = WS_QG + g * 65536 + (i >> 2) * 8192 + lane * 16;
#pragma unroll
      for (int t = 0; t < 8; ++t) wv[t] = __builtin_amdgcn_raw_buffer_load_b128(rws, off + (t & 1) * 1024, (t >> 1) * 2048, AUX_SC1);
#pragma unroll
      for (int hh = 0; hh < 2; ++hh) {
        const f32x4 q4 = (__builtin_bit_cast(f32x4, wv[hh]) + __builtin_bit_cast(f32x4, wv[2 + hh])) + (__builtin_bit_cast(f32x4, wv[4 + hh]) + __builtin_bit_cast(f32x4, wv[6 + hh]));
        *(LAS u32x2*)(L + L_Q + hh * 512 + lane * 8) = u32x2{pack_bf16(q4.x, q4.y), pack_bf16(q4.z, q4.w)};
      }
      asm volatile("s_waitcnt lgkmcnt(0)" ::: "memory");
      *(volatile LAS unsigned*)(L + L_FLAG) = 1u;
    }
    STAMP(3);
    lds_wait_ge<4>(L + L_FLAG + 4, 8u);
    STAMP(4);
    const f32x4 m0 = *(LAS f32x4*)(L + L_ML + (lane & 3) * 32), m1 = *(LAS f32x4*)(L + L_ML + (lane & 3) * 32 + 16);
    {
      const f32x4 l0 = *(LAS f32x4*)(L + L_ML + 128 + (lane & 3) * 32), l1 = *(LAS f32x4*)(L + L_ML + 128 + (lane & 3) * 32 + 16);
      const float M = fmaxf(fmaxf(fmaxf(m0.x, m0.y), fmaxf(m0.z, m0.w)), fmaxf(fmaxf(m1.x, m1.y), fmaxf(m1.z, m1.w)));
      float Ls = 0.f;
#pragma unroll
      for (int k = 0; k < 4; ++k) Ls += l0[k] * __builtin_amdgcn_exp2f(m0[k] - M) + l1[k] * __builtin_amdgcn_exp2f(m1[k] - M);
      if (lane < 4) __builtin_amdgcn_raw_buffer_store_b64(u32x2{min(__float_as_uint(M), SENT - 1u), min(__float_as_uint(Ls), SENT - 1u)}, rws, WS_ST + g * 1024 + i * 32 + lane * 8, 0, AUX_SC1);
    }
    lds_wait_ge<4>(L + L_FLAG + 8, TRIGS);
    STAMP(10);
    {
      u32x2 st;
#pragma unroll 1
      for (unsigned spins = 0; spins < SPIN_CAP; ++spins) {
        asm volatile("" ::: "memory");
        st = __builtin_amdgcn_raw_buffer_load_b64(rws, WS_ST + g * 1024 + (i >> 2) * 128 + ln * 8, 0, AUX_SC1);
        if (!__any(max(st.x, st.y) == SENT)) break;
        __builtin_amdgcn_s_sleep(8);
      }
      const float Ms = __uint_as_float(st.x);
      float Mg = fmaxf(Ms, dppf<ROR4>(Ms)); Mg = fmaxf(Mg, dppf<ROR8>(Mg));
      float Lg = __uint_as_float(st.y) * __builtin_amdgcn_exp2f(Ms - Mg);
      Lg += dppf<ROR4>(Lg); Lg += dppf<ROR8>(Lg);
      const float inv = 1.0f / Lg;
      if (lane < 4) {
#pragma unroll
        for (int k = 0; k < 4; ++k) {
          *(LAS float*)(L + L_AL + k * 16 + lane * 4) = __builtin_amdgcn_exp2f(m0[k] - Mg) * inv;
          *(LAS float*)(L + L_AL + 64 + k * 16 + lane * 4) = __builtin_amdgcn_exp2f(m1[k] - Mg) * inv;
        }
      }
    }
    STAMP(5);
    const __amdgpu_buffer_rsrc_t ro = mk_rsrc((const unsigned char*)kargs()[5] + g * 524288 + cb * 128, 524288 - 1024 + 128);
    const __amdgpu_buffer_rsrc_t rout = mk_rsrc((const void*)kargs()[6], 8192);
    lds_wait_ge<2>(L + L_FLAG + 12, 8u);
    {
      const int kk = lane >> 3;
      const int woff = (kk & 3) * 131072 + (kk >> 2) * 8192 + rb * 2048 + (lane & 7) * 16;
#pragma unroll
      for (int t = 0; t < 16; ++t) wv[t] = __builtin_amdgcn_raw_buffer_load_b128(ro, woff, (t & 7) * 16384 + (t >> 3) * 1024, AUX_NT);
    }
    __builtin_amdgcn_sched_barrier(0);
    __builtin_amdgcn_s_setprio(0);
    wg_barrier();
    __builtin_amdgcn_s_sleep(SLPI);
#pragma unroll
    for (int t = 0; t < 16; ++t) *(LAS u32x4*)(L + img_w + img_row(8 * t)) = wv[t];
    STAMP(11);
    wg_barrier();
    STAMP(6);
    {
      const int off = WS_P2 + (i >> 2) * 65536 + (4 * g + (i & 3)) * 2048 + lane * 16;
      u32x4 pr[2], pw[2];
      poll_words<2, H2S0, H2S1, 0>(mk_srd(a_ws, WS_END), off, pr, pw);
      STAMP(7);
      const f32x4 o = __builtin_bit_cast(f32x4, pw[0]) + __builtin_bit_cast(f32x4, pw[1]);
      float v = tsum32(tsum16(o.x, o.y), tsum16(o.z, o.w));
      v += dppf<ROR4>(v); v += dppf<ROR8>(v);
      if (ln < 4) __builtin_amdgcn_raw_buffer_store_b16((unsigned short)pack_bf16(v, v), rout, (i >> 2) * 1024 + (4 * g + (i & 3)) * 32 + ln * 8 + lg * 2, 0, 0);
      if (blockIdx.x == 0 && lane == 0) __builtin_amdgcn_raw_buffer_store_b64(u32x2{(unsigned)tag, (unsigned)(tag >> 32)}, rws, 0, 0, AUX_SC1);
      STAMP(8);
      TEND;
      POLL_KEEP(pr[0]); POLL_KEEP(pr[1]);
    }
  } else {
    const size_t base = ((size_t)((i >> 2) * 8 + g) * 4096 + (i & 3) * 1024) * 256;
#define CHUNK(c) ((c) * 32768 + ((w + (c)) & 7) * 4096)
    const int voff = lane * 16;
    const __amdgpu_buffer_rsrc_t rk = mk_rsrc((const unsigned char*)a_k + base, 262144), rv = mk_rsrc((const unsigned char*)a_v + base, 262144);
    const __amdgpu_buffer_rsrc_t rws = mk_rsrc((const void*)a_ws, WS_END);
    u32x4 kr[32], vr[32];
    const __amdgpu_buffer_rsrc_t rq = mk_rsrc((const void*)a_wq, 4194304), rx = mk_rsrc((const void*)a_x, 8192);
    const int qs = kq * 1048576 + gq * 1024 + cq * 128 + w * 131072;
    const u32x4 wq0 = __builtin_amdgcn_raw_buffer_load_b128(rq, qoff, qs, AUX_NT);
    const u32x4 wq1 = __builtin_amdgcn_raw_buffer_load_b128(rq, qoff, qs + 65536, AUX_NT);
    const u32x4 xq = __builtin_amdgcn_raw_buffer_load_b128(rx, xoff, w * 1024, 0);
#pragma unroll
    for (int n = 0; n < DINIT; ++n) kr[n] = __builtin_amdgcn_raw_buffer_load_b128(rk, koff + (n & 3) * 1024, CHUNK(n >> 2), AUX_NT);
    __builtin_amdgcn_sched_barrier(0);
    STAMP(8);
    int lag = 0;
#define ISSUE(n) do { \
      __builtin_amdgcn_s_waitcnt(0x0F70 | (DHI - 1)); \
      if (lag < EHI) { __builtin_amdgcn_s_waitcnt(0x0F70 | (DMID - 1)); if ((n) >= NTH && lag < ELO) __builtin_amdgcn_s_waitcnt(0x0F70 | (DLO - 1)); } \
      if ((n) < 32) kr[(n) & 31] = __builtin_amdgcn_raw_buffer_load_b128(rk, koff + ((n) & 3) * 1024, CHUNK(((n) & 31) >> 2), AUX_NT); \
      else vr[(n) & 31] = __builtin_amdgcn_raw_buffer_load_b128(rv, voff + ((n) & 3) * 1024, CHUNK(((n) & 31) >> 2), AUX_NT); \
      __builtin_amdgcn_sched_barrier(0); \
      if (((n) & (EVAL - 1)) == 0) lag = (int)((unsigned)__builtin_amdgcn_s_memrealtime() - t0) - (TOFF + ((n) * TAU) / 10); \
      if ((n) == TRIGQ || (n) == TRIGS) *(volatile LAS unsigned*)(L + L_FLAG + 8) = (n); \
      if ((n) == 63 && lane == 0) __hip_atomic_fetch_add((LAS unsigned*)(L + L_FLAG + 12), 1u, __ATOMIC_RELAXED, __HIP_MEMORY_SCOPE_WORKGROUP); \
      __builtin_amdgcn_sched_barrier(0); } while (0)
#pragma unroll
    for (int n = DINIT; n < PARK; ++n) {
      if (n == DINIT) {
        *(LAS u32x4*)(L + img_w + w * 512) = wq0;
        *(LAS u32x4*)(L + img_w + w * 512 + 128) = wq1;
        *(LAS u32x4*)(L + L_X + w * 272 + ln * 16) = xq;
        asm volatile("s_waitcnt lgkmcnt(0)" ::: "memory");
        if (lane == 0) __hip_atomic_fetch_add((LAS unsigned*)(L + L_FLAG + 16), 1u, __ATOMIC_RELAXED, __HIP_MEMORY_SCOPE_WORKGROUP);
        __builtin_amdgcn_sched_barrier(0);
      }
      ISSUE(n);
      if (n == 8) STAMP(10);
      if (n == 14) STAMP(11);
    }
    STAMP(1);
    lds_wait_ge<1>(L + L_FLAG, 1u);
    STAMP(2);
    const u32x4 qf = *(LAS u32x4*)(L + L_Q + (lane & 3) * 256 + (4 * lg + ((lane >> 2) & 3)) * 16);
    float sc[8];
    constexpr int NQ = PARK + 4;
#pragma unroll
    for (int u0 = 0; u0 < 32; u0 += 4) {
      const int t = u0 >> 3;
      f32x4 d[4];
      float y[4];
#pragma unroll
      for (int k = 0; k < 4; ++k) d[k] = mfma(kr[u0 + k], qf, f32x4{0.f, 0.f, 0.f, 0.f});
      __builtin_amdgcn_sched_barrier(0);
#pragma unroll
      for (int k = 0; k < 4; ++k) y[k] = d[k][t] + dppf<ROR12>(d[k][(t + 1) & 3]);
      __builtin_amdgcn_sched_barrier(0);
#pragma unroll
      for (int k = 0; k < 4; ++k) y[k] += dppf<ROR8>(d[k][(t + 2) & 3]);
      __builtin_amdgcn_sched_barrier(0);
#pragma unroll
      for (int k = 0; k < 4; ++k) y[k] += dppf<ROR4>(d[k][(t + 3) & 3]);
      __builtin_amdgcn_sched_barrier(0);
#pragma unroll
      for (int k = 0; k < 4; ++k) {
        const int T = (u0 + k) & 7;
        sc[T] = t == 0 ? y[k] : t == 1 ? bank_set<2>(sc[T], y[k]) : t == 2 ? bank_set<4>(sc[T], y[k]) : bank_set<8>(sc[T], y[k]);
      }
      __builtin_amdgcn_sched_barrier(0);
      if ((u0 & 4) == 0) ISSUE(PARK + (u0 >> 3));
    }
    float m = fmaxf(fmaxf(fmaxf(sc[0], sc[1]), fmaxf(sc[2], sc[3])), fmaxf(fmaxf(sc[4], sc[5]), fmaxf(sc[6], sc[7])));
    m = fmaxf(m, dppf<ROR4>(m)); m = fmaxf(m, dppf<ROR8>(m)); m = x32max(x16max(m));
    m *= C2;
    float ls = 0.f;
#pragma unroll
    for (int T = 0; T < 8; ++T) { sc[T] = __builtin_amdgcn_exp2f(fmaf(sc[T], C2, -m)); ls += sc[T]; }
    ls += dppf<ROR4>(ls); ls += dppf<ROR8>(ls); ls = x32sum(x16sum(ls));
    if (lane < 4) { *(LAS float*)(L + L_ML + lane * 32 + w * 4) = m; *(LAS float*)(L + L_ML + 128 + lane * 32 + w * 4) = ls; }
    asm volatile("s_waitcnt lgkmcnt(0)" ::: "memory");
    if (lane == 0) __hip_atomic_fetch_add((LAS unsigned*)(L + L_FLAG + 4), 1u, __ATOMIC_RELAXED, __HIP_MEMORY_SCOPE_WORKGROUP);
    STAMP(3);
    u32x4 pa;
#pragma unroll
    for (int k = 0; k < 4; ++k) pa[k] = pack_bf16(sc[2 * k], sc[2 * k + 1]);
    f32x4 acc[8];
#pragma unroll
    for (int j = 0; j < 8; ++j) acc[j] = f32x4{0.f, 0.f, 0.f, 0.f};
    __builtin_amdgcn_sched_barrier(0);
#pragma unroll
    for (int t = 0; t < 4; ++t) {
#pragma unroll
      for (int n = NQ; n < 64; ++n) if ((n > 31 + 8 * t + DHI && n <= 39 + 8 * t + DHI) || (t == 0 && n <= 31 + DHI)) ISSUE(n);
      u32x4 a, bb[8];
#pragma unroll
      for (int k = 0; k < 4; ++k) a[k] = t == 0 ? pa[k] : t == 1 ? dppu<SHL4>(pa[k]) : t == 2 ? dppu<SHL8>(pa[k]) : dppu<SHL12>(pa[k]);
#pragma unroll
      for (int k = 0; k < 4; ++k) {
#pragma unroll
        for (int j = 0; j < 8; ++j) bb[j][k] = __builtin_amdgcn_perm(vr[8 * t + 2 * k + 1][j >> 1], vr[8 * t + 2 * k][j >> 1], (j & 1) ? 0x07060302u : 0x05040100u);
        if (t == 3 && k < 3) __builtin_amdgcn_sched_barrier(0);
      }
#pragma unroll
      for (int j = 0; j < 8; ++j) acc[j] = mfma(a, bb[j], acc[j]);
      __builtin_amdgcn_sched_barrier(0);
    }
    if (lane < 16) {
#pragma unroll
      for (int j = 0; j < 8; ++j) *(LAS f32x4*)(L + L_RED + w * 2048 + j * 256 + lane * 16) = acc[j];
    }
    STAMP(4);
    wg_barrier();
    STAMP(5);
    if (w < 2) {
      f32x4 v = *(LAS f32x4*)(L + L_RED + tid * 16) * *(LAS f32x4*)(L + L_AL);
#pragma unroll
      for (int ww = 1; ww < 8; ++ww) v += *(LAS f32x4*)(L + L_RED + ww * 2048 + tid * 16) * *(LAS f32x4*)(L + L_AL + ww * 16);
      const unsigned h0 = min(pack_bf16(v.x, v.y), SENT - 1u), h1 = min(pack_bf16(v.z, v.w), SENT - 1u);
      const u32x4 pk = {h0, h1, dppu<XOR1>(h0), dppu<XOR1>(h1)};
      if (!(lane & 1)) __builtin_amdgcn_raw_buffer_store_b128(pk, rws, WS_P1 + g * 32768 + (tid >> 5) * 8192 + i * 256 + (tid & 31) * 8, 0, AUX_SC1);
    }
    STAMP(6);
    u32x4 pr[2];
    {
      u32x4 pw[2];
      poll_words<1, H1S0, H1S1, H1NEAR>(mk_srd(a_ws, WS_END), WS_P1 + g * 32768 + rb * 8192 + w * 1024 + lane * 16, pr, pw);
      const u32x4 p = pw[0];
      const float a0 = tsum16(__uint_as_float(p.x << 16), __uint_as_float(p.y << 16)), a1 = tsum16(__uint_as_float(p.x & 0xFFFF0000u), __uint_as_float(p.y & 0xFFFF0000u));
      const float a2 = tsum16(__uint_as_float(p.z << 16), __uint_as_float(p.w << 16)), a3 = tsum16(__uint_as_float(p.z & 0xFFFF0000u), __uint_as_float(p.w & 0xFFFF0000u));
      *(LAS unsigned*)(L + L_OI + w * 272 + ln * 16 + lg * 4) = pack_bf16(tsum32(a0, a2), tsum32(a1, a3));
    }
    STAMP(7);
    wg_barrier();
    if (w < 4) {
      LAS unsigned char* ip = L + L_IMG + w * 4096 + (lg >> 1) * 512 + (lg & 1) * 128 + ln * 8;
      f32x4 d0 = {0.f, 0.f, 0.f, 0.f}, d1 = {0.f, 0.f, 0.f, 0.f};
#pragma unroll
      for (int ks = 0; ks < 4; ks += 2) {
        const u32x2 a0 = tr_read(ip + ks * 1024), a1 = tr_read(ip + ks * 1024 + 256), a2 = tr_read(ip + ks * 1024 + 1024), a3 = tr_read(ip + ks * 1024 + 1280);
        d0 = mfma(u32x4{a0.x, a0.y, a1.x, a1.y}, *(LAS u32x4*)(L + L_OI + (ln & 7) * 272 + ks * 64 + lg * 16), d0);
        d1 = mfma(u32x4{a2.x, a2.y, a3.x, a3.y}, *(LAS u32x4*)(L + L_OI + (ln & 7) * 272 + ks * 64 + 64 + lg * 16), d1);
      }
      if (ln < 8) __builtin_amdgcn_raw_buffer_store_b128(no_sent(d0 + d1), rws, WS_P2 + ln * 65536 + (cb * 4 + w) * 2048 + (g * 4 + rb) * 64 + lg * 16, 0, AUX_SC1);
    }
    STAMP(9);
    POLL_KEEP(pr[0]);
  }
  STAMP_OUT;
}

extern "C" void kernel_launch(void* const* d_in, const int* in_sizes, int n_in, void* d_out, int out_size, void* d_ws, size_t ws_size,
                              hipStream_t stream) {
  gqa_decode_kernel<<<256, NTHR, 0, stream>>>(d_in[1], d_in[2], d_ws, d_in[3], d_in[0], d_in[4], d_out);
}
```

```cpp
#define AUX_OUT 0
#include <hip/hip_runtime.h>
#include <hip/hip_bf16.h>

#define GAS __attribute__((address_space(1)))
#define LAS __attribute__((address_space(3)))
typedef unsigned u32x4 __attribute__((ext_vector_type(4)));
typedef unsigned u32x2 __attribute__((ext_vector_type(2)));
typedef float f32x4 __attribute__((ext_vector_type(4)));
typedef short s16x4 __attribute__((ext_vector_type(4)));
typedef __bf16 bf16x8 __attribute__((ext_vector_type(8)));
typedef __bf16 bf16x2 __attribute__((ext_vector_type(2)));
typedef float f32x2 __attribute__((ext_vector_type(2)));
typedef unsigned long long u64;

constexpr int NTHR = 576;
#define DLO 1
#define DHI 6
#define DMID 3
#define ELO -40
#define EHI 30
#define EVAL 2
#define PD 1
#define PSLP 5
#define H1S0 24
#define H1S1 0
#define H2S0 24
#define H2S1 0
#define NTH 16
#define H1NEAR 0
#define NEARFL "nt"
#define NEND 64
#define NBURST 64
#define H1PD 1
#define H2PD 1
#define H1SG 10
#define H2SG 10
#define AUX_PUB 16
#define AUX_CLR 16
#define POLLFL "sc1"
#define P2L 0
#define DINIT 4
#define TOFF 110
#define TAU 290
#define PARK 36
#define TRIGQ 20
#define TRIGS 54
#define AUX_Q1 0
#define SLPI 6
constexpr int AUX_NT = 2, AUX_SC1 = 16;
constexpr unsigned SENT = 0xFFFFFFFFu;
constexpr unsigned SPIN_CAP = 1u << 16;
constexpr int WS_HI = 1024;
constexpr int WS_QG = 4096;
constexpr int WS_P1 = WS_QG + 524288;
constexpr int WS_ST = WS_P1 + 262144;
constexpr int WS_P2 = WS_ST + 8192;
constexpr int WS_END = WS_P2 + 524288;
constexpr int L_IMG = 0;
constexpr int L_RED = 16384;
constexpr int L_Q = 32768;
constexpr int L_ML = L_Q + 1024;
constexpr int L_OI = L_ML + 256;
constexpr int L_AL = L_OI + 8 * 272;
constexpr int L_FLAG = L_AL + 128;
constexpr int L_X = L_FLAG + 32;
constexpr int L_END = L_X + 8 * 272;

constexpr float C2 = 0.08838834764831845f * 1.4426950408889634f;

#define STAMP(k) do { } while (0)
#define STAMP_DECL do { } while (0)
#define STAMP_OUT do { } while (0)

#define TEND do { } while (0)
__device__ __forceinline__ f32x4 mfma(u32x4 a, u32x4 b, f32x4 c) {
  return __builtin_amdgcn_mfma_f32_16x16x32_bf16(__builtin_bit_cast(bf16x8, a), __builtin_bit_cast(bf16x8, b), c, 0, 0, 0);
}
template <int CTRL> __device__ __forceinline__ float dppf(float x) {
  return __builtin_bit_cast(float, __builtin_amdgcn_mov_dpp(__builtin_bit_cast(int, x), CTRL, 0xf, 0xf, true));
}
template <int CTRL> __device__ __forceinline__ unsigned dppu(unsigned x) {
  return (unsigned)__builtin_amdgcn_mov_dpp((int)x, CTRL, 0xf, 0xf, true);
}
constexpr int ROR4 = 0x124, ROR8 = 0x128, ROR12 = 0x12C, SHL4 = 0x104, SHL8 = 0x108, SHL12 = 0x10C, QID = 0xE4, XOR1 = 0xB1, XOR2 = 0x4E, HMIR = 0x141;
template <int BANKS> __device__ __forceinline__ float bank_set(float old, float y) {
  return __builtin_bit_cast(float, __builtin_amdgcn_update_dpp(__builtin_bit_cast(int, old), __builtin_bit_cast(int, y), QID, 0xf, BANKS, false));
}
__device__ __forceinline__ float x16max(float x) {
  auto s = __builtin_amdgcn_permlane16_swap(__float_as_uint(x), __float_as_uint(x), false, false);
  return fmaxf(__uint_as_float(s[0]), __uint_as_float(s[1]));
}
__device__ __forceinline__ float x32max(float x) {
  auto s = __builtin_amdgcn_permlane32_swap(__float_as_uint(x), __float_as_uint(x), false, false);
  return fmaxf(__uint_as_float(s[0]), __uint_as_float(s[1]));
}
__device__ __forceinline__ float x16sum(float x) {
  auto s = __builtin_amdgcn_permlane16_swap(__float_as_uint(x), __float_as_uint(x), false, false);
  return __uint_as_float(s[0]) + __uint_as_float(s[1]);
}
__device__ __forceinline__ float x32sum(float x) {
  auto s = __builtin_amdgcn_permlane32_swap(__float_as_uint(x), __float_as_uint(x), false, false);
  return __uint_as_float(s[0]) + __uint_as_float(s[1]);
}
__device__ __forceinline__ float tsum16(float a, float b) {
  auto s = __builtin_amdgcn_permlane16_swap(__float_as_uint(a), __float_as_uint(b), false, false);
  return __uint_as_float(s[0]) + __uint_as_float(s[1]);
}
__device__ __forceinline__ float tsum32(float a, float b) {
  auto s = __builtin_amdgcn_permlane32_swap(__float_as_uint(a), __float_as_uint(b), false, false);
  return __uint_as_float(s[0]) + __uint_as_float(s[1]);
}
__device__ __forceinline__ unsigned pack_bf16(float lo, float hi) {
  f32x2 v = {lo, hi};
  return __builtin_bit_cast(unsigned, __builtin_convertvector(v, bf16x2));
}
__device__ __forceinline__ u32x4 no_sent(f32x4 v) {
  u32x4 u = __builtin_bit_cast(u32x4, v);
  u.x = min(u.x, SENT - 1u); u.y = min(u.y, SENT - 1u); u.z = min(u.z, SENT - 1u); u.w = min(u.w, SENT - 1u);
  return u;
}
__device__ __forceinline__ bool has_sent(u32x4 u) { return max(max(u.x, u.y), max(u.z, u.w)) == SENT; }
__device__ __forceinline__ void wg_barrier() {
  asm volatile("s_waitcnt lgkmcnt(0)\n\ts_barrier" ::: "memory");
}
__device__ __forceinline__ __amdgpu_buffer_rsrc_t mk_rsrc(const void* p, int bytes) {
  return __builtin_amdgcn_make_buffer_rsrc((void*)p, 0, bytes, 0x00020000);
}
template <int K> __device__ __forceinline__ void poll_issue(u32x4& r, int off, u64 mask, u32x4 srd) {
  asm volatile("s_mov_b64 exec, %2\n\tbuffer_load_dwordx4 %0, %1, %3, 0 offen offset:%4 " POLLFL "\n\ts_mov_b64 exec, -1"
               : "+v"(r) : "v"(off), "s"(mask), "s"(srd), "i"(K * (P2L ? 2048 : 1024)) : "memory");
}
template <int N, int S0, int PDN, int SG> __device__ __forceinline__ void poll_words(u32x4 srd, int off, u32x4 (&r)[2], u32x4 (&res)[2]) {
  r[0] = u32x4{SENT, SENT, SENT, SENT}; r[1] = r[0];
  if (S0) __builtin_amdgcn_s_sleep(S0);
  poll_issue<0>(r[0], off, ~0ull, srd);
  if (N > 1) poll_issue<1>(r[1], off, ~0ull, srd);
  asm volatile("s_waitcnt vmcnt(0)" : "+v"(r[0]), "+v"(r[1]) :: "memory");
#pragma unroll 1
  for (unsigned spins = 0; spins < SPIN_CAP; ++spins) {
    res[0] = r[0]; res[1] = r[1];
    asm volatile("" : "+v"(res[0]), "+v"(res[1]));
    const u64 m0 = __builtin_amdgcn_ballot_w64(has_sent(res[0]));
    const u64 m1 = N > 1 ? __builtin_amdgcn_ballot_w64(has_sent(res[1])) : 0ull;
    if ((m0 | m1) == 0ull) break;
    poll_issue<0>(r[0], off, m0 | 1ull, srd);
    if (N > 1) poll_issue<1>(r[1], off, m1 | 1ull, srd);
    if (PDN > 1 && spins < PDN - 1) __builtin_amdgcn_s_sleep(SG);
    asm volatile("s_waitcnt vmcnt(%2)" : "+v"(r[0]), "+v"(r[1]) : "i"((PDN - 1) * N) : "memory");
  }
}
#define POLL_KEEP(r) asm volatile("" :: "v"(r) : "memory")
__device__ __forceinline__ u32x4 mk_srd(u64 p, int bytes) { return u32x4{(unsigned)p, (unsigned)(p >> 32) & 0xFFFFu, (unsigned)bytes, 0x00020000u}; }
__device__ __forceinline__ u32x2 tr_read(LAS unsigned char* p) {
  return __builtin_bit_cast(u32x2, __builtin_amdgcn_ds_read_tr16_b64_v4i16((LAS s16x4*)p));
}
__device__ __forceinline__ int img_row(int k) { return (k & ~12) * 32 + ((k >> 2) & 1) * 256 + ((k >> 3) & 1) * 128; }

template <int SLP> __device__ __forceinline__ void lds_wait_ge(LAS unsigned char* p, unsigned want) {
#pragma unroll 1
  for (unsigned spins = 0; spins < (SPIN_CAP << 6); ++spins) {
    if ((unsigned)__builtin_amdgcn_readfirstlane(*(volatile LAS unsigned*)p) >= want) break;
    __builtin_amdgcn_s_sleep(SLP);
  }
  asm volatile("" ::: "memory");
}
typedef __attribute__((address_space(4))) const u64 karg_t;
__device__ __forceinline__ karg_t* kargs() { karg_t* p = (karg_t*)__builtin_amdgcn_kernarg_segment_ptr(); asm volatile("" : "+s"(p)); return p; }

__global__ void __launch_bounds__(NTHR, 3)
gqa_decode_kernel(const void* kc_, const void* vc_, void* ws_, const void* wq_, const void* x_, const void* wo_, void* out_) {
  karg_t* ka = kargs();
  const u64 a_k = ka[0], a_v = ka[1], a_ws = ka[2], a_wq = ka[3], a_x = ka[4];
  const unsigned t0 = (unsigned)__builtin_amdgcn_s_memrealtime();
  __builtin_amdgcn_sched_barrier(0);
  __shared__ __attribute__((aligned(16))) unsigned char smem[L_END];
  LAS unsigned char* L = (LAS unsigned char*)smem;
  const int tid = threadIdx.x, lane = tid & 63, w = __builtin_amdgcn_readfirstlane(tid >> 6);
  const int g = blockIdx.x & 7, i = blockIdx.x >> 3;
  const int lg = lane >> 4, ln = lane & 15;
  const int rb = i >> 3, cb = i & 7;
  const int gq = i & 7, kq = i >> 3, cq = (g + gq) & 7;
  const int img_w = L_IMG + ((lane & 7) >> 1) * 4096 + img_row(lane >> 3) + (lane & 1) * 16;
  STAMP_DECL;
  STAMP(0);
  int koff = ((lane >> 2) & 3) * 256 + lg * 64 + (lane & 3) * 16;
  int qoff = (lane >> 3) * 8192 + (lane & 7) * 16, xoff = kq * 256 + ln * 16;
  asm volatile("" : "+v"(koff), "+v"(qoff), "+v"(xoff));
  if (tid < 8) *(LAS unsigned*)(L + L_FLAG + tid * 4) = 0u;
  wg_barrier();

  if (__builtin_expect(w >= 8, 0)) {
    __builtin_amdgcn_s_setprio(3);
    const __amdgpu_buffer_rsrc_t rws = mk_rsrc((const void*)a_ws, WS_END);
    u32x4 wv[16];
    const u32x2 ep = __builtin_amdgcn_raw_buffer_load_b64(rws, 0, 0, AUX_SC1);
    {
      const int sl = cq * 4 + kq;
      const u32x4 sv = {SENT, SENT, SENT, SENT};
      __builtin_amdgcn_raw_buffer_store_b128(sv, rws, WS_P1 + gq * 32768 + sl * 1024 + lane * 16, 0, AUX_CLR);
      if (lane < 2) __builtin_amdgcn_raw_buffer_store_b128(sv, rws, WS_ST + gq * 1024 + sl * 32 + lane * 16, 0, AUX_SC1);
      const int o2 = P2L ? WS_P2 + lg * 65536 + (cb * 2 + ((lane >> 3) & 1)) * 4096 + (g * 4 + rb) * 128 + (lane & 7) * 16
                         : WS_P2 + lg * 65536 + (cb * 4 + ((lane >> 2) & 3)) * 2048 + (g * 4 + rb) * 64 + (lane & 3) * 16;
      __builtin_amdgcn_raw_buffer_store_b128(sv, rws, o2, 0, AUX_CLR);
      __builtin_amdgcn_raw_buffer_store_b128(sv, rws, o2 + 4 * 65536, 0, AUX_CLR);
    }
    __builtin_amdgcn_sched_barrier(0);
    lds_wait_ge<1>(L + L_FLAG + 16, 8u);
    STAMP(1);
    const u64 tag = (((u64)ep.y << 32) | ep.x) + 1ull;
    {
      LAS unsigned char* ip = L + L_IMG + (lg >> 1) * 512 + (lg & 1) * 128 + ln * 8;
      LAS unsigned char* xp = L + L_X + (ln & 7) * 272 + lg * 16;
      f32x4 d[4];
#pragma unroll
      for (int ct = 0; ct < 4; ++ct) d[ct] = f32x4{0.f, 0.f, 0.f, 0.f};
#pragma unroll
      for (int ks = 0; ks < 4; ++ks) {
        const u32x4 xb = *(LAS u32x4*)(xp + ks * 64);
#pragma unroll
        for (int ct = 0; ct < 4; ++ct) {
          const u32x2 a0 = tr_read(ip + ct * 4096 + ks * 1024), a1 = tr_read(ip + ct * 4096 + ks * 1024 + 256);
          d[ct] = mfma(u32x4{a0.x, a0.y, a1.x, a1.y}, xb, d[ct]);
        }
      }
      const int off = WS_QG + gq * 65536 + (ln & 7) * 8192 + kq * 2048 + cq * 256 + (ln >> 3) * 128 + lg * 16;
      __builtin_amdgcn_raw_buffer_store_b128(__builtin_bit_cast(u32x4, ln < 8 ? d[0] : d[2]), rws, off, 0, AUX_SC1);
      __builtin_amdgcn_raw_buffer_store_b128(__builtin_bit_cast(u32x4, ln < 8 ? d[1] : d[3]), rws, off + 64, 0, AUX_SC1);
      asm volatile("s_waitcnt vmcnt(0)" ::: "memory");
      if (lane == 0) __builtin_amdgcn_raw_buffer_store_b64(u32x2{(unsigned)tag, (unsigned)(tag >> 32)}, rws, WS_HI + gq * 256 + (cq * 4 + kq) * 8, 0, AUX_SC1);
    }
    STAMP(2);
    lds_wait_ge<4>(L + L_FLAG + 8, TRIGQ);
    STAMP(9);
    {
#pragma unroll 1
      for (unsigned spins = 0; spins < SPIN_CAP; ++spins) {
        asm volatile("" ::: "memory");
        const u32x2 hi = __builtin_amdgcn_raw_buffer_load_b64(rws, WS_HI + g * 256 + (lane & 31) * 8, 0, AUX_SC1);
        if (!__any(((hi.x ^ (unsigned)tag) | (hi.y ^ (unsigned)(tag >> 32))) != 0u)) break;
        __builtin_amdgcn_s_sleep(8);
      }
      asm volatile("" ::: "memory");
      const int off = WS_QG + g * 65536 + (i >> 2) * 8192 + lane * 16;
#pragma unroll
      for (int t = 0; t < 8; ++t) wv[t] = __builtin_amdgcn_raw_buffer_load_b128(rws, off + (t & 1) * 1024, (t >> 1) * 2048, AUX_SC1);
#pragma unroll
      for (int hh = 0; hh < 2; ++hh) {
        const f32x4 q4 = (__builtin_bit_cast(f32x4, wv[hh]) + __builtin_bit_cast(f32x4, wv[2 + hh])) + (__builtin_bit_cast(f32x4, wv[4 + hh]) + __builtin_bit_cast(f32x4, wv[6 + hh]));
        *(LAS u32x2*)(L + L_Q + hh * 512 + lane * 8) = u32x2{pack_bf16(q4.x, q4.y), pack_bf16(q4.z, q4.w)};
      }
      asm volatile("s_waitcnt lgkmcnt(0)" ::: "memory");
      *(volatile LAS unsigned*)(L + L_FLAG) = 1u;
    }
    STAMP(3);
    lds_wait_ge<4>(L + L_FLAG + 4, 8u);
    STAMP(4);
    const f32x4 m0 = *(LAS f32x4*)(L + L_ML + (lane & 3) * 32), m1 = *(LAS f32x4*)(L + L_ML + (lane & 3) * 32 + 16);
    {
      const f32x4 l0 = *(LAS f32x4*)(L + L_ML + 128 + (lane & 3) * 32), l1 = *(LAS f32x4*)(L + L_ML + 128 + (lane & 3) * 32 + 16);
      const float M = fmaxf(fmaxf(fmaxf(m0.x, m0.y), fmaxf(m0.z, m0.w)), fmaxf(fmaxf(m1.x, m1.y), fmaxf(m1.z, m1.w)));
      float Ls = 0.f;
#pragma unroll
      for (int k = 0; k < 4; ++k) Ls += l0[k] * __builtin_amdgcn_exp2f(m0[k] - M) + l1[k] * __builtin_amdgcn_exp2f(m1[k] - M);
      if (lane < 4) __builtin_amdgcn_raw_buffer_store_b64(u32x2{min(__float_as_uint(M), SENT - 1u), min(__float_as_uint(Ls), SENT - 1u)}, rws, WS_ST + g * 1024 + i * 32 + lane * 8, 0, AUX_SC1);
    }
    lds_wait_ge<4>(L + L_FLAG + 8, TRIGS);
    STAMP(10);
    {
      u32x2 st;
#pragma unroll 1
      for (unsigned spins = 0; spins < SPIN_CAP; ++spins) {
        asm volatile("" ::: "memory");
        st = __builtin_amdgcn_raw_buffer_load_b64(rws, WS_ST + g * 1024 + (i >> 2) * 128 + ln * 8, 0, AUX_SC1);
        if (!__any(max(st.x, st.y) == SENT)) break;
        __builtin_amdgcn_s_sleep(8);
      }
      const float Ms = __uint_as_float(st.x);
      float Mg = fmaxf(Ms, dppf<ROR4>(Ms)); Mg = fmaxf(Mg, dppf<ROR8>(Mg));
      float Lg = __uint_as_float(st.y) * __builtin_amdgcn_exp2f(Ms - Mg);
      Lg += dppf<ROR4>(Lg); Lg += dppf<ROR8>(Lg);
      const float inv = 1.0f / Lg;
      if (lane < 4) {
#pragma unroll
        for (int k = 0; k < 4; ++k) {
          *(LAS float*)(L + L_AL + k * 16 + lane * 4) = __builtin_amdgcn_exp2f(m0[k] - Mg) * inv;
          *(LAS float*)(L + L_AL + 64 + k * 16 + lane * 4) = __builtin_amdgcn_exp2f(m1[k] - Mg) * inv;
        }
      }
    }
    STAMP(5);
    const __amdgpu_buffer_rsrc_t ro = mk_rsrc((const unsigned char*)kargs()[5] + g * 524288 + cb * 128, 524288 - 1024 + 128);
    const __amdgpu_buffer_rsrc_t rout = mk_rsrc((const void*)kargs()[6], 8192);
    lds_wait_ge<2>(L + L_FLAG + 12, 8u);
    {
      const int kk = lane >> 3;
      const int woff = (kk & 3) * 131072 + (kk >> 2) * 8192 + rb * 2048 + (lane & 7) * 16;
#pragma unroll
      for (int t = 0; t < 16; ++t) wv[t] = __builtin_amdgcn_raw_buffer_load_b128(ro, woff, (t & 7) * 16384 + (t >> 3) * 1024, AUX_NT);
    }
    __builtin_amdgcn_sched_barrier(0);
    __builtin_amdgcn_s_setprio(0);
    wg_barrier();
    __builtin_amdgcn_s_sleep(SLPI);
#pragma unroll
    for (int t = 0; t < 16; ++t) *(LAS u32x4*)(L + img_w + img_row(8 * t)) = wv[t];
    STAMP(11);
    wg_barrier();
    STAMP(6);
    {
      const int off = P2L ? WS_P2 + (i >> 2) * 65536 + (2 * g + ((i >> 1) & 1)) * 4096 + (lane >> 2) * 128 + (i & 1) * 64 + (lane & 3) * 16 : WS_P2 + (i >> 2) * 65536 + (4 * g + (i & 3)) * 2048 + lane * 16;
      u32x4 pr[2], pw[2];
      poll_words<2, H2S0, H2PD, H2SG>(mk_srd(a_ws, WS_END), off, pr, pw);
      STAMP(7);
      const f32x4 o = __builtin_bit_cast(f32x4, pw[0]) + __builtin_bit_cast(f32x4, pw[1]);
      float v = tsum32(tsum16(o.x, o.y), tsum16(o.z, o.w));
      v += dppf<ROR4>(v); v += dppf<ROR8>(v);
      if (ln < 4) __builtin_amdgcn_raw_buffer_store_b16((unsigned short)pack_bf16(v, v), rout, (i >> 2) * 1024 + (4 * g + (i & 3)) * 32 + ln * 8 + lg * 2, 0, AUX_OUT);
      if (blockIdx.x == 0 && lane == 0) __builtin_amdgcn_raw_buffer_store_b64(u32x2{(unsigned)tag, (unsigned)(tag >> 32)}, rws, 0, 0, AUX_SC1);
      STAMP(8);
      TEND;
      POLL_KEEP(pr[0]); POLL_KEEP(pr[1]);
    }
  } else {
    const size_t base = ((size_t)((i >> 2) * 8 + g) * 4096 + (i & 3) * 1024) * 256;
#define CHUNK(c) ((c) * 32768 + ((w + (c)) & 7) * 4096)
    const int voff = lane * 16;
    const __amdgpu_buffer_rsrc_t rk = mk_rsrc((const unsigned char*)a_k + base, 262144), rv = mk_rsrc((const unsigned char*)a_v + base, 262144);
    const __amdgpu_buffer_rsrc_t rws = mk_rsrc((const void*)a_ws, WS_END);
    u32x4 kr[32], vr[32];
    const __amdgpu_buffer_rsrc_t rq = mk_rsrc((const void*)a_wq, 4194304), rx = mk_rsrc((const void*)a_x, 8192);
    const int qs = kq * 1048576 + gq * 1024 + cq * 128 + w * 131072;
    const u32x4 wq0 = __builtin_amdgcn_raw_buffer_load_b128(rq, qoff, qs, AUX_NT);
    const u32x4 wq1 = __builtin_amdgcn_raw_buffer_load_b128(rq, qoff, qs + 65536, AUX_NT);
    const u32x4 xq = __builtin_amdgcn_raw_buffer_load_b128(rx, xoff, w * 1024, 0);
#pragma unroll
    for (int n = 0; n < DINIT; ++n) kr[n] = __builtin_amdgcn_raw_buffer_load_b128(rk, koff + (n & 3) * 1024, CHUNK(n >> 2), AUX_NT);
    __builtin_amdgcn_sched_barrier(0);
    STAMP(8);
    int lag = 0;
#define ISSUE(n) do { \
      __builtin_amdgcn_s_waitcnt(0x0F70 | (DHI - 1)); \
      if ((n) < NBURST && lag < EHI) { __builtin_amdgcn_s_waitcnt(0x0F70 | (DMID - 1)); if ((n) >= NTH && (n) < NEND && lag < ELO) __builtin_amdgcn_s_waitcnt(0x0F70 | (DLO - 1)); } \
      if ((n) < 32) kr[(n) & 31] = __builtin_amdgcn_raw_buffer_load_b128(rk, koff + ((n) & 3) * 1024, CHUNK(((n) & 31) >> 2), AUX_NT); \
      else vr[(n) & 31] = __builtin_amdgcn_raw_buffer_load_b128(rv, voff + ((n) & 3) * 1024, CHUNK(((n) & 31) >> 2), AUX_NT); \
      __builtin_amdgcn_sched_barrier(0); \
      if (((n) & (EVAL - 1)) == 0) lag = (int)((unsigned)__builtin_amdgcn_s_memrealtime() - t0) - (TOFF + ((n) * TAU) / 10); \
      if ((n) == TRIGQ || (n) == TRIGS) *(volatile LAS unsigned*)(L + L_FLAG + 8) = (n); \
      if ((n) == 63 && lane == 0) __hip_atomic_fetch_add((LAS unsigned*)(L + L_FLAG + 12), 1u, __ATOMIC_RELAXED, __HIP_MEMORY_SCOPE_WORKGROUP); \
      __builtin_amdgcn_sched_barrier(0); } while (0)
#pragma unroll
    for (int n = DINIT; n < PARK; ++n) {
      if (n == DINIT) {
        *(LAS u32x4*)(L + img_w + w * 512) = wq0;
        *(LAS u32x4*)(L + img_w + w * 512 + 128) = wq1;
        *(LAS u32x4*)(L + L_X + w * 272 + ln * 16) = xq;
        asm volatile("s_waitcnt lgkmcnt(0)" ::: "memory");
        if (lane == 0) __hip_atomic_fetch_add((LAS unsigned*)(L + L_FLAG + 16), 1u, __ATOMIC_RELAXED, __HIP_MEMORY_SCOPE_WORKGROUP);
        __builtin_amdgcn_sched_barrier(0);
      }
      ISSUE(n);
      if (n == 8) STAMP(10);
      if (n == 14) STAMP(11);
    }
    STAMP(1);
    lds_wait_ge<1>(L + L_FLAG, 1u);
    STAMP(2);
    const u32x4 qf = *(LAS u32x4*)(L + L_Q + (lane & 3) * 256 + (4 * lg + ((lane >> 2) & 3)) * 16);
    float sc[8];
    constexpr int NQ = PARK + 4;
#pragma unroll
    for (int u0 = 0; u0 < 32; u0 += 4) {
      const int t = u0 >> 3;
      f32x4 d[4];
      float y[4];
#pragma unroll
      for (int k = 0; k < 4; ++k) d[k] = mfma(kr[u0 + k], qf, f32x4{0.f, 0.f, 0.f, 0.f});
      __builtin_amdgcn_sched_barrier(0);
#pragma unroll
      for (int k = 0; k < 4; ++k) y[k] = d[k][t] + dppf<ROR12>(d[k][(t + 1) & 3]);
      __builtin_amdgcn_sched_barrier(0);
#pragma unroll
      for (int k = 0; k < 4; ++k) y[k] += dppf<ROR8>(d[k][(t + 2) & 3]);
      __builtin_amdgcn_sched_barrier(0);
#pragma unroll
      for (int k = 0; k < 4; ++k) y[k] += dppf<ROR4>(d[k][(t + 3) & 3]);
      __builtin_amdgcn_sched_barrier(0);
#pragma unroll
      for (int k = 0; k < 4; ++k) {
        const int T = (u0 + k) & 7;
        sc[T] = t == 0 ? y[k] : t == 1 ? bank_set<2>(sc[T], y[k]) : t == 2 ? bank_set<4>(sc[T], y[k]) : bank_set<8>(sc[T], y[k]);
      }
      __builtin_amdgcn_sched_barrier(0);
      if ((u0 & 4) == 0) ISSUE(PARK + (u0 >> 3));
    }
    float m = fmaxf(fmaxf(fmaxf(sc[0], sc[1]), fmaxf(sc[2], sc[3])), fmaxf(fmaxf(sc[4], sc[5]), fmaxf(sc[6], sc[7])));
    m = fmaxf(m, dppf<ROR4>(m)); m = fmaxf(m, dppf<ROR8>(m)); m = x32max(x16max(m));
    m *= C2;
    float ls = 0.f;
#pragma unroll
    for (int T = 0; T < 8; ++T) { sc[T] = __builtin_amdgcn_exp2f(fmaf(sc[T], C2, -m)); ls += sc[T]; }
    ls += dppf<ROR4>(ls); ls += dppf<ROR8>(ls); ls = x32sum(x16sum(ls));
    if (lane < 4) { *(LAS float*)(L + L_ML + lane * 32 + w * 4) = m; *(LAS float*)(L + L_ML + 128 + lane * 32 + w * 4) = ls; }
    asm volatile("s_waitcnt lgkmcnt(0)" ::: "memory");
    if (lane == 0) __hip_atomic_fetch_add((LAS unsigned*)(L + L_FLAG + 4), 1u, __ATOMIC_RELAXED, __HIP_MEMORY_SCOPE_WORKGROUP);
    STAMP(3);
    u32x4 pa;
#pragma unroll
    for (int k = 0; k < 4; ++k) pa[k] = pack_bf16(sc[2 * k], sc[2 * k + 1]);
    f32x4 acc[8];
#pragma unroll
    for (int j = 0; j < 8; ++j) acc[j] = f32x4{0.f, 0.f, 0.f, 0.f};
    __builtin_amdgcn_sched_barrier(0);
#pragma unroll
    for (int t = 0; t < 4; ++t) {
#pragma unroll
      for (int n = NQ; n < 64; ++n) if ((n > 31 + 8 * t + DHI && n <= 39 + 8 * t + DHI) || (t == 0 && n <= 31 + DHI)) ISSUE(n);
      u32x4 a, bb[8];
#pragma unroll
      for (int k = 0; k < 4; ++k) a[k] = t == 0 ? pa[k] : t == 1 ? dppu<SHL4>(pa[k]) : t == 2 ? dppu<SHL8>(pa[k]) : dppu<SHL12>(pa[k]);
#pragma unroll
      for (int k = 0; k < 4; ++k) {
#pragma unroll
        for (int j = 0; j < 8; ++j) bb[j][k] = __builtin_amdgcn_perm(vr[8 * t + 2 * k + 1][j >> 1], vr[8 * t + 2 * k][j >> 1], (j & 1) ? 0x07060302u : 0x05040100u);
        if (t == 3 && k < 3) __builtin_amdgcn_sched_barrier(0);
      }
#pragma unroll
      for (int j = 0; j < 8; ++j) acc[j] = mfma(a, bb[j], acc[j]);
      __builtin_amdgcn_sched_barrier(0);
    }
    if (lane < 16) {
#pragma unroll
      for (int j = 0; j < 8; ++j) *(LAS f32x4*)(L + L_RED + w * 2048 + j * 256 + lane * 16) = acc[j];
    }
    STAMP(4);
    wg_barrier();
    STAMP(5);
    if (w < 2) {
      f32x4 v = *(LAS f32x4*)(L + L_RED + tid * 16) * *(LAS f32x4*)(L + L_AL);
#pragma unroll
      for (int ww = 1; ww < 8; ++ww) v += *(LAS f32x4*)(L + L_RED + ww * 2048 + tid * 16) * *(LAS f32x4*)(L + L_AL + ww * 16);
      const unsigned h0 = min(pack_bf16(v.x, v.y), SENT - 1u), h1 = min(pack_bf16(v.z, v.w), SENT - 1u);
      const u32x4 pk = {h0, h1, dppu<XOR1>(h0), dppu<XOR1>(h1)};
      if (!(lane & 1)) __builtin_amdgcn_raw_buffer_store_b128(pk, rws, WS_P1 + g * 32768 + (tid >> 5) * 8192 + i * 256 + (tid & 31) * 8, 0, AUX_PUB);
    }
    STAMP(6);
    u32x4 pr[2];
    {
      u32x4 pw[2];
      poll_words<1, H1S0, H1PD, H1SG>(mk_srd(a_ws, WS_END), WS_P1 + g * 32768 + rb * 8192 + w * 1024 + lane * 16, pr, pw);
      const u32x4 p = pw[0];
      const float a0 = tsum16(__uint_as_float(p.x << 16), __uint_as_float(p.y << 16)), a1 = tsum16(__uint_as_float(p.x & 0xFFFF0000u), __uint_as_float(p.y & 0xFFFF0000u));
      const float a2 = tsum16(__uint_as_float(p.z << 16), __uint_as_float(p.w << 16)), a3 = tsum16(__uint_as_float(p.z & 0xFFFF0000u), __uint_as_float(p.w & 0xFFFF0000u));
      *(LAS unsigned*)(L + L_OI + w * 272 + ln * 16 + lg * 4) = pack_bf16(tsum32(a0, a2), tsum32(a1, a3));
    }
    STAMP(7);
    wg_barrier();
    if (w < 4) {
      LAS unsigned char* ip = L + L_IMG + w * 4096 + (lg >> 1) * 512 + (lg & 1) * 128 + ln * 8;
      f32x4 d0 = {0.f, 0.f, 0.f, 0.f}, d1 = {0.f, 0.f, 0.f, 0.f};
#pragma unroll
      for (int ks = 0; ks < 4; ks += 2) {
        const u32x2 a0 = tr_read(ip + ks * 1024), a1 = tr_read(ip + ks * 1024 + 256), a2 = tr_read(ip + ks * 1024 + 1024), a3 = tr_read(ip + ks * 1024 + 1280);
        d0 = mfma(u32x4{a0.x, a0.y, a1.x, a1.y}, *(LAS u32x4*)(L + L_OI + (ln & 7) * 272 + ks * 64 + lg * 16), d0);
        d1 = mfma(u32x4{a2.x, a2.y, a3.x, a3.y}, *(LAS u32x4*)(L + L_OI + (ln & 7) * 272 + ks * 64 + 64 + lg * 16), d1);
      }
      if (ln < 8) __builtin_amdgcn_raw_buffer_store_b128(no_sent(d0 + d1), rws, P2L ? WS_P2 + ln * 65536 + (cb * 2 + (w >> 1)) * 4096 + (g * 4 + rb) * 128 + (w & 1) * 64 + lg * 16 : WS_P2 + ln * 65536 + (cb * 4 + w) * 2048 + (g * 4 + rb) * 64 + lg * 16, 0, AUX_PUB);
    }
    STAMP(9);
    POLL_KEEP(pr[0]);
  }
  STAMP_OUT;
}

extern "C" void kernel_launch(void* const* d_in, const int* in_sizes, int n_in, void* d_out, int out_size, void* d_ws, size_t ws_size,
                              hipStream_t stream) {
  gqa_decode_kernel<<<256, NTHR, 0, stream>>>(d_in[1], d_in[2], d_ws, d_in[3], d_in[0], d_in[4], d_out);
}
```

```cpp
#define PROBE 0
#include <hip/hip_runtime.h>
#include <hip/hip_bf16.h>

#define GAS __attribute__((address_space(1)))
#define LAS __attribute__((address_space(3)))
typedef unsigned u32x4 __attribute__((ext_vector_type(4)));
typedef unsigned u32x2 __attribute__((ext_vector_type(2)));
typedef float f32x4 __attribute__((ext_vector_type(4)));
typedef short s16x4 __attribute__((ext_vector_type(4)));
typedef __bf16 bf16x8 __attribute__((ext_vector_type(8)));
typedef __bf16 bf16x2 __attribute__((ext_vector_type(2)));
typedef float f32x2 __attribute__((ext_vector_type(2)));
typedef unsigned long long u64;

constexpr int NTHR = 576;
#define DLO 1
#define DHI 6
#define DMID 3
#define ELO -40
#define EHI 30
#define EVAL 2
#define PD 1
#define PSLP 5
#define H1S0 24
#define H1S1 0
#define H2S0 24
#define H2S1 0
#define NTH 16
#define H1NEAR 0
#define NEARFL "nt"
#define NEND 64
#define NBURST 64
#define H1PD 1
#define H2PD 1
#define H1SG 10
#define H2SG 10
#define AUX_PUB 16
#define AUX_CLR 16
#define POLLFL "sc1"
#define P2L 0
#define AUX_OUT 0
#define DINIT 4
#define TOFF 110
#define TAU 290
#define PARK 36
#define TRIGQ 20
#define TRIGS 54
#define AUX_Q1 0
#define SLPI 6
constexpr int AUX_NT = 2, AUX_SC1 = 16;
constexpr unsigned SENT = 0xFFFFFFFFu;
constexpr unsigned SPIN_CAP = 1u << 16;
constexpr int WS_HI = 1024;
constexpr int WS_QG = 4096;
constexpr int WS_P1 = WS_QG + 524288;
constexpr int WS_ST = WS_P1 + 262144;
constexpr int WS_P2 = WS_ST + 8192;
constexpr int WS_END = WS_P2 + 524288;
constexpr int L_IMG = 0;
constexpr int L_RED = 16384;
constexpr int L_Q = 32768;
constexpr int L_ML = L_Q + 1024;
constexpr int L_OI = L_ML + 256;
constexpr int L_AL = L_OI + 8 * 272;
constexpr int L_FLAG = L_AL + 128;
constexpr int L_X = L_FLAG + 32;
constexpr int L_END = L_X + 8 * 272;

constexpr float C2 = 0.08838834764831845f * 1.4426950408889634f;

#define STAMP(k) do { __builtin_amdgcn_sched_barrier(0); asm volatile("s_memrealtime %0\n\ts_waitcnt lgkmcnt(0)" : "=s"(stamps_[k]) :: "memory"); __builtin_amdgcn_sched_barrier(0); } while (0)
#define STAMP_DECL u64 stamps_[12] = {0, 0, 0, 0, 0, 0, 0, 0, 0, 0, 0, 0}
#define STAMP_OUT do { if (w == 8) { const u64 until_ = stamps_[8] + (stamps_[PROBE] - stamps_[0]) * 4; while (__builtin_amdgcn_s_memrealtime() < until_) __builtin_amdgcn_s_sleep(1); } } while (0)

#define TEND do { } while (0)
__device__ __forceinline__ f32x4 mfma(u32x4 a, u32x4 b, f32x4 c) {
  return __builtin_amdgcn_mfma_f32_16x16x32_bf16(__builtin_bit_cast(bf16x8, a), __builtin_bit_cast(bf16x8, b), c, 0, 0, 0);
}
template <int CTRL> __device__ __forceinline__ float dppf(float x) {
  return __builtin_bit_cast(float, __builtin_amdgcn_mov_dpp(__builtin_bit_cast(int, x), CTRL, 0xf, 0xf, true));
}
template <int CTRL> __device__ __forceinline__ unsigned dppu(unsigned x) {
  return (unsigned)__builtin_amdgcn_mov_dpp((int)x, CTRL, 0xf, 0xf, true);
}
constexpr int ROR4 = 0x124, ROR8 = 0x128, ROR12 = 0x12C, SHL4 = 0x104, SHL8 = 0x108, SHL12 = 0x10C, QID = 0xE4, XOR1 = 0xB1, XOR2 = 0x4E, HMIR = 0x141;
template <int BANKS> __device__ __forceinline__ float bank_set(float old, float y) {
  return __builtin_bit_cast(float, __builtin_amdgcn_update_dpp(__builtin_bit_cast(int, old), __builtin_bit_cast(int, y), QID, 0xf, BANKS, false));
}
__device__ __forceinline__ float x16max(float x) {
  auto s = __builtin_amdgcn_permlane16_swap(__float_as_uint(x), __float_as_uint(x), false, false);
  return fmaxf(__uint_as_float(s[0]), __uint_as_float(s[1]));
}
__device__ __forceinline__ float x32max(float x) {
  auto s = __builtin_amdgcn_permlane32_swap(__float_as_uint(x), __float_as_uint(x), false, false);
  return fmaxf(__uint_as_float(s[0]), __uint_as_float(s[1]));
}
__device__ __forceinline__ float x16sum(float x) {
  auto s = __builtin_amdgcn_permlane16_swap(__float_as_uint(x), __float_as_uint(x), false, false);
  return __uint_as_float(s[0]) + __uint_as_float(s[1]);
}
__device__ __forceinline__ float x32sum(float x) {
  auto s = __builtin_amdgcn_permlane32_swap(__float_as_uint(x), __float_as_uint(x), false, false);
  return __uint_as_float(s[0]) + __uint_as_float(s[1]);
}
__device__ __forceinline__ float tsum16(float a, float b) {
  auto s = __builtin_amdgcn_permlane16_swap(__float_as_uint(a), __float_as_uint(b), false, false);
  return __uint_as_float(s[0]) + __uint_as_float(s[1]);
}
__device__ __forceinline__ float tsum32(float a, float b) {
  auto s = __builtin_amdgcn_permlane32_swap(__float_as_uint(a), __float_as_uint(b), false, false);
  return __uint_as_float(s[0]) + __uint_as_float(s[1]);
}
__device__ __forceinline__ unsigned pack_bf16(float lo, float hi) {
  f32x2 v = {lo, hi};
  return __builtin_bit_cast(unsigned, __builtin_convertvector(v, bf16x2));
}
__device__ __forceinline__ u32x4 no_sent(f32x4 v) {
  u32x4 u = __builtin_bit_cast(u32x4, v);
  u.x = min(u.x, SENT - 1u); u.y = min(u.y, SENT - 1u); u.z = min(u.z, SENT - 1u); u.w = min(u.w, SENT - 1u);
  return u;
}
__device__ __forceinline__ bool has_sent(u32x4 u) { return max(max(u.x, u.y), max(u.z, u.w)) == SENT; }
__device__ __forceinline__ void wg_barrier() {
  asm volatile("s_waitcnt lgkmcnt(0)\n\ts_barrier" ::: "memory");
}
__device__ __forceinline__ __amdgpu_buffer_rsrc_t mk_rsrc(const void* p, int bytes) {
  return __builtin_amdgcn_make_buffer_rsrc((void*)p, 0, bytes, 0x00020000);
}
template <int K> __device__ __forceinline__ void poll_issue(u32x4& r, int off, u64 mask, u32x4 srd) {
  asm volatile("s_mov_b64 exec, %2\n\tbuffer_load_dwordx4 %0, %1, %3, 0 offen offset:%4 " POLLFL "\n\ts_mov_b64 exec, -1"
               : "+v"(r) : "v"(off), "s"(mask), "s"(srd), "i"(K * (P2L ? 2048 : 1024)) : "memory");
}
template <int N, int S0, int PDN, int SG> __device__ __forceinline__ void poll_words(u32x4 srd, int off, u32x4 (&r)[2], u32x4 (&res)[2]) {
  r[0] = u32x4{SENT, SENT, SENT, SENT}; r[1] = r[0];
  if (S0) __builtin_amdgcn_s_sleep(S0);
  poll_issue<0>(r[0], off, ~0ull, srd);
  if (N > 1) poll_issue<1>(r[1], off, ~0ull, srd);
  asm volatile("s_waitcnt vmcnt(0)" : "+v"(r[0]), "+v"(r[1]) :: "memory");
#pragma unroll 1
  for (unsigned spins = 0; spins < SPIN_CAP; ++spins) {
    res[0] = r[0]; res[1] = r[1];
    asm volatile("" : "+v"(res[0]), "+v"(res[1]));
    const u64 m0 = __builtin_amdgcn_ballot_w64(has_sent(res[0]));
    const u64 m1 = N > 1 ? __builtin_amdgcn_ballot_w64(has_sent(res[1])) : 0ull;
    if ((m0 | m1) == 0ull) break;
    poll_issue<0>(r[0], off, m0 | 1ull, srd);
    if (N > 1) poll_issue<1>(r[1], off, m1 | 1ull, srd);
    if (PDN > 1 && spins < PDN - 1) __builtin_amdgcn_s_sleep(SG);
    asm volatile("s_waitcnt vmcnt(%2)" : "+v"(r[0]), "+v"(r[1]) : "i"((PDN - 1) * N) : "memory");
  }
}
#define POLL_KEEP(r) asm volatile("s_waitcnt vmcnt(0)" :: "v"(r) : "memory")
__device__ __forceinline__ u32x4 mk_srd(u64 p, int bytes) { return u32x4{(unsigned)p, (unsigned)(p >> 32) & 0xFFFFu, (unsigned)bytes, 0x00020000u}; }
__device__ __forceinline__ u32x2 tr_read(LAS unsigned char* p) {
  return __builtin_bit_cast(u32x2, __builtin_amdgcn_ds_read_tr16_b64_v4i16((LAS s16x4*)p));
}
__device__ __forceinline__ int img_row(int k) { return (k & ~12) * 32 + ((k >> 2) & 1) * 256 + ((k >> 3) & 1) * 128; }

template <int SLP> __device__ __forceinline__ void lds_wait_ge(LAS unsigned char* p, unsigned want) {
#pragma unroll 1
  for (unsigned spins = 0; spins < (SPIN_CAP << 6); ++spins) {
    if ((unsigned)__builtin_amdgcn_readfirstlane(*(volatile LAS unsigned*)p) >= want) break;
    __builtin_amdgcn_s_sleep(SLP);
  }
  asm volatile("" ::: "memory");
}
typedef __attribute__((address_space(4))) const u64 karg_t;
__device__ __forceinline__ karg_t* kargs() { karg_t* p = (karg_t*)__builtin_amdgcn_kernarg_segment_ptr(); asm volatile("" : "+s"(p)); return p; }

__global__ void __launch_bounds__(NTHR, 3)
gqa_decode_kernel(const void* kc_, const void* vc_, void* ws_, const void* wq_, const void* x_, const void* wo_, void* out_) {
  karg_t* ka = kargs();
  const u64 a_k = ka[0], a_v = ka[1], a_ws = ka[2], a_wq = ka[3], a_x = ka[4];
  const unsigned t0 = (unsigned)__builtin_amdgcn_s_memrealtime();
  __builtin_amdgcn_sched_barrier(0);
  __shared__ __attribute__((aligned(16))) unsigned char smem[L_END];
  LAS unsigned char* L = (LAS unsigned char*)smem;
  const int tid = threadIdx.x, lane = tid & 63, w = __builtin_amdgcn_readfirstlane(tid >> 6);
  const int g = blockIdx.x & 7, i = blockIdx.x >> 3;
  const int lg = lane >> 4, ln = lane & 15;
  const int rb = i >> 3, cb = i & 7;
  const int gq = i & 7, kq = i >> 3, cq = (g + gq) & 7;
  const int img_w = L_IMG + ((lane & 7) >> 1) * 4096 + img_row(lane >> 3) + (lane & 1) * 16;
  STAMP_DECL;
  STAMP(0);
  int koff = ((lane >> 2) & 3) * 256 + lg * 64 + (lane & 3) * 16;
  int qoff = (lane >> 3) * 8192 + (lane & 7) * 16, xoff = kq * 256 + ln * 16;
  asm volatile("" : "+v"(koff), "+v"(qoff), "+v"(xoff));
  if (tid < 8) *(LAS unsigned*)(L + L_FLAG + tid * 4) = 0u;
  wg_barrier();

  if (__builtin_expect(w >= 8, 0)) {
    __builtin_amdgcn_s_setprio(3);
    const __amdgpu_buffer_rsrc_t rws = mk_rsrc((const void*)a_ws, WS_END);
    u32x4 wv[16];
    const u32x2 ep = __builtin_amdgcn_raw_buffer_load_b64(rws, 0, 0, AUX_SC1);
    {
      const int sl = cq * 4 + kq;
      const u32x4 sv = {SENT, SENT, SENT, SENT};
      __builtin_amdgcn_raw_buffer_store_b128(sv, rws, WS_P1 + gq * 32768 + sl * 1024 + lane * 16, 0, AUX_CLR);
      if (lane < 2) __builtin_amdgcn_raw_buffer_store_b128(sv, rws, WS_ST + gq * 1024 + sl * 32 + lane * 16, 0, AUX_SC1);
      const int o2 = P2L ? WS_P2 + lg * 65536 + (cb * 2 + ((lane >> 3) & 1)) * 4096 + (g * 4 + rb) * 128 + (lane & 7) * 16
                         : WS_P2 + lg * 65536 + (cb * 4 + ((lane >> 2) & 3)) * 2048 + (g * 4 + rb) * 64 + (lane & 3) * 16;
      __builtin_amdgcn_raw_buffer_store_b128(sv, rws, o2, 0, AUX_CLR);
      __builtin_amdgcn_raw_buffer_store_b128(sv, rws, o2 + 4 * 65536, 0, AUX_CLR);
    }
    __builtin_amdgcn_sched_barrier(0);
    lds_wait_ge<1>(L + L_FLAG + 16, 8u);
    STAMP(1);
    const u64 tag = (((u64)ep.y << 32) | ep.x) + 1ull;
    {
      LAS unsigned char* ip = L + L_IMG + (lg >> 1) * 512 + (lg & 1) * 128 + ln * 8;
      LAS unsigned char* xp = L + L_X + (ln & 7) * 272 + lg * 16;
      f32x4 d[4];
#pragma unroll
      for (int ct = 0; ct < 4; ++ct) d[ct] = f32x4{0.f, 0.f, 0.f, 0.f};
#pragma unroll
      for (int ks = 0; ks < 4; ++ks) {
        const u32x4 xb = *(LAS u32x4*)(xp + ks * 64);
#pragma unroll
        for (int ct = 0; ct < 4; ++ct) {
          const u32x2 a0 = tr_read(ip + ct * 4096 + ks * 1024), a1 = tr_read(ip + ct * 4096 + ks * 1024 + 256);
          d[ct] = mfma(u32x4{a0.x, a0.y, a1.x, a1.y}, xb, d[ct]);
        }
      }
      const int off = WS_QG + gq * 65536 + (ln & 7) * 8192 + kq * 2048 + cq * 256 + (ln >> 3) * 128 + lg * 16;
      __builtin_amdgcn_raw_buffer_store_b128(__builtin_bit_cast(u32x4, ln < 8 ? d[0] : d[2]), rws, off, 0, AUX_SC1);
      __builtin_amdgcn_raw_buffer_store_b128(__builtin_bit_cast(u32x4, ln < 8 ? d[1] : d[3]), rws, off + 64, 0, AUX_SC1);
      asm volatile("s_waitcnt vmcnt(0)" ::: "memory");
      if (lane == 0) __builtin_amdgcn_raw_buffer_store_b64(u32x2{(unsigned)tag, (unsigned)(tag >> 32)}, rws, WS_HI + gq * 256 + (cq * 4 + kq) * 8, 0, AUX_SC1);
    }
    STAMP(2);
    lds_wait_ge<4>(L + L_FLAG + 8, TRIGQ);
    STAMP(9);
    {
#pragma unroll 1
      for (unsigned spins = 0; spins < SPIN_CAP; ++spins) {
        asm volatile("" ::: "memory");
        const u32x2 hi = __builtin_amdgcn_raw_buffer_load_b64(rws, WS_HI + g * 256 + (lane & 31) * 8, 0, AUX_SC1);
        if (!__any(((hi.x ^ (unsigned)tag) | (hi.y ^ (unsigned)(tag >> 32))) != 0u)) break;
        __builtin_amdgcn_s_sleep(8);
      }
      asm volatile("" ::: "memory");
      const int off = WS_QG + g * 65536 + (i >> 2) * 8192 + lane * 16;
#pragma unroll
      for (int t = 0; t < 8; ++t) wv[t] = __builtin_amdgcn_raw_buffer_load_b128(rws, off + (t & 1) * 1024, (t >> 1) * 2048, AUX_SC1);
#pragma unroll
      for (int hh = 0; hh < 2; ++hh) {
        const f32x4 q4 = (__builtin_bit_cast(f32x4, wv[hh]) + __builtin_bit_cast(f32x4, wv[2 + hh])) + (__builtin_bit_cast(f32x4, wv[4 + hh]) + __builtin_bit_cast(f32x4, wv[6 + hh]));
        *(LAS u32x2*)(L + L_Q + hh * 512 + lane * 8) = u32x2{pack_bf16(q4.x, q4.y), pack_bf16(q4.z, q4.w)};
      }
      asm volatile("s_waitcnt lgkmcnt(0)" ::: "memory");
      *(volatile LAS unsigned*)(L + L_FLAG) = 1u;
    }
    STAMP(3);
    lds_wait_ge<4>(L + L_FLAG + 4, 8u);
    STAMP(4);
    const f32x4 m0 = *(LAS f32x4*)(L + L_ML + (lane & 3) * 32), m1 = *(LAS f32x4*)(L + L_ML + (lane & 3) * 32 + 16);
    {
      const f32x4 l0 = *(LAS f32x4*)(L + L_ML + 128 + (lane & 3) * 32), l1 = *(LAS f32x4*)(L + L_ML + 128 + (lane & 3) * 32 + 16);
      const float M = fmaxf(fmaxf(fmaxf(m0.x, m0.y), fmaxf(m0.z, m0.w)), fmaxf(fmaxf(m1.x, m1.y), fmaxf(m1.z, m1.w)));
      float Ls = 0.f;
#pragma unroll
      for (int k = 0; k < 4; ++k) Ls += l0[k] * __builtin_amdgcn_exp2f(m0[k] - M) + l1[k] * __builtin_amdgcn_exp2f(m1[k] - M);
      if (lane < 4) __builtin_amdgcn_raw_buffer_store_b64(u32x2{min(__float_as_uint(M), SENT - 1u), min(__float_as_uint(Ls), SENT - 1u)}, rws, WS_ST + g * 1024 + i * 32 + lane * 8, 0, AUX_SC1);
    }
    lds_wait_ge<4>(L + L_FLAG + 8, TRIGS);
    STAMP(10);
    {
      u32x2 st;
#pragma unroll 1
      for (unsigned spins = 0; spins < SPIN_CAP; ++spins) {
        asm volatile("" ::: "memory");
        st = __builtin_amdgcn_raw_buffer_load_b64(rws, WS_ST + g * 1024 + (i >> 2) * 128 + ln * 8, 0, AUX_SC1);
        if (!__any(max(st.x, st.y) == SENT)) break;
        __builtin_amdgcn_s_sleep(8);
      }
      const float Ms = __uint_as_float(st.x);
      float Mg = fmaxf(Ms, dppf<ROR4>(Ms)); Mg = fmaxf(Mg, dppf<ROR8>(Mg));
      float Lg = __uint_as_float(st.y) * __builtin_amdgcn_exp2f(Ms - Mg);
      Lg += dppf<ROR4>(Lg); Lg += dppf<ROR8>(Lg);
      const float inv = 1.0f / Lg;
      if (lane < 4) {
#pragma unroll
        for (int k = 0; k < 4; ++k) {
          *(LAS float*)(L + L_AL + k * 16 + lane * 4) = __builtin_amdgcn_exp2f(m0[k] - Mg) * inv;
          *(LAS float*)(L + L_AL + 64 + k * 16 + lane * 4) = __builtin_amdgcn_exp2f(m1[k] - Mg) * inv;
        }
      }
    }
    STAMP(5);
    const __amdgpu_buffer_rsrc_t ro = mk_rsrc((const unsigned char*)kargs()[5] + g * 524288 + cb * 128, 524288 - 1024 + 128);
    const __amdgpu_buffer_rsrc_t rout = mk_rsrc((const void*)kargs()[6], 8192);
    lds_wait_ge<2>(L + L_FLAG + 12, 8u);
    {
      const int kk = lane >> 3;
      const int woff = (kk & 3) * 131072 + (kk >> 2) * 8192 + rb * 2048 + (lane & 7) * 16;
#pragma unroll
      for (int t = 0; t < 16; ++t) wv[t] = __builtin_amdgcn_raw_buffer_load_b128(ro, woff, (t & 7) * 16384 + (t >> 3) * 1024, AUX_NT);
    }
    __builtin_amdgcn_sched_barrier(0);
    __builtin_amdgcn_s_setprio(0);
    wg_barrier();
    __builtin_amdgcn_s_sleep(SLPI);
#pragma unroll
    for (int t = 0; t < 16; ++t) *(LAS u32x4*)(L + img_w + img_row(8 * t)) = wv[t];
    STAMP(11);
    wg_barrier();
    STAMP(6);
    {
      const int off = P2L ? WS_P2 + (i >> 2) * 65536 + (2 * g + ((i >> 1) & 1)) * 4096 + (lane >> 2) * 128 + (i & 1) * 64 + (lane & 3) * 16 : WS_P2 + (i >> 2) * 65536 + (4 * g + (i & 3)) * 2048 + lane * 16;
      u32x4 pr[2], pw[2];
      poll_words<2, H2S0, H2PD, H2SG>(mk_srd(a_ws, WS_END), off, pr, pw);
      STAMP(7);
      const f32x4 o = __builtin_bit_cast(f32x4, pw[0]) + __builtin_bit_cast(f32x4, pw[1]);
      float v = tsum32(tsum16(o.x, o.y), tsum16(o.z, o.w));
      v += dppf<ROR4>(v); v += dppf<ROR8>(v);
      if (ln < 4) __builtin_amdgcn_raw_buffer_store_b16((unsigned short)pack_bf16(v, v), rout, (i >> 2) * 1024 + (4 * g + (i & 3)) * 32 + ln * 8 + lg * 2, 0, AUX_OUT);
      if (blockIdx.x == 0 && lane == 0) __builtin_amdgcn_raw_buffer_store_b64(u32x2{(unsigned)tag, (unsigned)(tag >> 32)}, rws, 0, 0, AUX_SC1);
      STAMP(8);
      TEND;
      POLL_KEEP(pr[0]); POLL_KEEP(pr[1]);
    }
  } else {
    const size_t base = ((size_t)((i >> 2) * 8 + g) * 4096 + (i & 3) * 1024) * 256;
#define CHUNK(c) ((c) * 32768 + ((w + (c)) & 7) * 4096)
    const int voff = lane * 16;
    const __amdgpu_buffer_rsrc_t rk = mk_rsrc((const unsigned char*)a_k + base, 262144), rv = mk_rsrc((const unsigned char*)a_v + base, 262144);
    const __amdgpu_buffer_rsrc_t rws = mk_rsrc((const void*)a_ws, WS_END);
    u32x4 kr[32], vr[32];
    const __amdgpu_buffer_rsrc_t rq = mk_rsrc((const void*)a_wq, 4194304), rx = mk_rsrc((const void*)a_x, 8192);
    const int qs = kq * 1048576 + gq * 1024 + cq * 128 + w * 131072;
    const u32x4 wq0 = __builtin_amdgcn_raw_buffer_load_b128(rq, qoff, qs, AUX_NT);
    const u32x4 wq1 = __builtin_amdgcn_raw_buffer_load_b128(rq, qoff, qs + 65536, AUX_NT);
    const u32x4 xq = __builtin_amdgcn_raw_buffer_load_b128(rx, xoff, w * 1024, 0);
#pragma unroll
    for (int n = 0; n < DINIT; ++n) kr[n] = __builtin_amdgcn_raw_buffer_load_b128(rk, koff + (n & 3) * 1024, CHUNK(n >> 2), AUX_NT);
    __builtin_amdgcn_sched_barrier(0);
    STAMP(8);
    int lag = 0;
#define ISSUE(n) do { \
      __builtin_amdgcn_s_waitcnt(0x0F70 | (DHI - 1)); \
      if ((n) < NBURST && lag < EHI) { __builtin_amdgcn_s_waitcnt(0x0F70 | (DMID - 1)); if ((n) >= NTH && (n) < NEND && lag < ELO) __builtin_amdgcn_s_waitcnt(0x0F70 | (DLO - 1)); } \
      if ((n) < 32) kr[(n) & 31] = __builtin_amdgcn_raw_buffer_load_b128(rk, koff + ((n) & 3) * 1024, CHUNK(((n) & 31) >> 2), AUX_NT); \
      else vr[(n) & 31] = __builtin_amdgcn_raw_buffer_load_b128(rv, voff + ((n) & 3) * 1024, CHUNK(((n) & 31) >> 2), AUX_NT); \
      __builtin_amdgcn_sched_barrier(0); \
      if (((n) & (EVAL - 1)) == 0) lag = (int)((unsigned)__builtin_amdgcn_s_memrealtime() - t0) - (TOFF + ((n) * TAU) / 10); \
      if ((n) == TRIGQ || (n) == TRIGS) *(volatile LAS unsigned*)(L + L_FLAG + 8) = (n); \
      if ((n) == 63 && lane == 0) __hip_atomic_fetch_add((LAS unsigned*)(L + L_FLAG + 12), 1u, __ATOMIC_RELAXED, __HIP_MEMORY_SCOPE_WORKGROUP); \
      __builtin_amdgcn_sched_barrier(0); } while (0)
#pragma unroll
    for (int n = DINIT; n < PARK; ++n) {
      if (n == DINIT) {
        *(LAS u32x4*)(L + img_w + w * 512) = wq0;
        *(LAS u32x4*)(L + img_w + w * 512 + 128) = wq1;
        *(LAS u32x4*)(L + L_X + w * 272 + ln * 16) = xq;
        asm volatile("s_waitcnt lgkmcnt(0)" ::: "memory");
        if (lane == 0) __hip_atomic_fetch_add((LAS unsigned*)(L + L_FLAG + 16), 1u, __ATOMIC_RELAXED, __HIP_MEMORY_SCOPE_WORKGROUP);
        __builtin_amdgcn_sched_barrier(0);
      }
      ISSUE(n);
      if (n == 8) STAMP(10);
      if (n == 14) STAMP(11);
    }
    STAMP(1);
    lds_wait_ge<1>(L + L_FLAG, 1u);
    STAMP(2);
    const u32x4 qf = *(LAS u32x4*)(L + L_Q + (lane & 3) * 256 + (4 * lg + ((lane >> 2) & 3)) * 16);
    float sc[8];
    constexpr int NQ = PARK + 4;
#pragma unroll
    for (int u0 = 0; u0 < 32; u0 += 4) {
      const int t = u0 >> 3;
      f32x4 d[4];
      float y[4];
#pragma unroll
      for (int k = 0; k < 4; ++k) d[k] = mfma(kr[u0 + k], qf, f32x4{0.f, 0.f, 0.f, 0.f});
      __builtin_amdgcn_sched_barrier(0);
#pragma unroll
      for (int k = 0; k < 4; ++k) y[k] = d[k][t] + dppf<ROR12>(d[k][(t + 1) & 3]);
      __builtin_amdgcn_sched_barrier(0);
#pragma unroll
      for (int k = 0; k < 4; ++k) y[k] += dppf<ROR8>(d[k][(t + 2) & 3]);
      __builtin_amdgcn_sched_barrier(0);
#pragma unroll
      for (int k = 0; k < 4; ++k) y[k] += dppf<ROR4>(d[k][(t + 3) & 3]);
      __builtin_amdgcn_sched_barrier(0);
#pragma unroll
      for (int k = 0; k < 4; ++k) {
        const int T = (u0 + k) & 7;
        sc[T] = t == 0 ? y[k] : t == 1 ? bank_set<2>(sc[T], y[k]) : t == 2 ? bank_set<4>(sc[T], y[k]) : bank_set<8>(sc[T], y[k]);
      }
      __builtin_amdgcn_sched_barrier(0);
      if ((u0 & 4) == 0) ISSUE(PARK + (u0 >> 3));
    }
    float m = fmaxf(fmaxf(fmaxf(sc[0], sc[1]), fmaxf(sc[2], sc[3])), fmaxf(fmaxf(sc[4], sc[5]), fmaxf(sc[6], sc[7])));
    m = fmaxf(m, dppf<ROR4>(m)); m = fmaxf(m, dppf<ROR8>(m)); m = x32max(x16max(m));
    m *= C2;
    float ls = 0.f;
#pragma unroll
    for (int T = 0; T < 8; ++T) { sc[T] = __builtin_amdgcn_exp2f(fmaf(sc[T], C2, -m)); ls += sc[T]; }
    ls += dppf<ROR4>(ls); ls += dppf<ROR8>(ls); ls = x32sum(x16sum(ls));
    if (lane < 4) { *(LAS float*)(L + L_ML + lane * 32 + w * 4) = m; *(LAS float*)(L + L_ML + 128 + lane * 32 + w * 4) = ls; }
    asm volatile("s_waitcnt lgkmcnt(0)" ::: "memory");
    if (lane == 0) __hip_atomic_fetch_add((LAS unsigned*)(L + L_FLAG + 4), 1u, __ATOMIC_RELAXED, __HIP_MEMORY_SCOPE_WORKGROUP);
    STAMP(3);
    u32x4 pa;
#pragma unroll
    for (int k = 0; k < 4; ++k) pa[k] = pack_bf16(sc[2 * k], sc[2 * k + 1]);
    f32x4 acc[8];
#pragma unroll
    for (int j = 0; j < 8; ++j) acc[j] = f32x4{0.f, 0.f, 0.f, 0.f};
    __builtin_amdgcn_sched_barrier(0);
#pragma unroll
    for (int t = 0; t < 4; ++t) {
#pragma unroll
      for (int n = NQ; n < 64; ++n) if ((n > 31 + 8 * t + DHI && n <= 39 + 8 * t + DHI) || (t == 0 && n <= 31 + DHI)) ISSUE(n);
      u32x4 a, bb[8];
#pragma unroll
      for (int k = 0; k < 4; ++k) a[k] = t == 0 ? pa[k] : t == 1 ? dppu<SHL4>(pa[k]) : t == 2 ? dppu<SHL8>(pa[k]) : dppu<SHL12>(pa[k]);
#pragma unroll
      for (int k = 0; k < 4; ++k) {
#pragma unroll
        for (int j = 0; j < 8; ++j) bb[j][k] = __builtin_amdgcn_perm(vr[8 * t + 2 * k + 1][j >> 1], vr[8 * t + 2 * k][j >> 1], (j & 1) ? 0x07060302u : 0x05040100u);
        if (t == 3 && k < 3) __builtin_amdgcn_sched_barrier(0);
      }
#pragma unroll
      for (int j = 0; j < 8; ++j) acc[j] = mfma(a, bb[j], acc[j]);
      __builtin_amdgcn_sched_barrier(0);
    }
    if (lane < 16) {
#pragma unroll
      for (int j = 0; j < 8; ++j) *(LAS f32x4*)(L + L_RED + w * 2048 + j * 256 + lane * 16) = acc[j];
    }
    STAMP(4);
    wg_barrier();
    STAMP(5);
    if (w < 2) {
      f32x4 v = *(LAS f32x4*)(L + L_RED + tid * 16) * *(LAS f32x4*)(L + L_AL);
#pragma unroll
      for (int ww = 1; ww < 8; ++ww) v += *(LAS f32x4*)(L + L_RED + ww * 2048 + tid * 16) * *(LAS f32x4*)(L + L_AL + ww * 16);
      const unsigned h0 = min(pack_bf16(v.x, v.y), SENT - 1u), h1 = min(pack_bf16(v.z, v.w), SENT - 1u);
      const u32x4 pk = {h0, h1, dppu<XOR1>(h0), dppu<XOR1>(h1)};
      if (!(lane & 1)) __builtin_amdgcn_raw_buffer_store_b128(pk, rws, WS_P1 + g * 32768 + (tid >> 5) * 8192 + i * 256 + (tid & 31) * 8, 0, AUX_PUB);
    }
    STAMP(6);
    u32x4 pr[2];
    {
      u32x4 pw[2];
      poll_words<1, H1S0, H1PD, H1SG>(mk_srd(a_ws, WS_END), WS_P1 + g * 32768 + rb * 8192 + w * 1024 + lane * 16, pr, pw);
      const u32x4 p = pw[0];
      const float a0 = tsum16(__uint_as_float(p.x << 16), __uint_as_float(p.y << 16)), a1 = tsum16(__uint_as_float(p.x & 0xFFFF0000u), __uint_as_float(p.y & 0xFFFF0000u));
      const float a2 = tsum16(__uint_as_float(p.z << 16), __uint_as_float(p.w << 16)), a3 = tsum16(__uint_as_float(p.z & 0xFFFF0000u), __uint_as_float(p.w & 0xFFFF0000u));
      *(LAS unsigned*)(L + L_OI + w * 272 + ln * 16 + lg * 4) = pack_bf16(tsum32(a0, a2), tsum32(a1, a3));
    }
    STAMP(7);
    wg_barrier();
    if (w < 4) {
      LAS unsigned char* ip = L + L_IMG + w * 4096 + (lg >> 1) * 512 + (lg & 1) * 128 + ln * 8;
      f32x4 d0 = {0.f, 0.f, 0.f, 0.f}, d1 = {0.f, 0.f, 0.f, 0.f};
#pragma unroll
      for (int ks = 0; ks < 4; ks += 2) {
        const u32x2 a0 = tr_read(ip + ks * 1024), a1 = tr_read(ip + ks * 1024 + 256), a2 = tr_read(ip + ks * 1024 + 1024), a3 = tr_read(ip + ks * 1024 + 1280);
        d0 = mfma(u32x4{a0.x, a0.y, a1.x, a1.y}, *(LAS u32x4*)(L + L_OI + (ln & 7) * 272 + ks * 64 + lg * 16), d0);
        d1 = mfma(u32x4{a2.x, a2.y, a3.x, a3.y}, *(LAS u32x4*)(L + L_OI + (ln & 7) * 272 + ks * 64 + 64 + lg * 16), d1);
      }
      if (ln < 8) __builtin_amdgcn_raw_buffer_store_b128(no_sent(d0 + d1), rws, P2L ? WS_P2 + ln * 65536 + (cb * 2 + (w >> 1)) * 4096 + (g * 4 + rb) * 128 + (w & 1) * 64 + lg * 16 : WS_P2 + ln * 65536 + (cb * 4 + w) * 2048 + (g * 4 + rb) * 64 + lg * 16, 0, AUX_PUB);
    }
    STAMP(9);
    POLL_KEEP(pr[0]);
  }
  STAMP_OUT;
}

extern "C" void kernel_launch(void* const* d_in, const int* in_sizes, int n_in, void* d_out, int out_size, void* d_ws, size_t ws_size,
                              hipStream_t stream) {
  gqa_decode_kernel<<<256, NTHR, 0, stream>>>(d_in[1], d_in[2], d_ws, d_in[3], d_in[0], d_in[4], d_out);
}
```

```cpp
#define SPILL 4
#include <hip/hip_runtime.h>
#include <hip/hip_bf16.h>

#define GAS __attribute__((address_space(1)))
#define LAS __attribute__((address_space(3)))
typedef unsigned u32x4 __attribute__((ext_vector_type(4)));
typedef unsigned u32x2 __attribute__((ext_vector_type(2)));
typedef float f32x4 __attribute__((ext_vector_type(4)));
typedef short s16x4 __attribute__((ext_vector_type(4)));
typedef __bf16 bf16x8 __attribute__((ext_vector_type(8)));
typedef __bf16 bf16x2 __attribute__((ext_vector_type(2)));
typedef float f32x2 __attribute__((ext_vector_type(2)));
typedef unsigned long long u64;

constexpr int NTHR = 576;
#define DLO 1
#define DHI 6
#define DMID 3
#define ELO -40
#define EHI 30
#define EVAL 2
#define PD 1
#define PSLP 5
#define H1S0 24
#define H1S1 0
#define H2S0 24
#define H2S1 0
#define NTH 16
#define H1NEAR 0
#define NEARFL "nt"
#define NEND 64
#define NBURST 64
#define H1PD 1
#define H2PD 1
#define H1SG 10
#define H2SG 10
#define AUX_PUB 16
#define AUX_CLR 16
#define POLLFL "sc1"
#define P2L 0
#define AUX_OUT 0
#define CREF 1
#define CADJ 15
#define DINIT 4
#define TOFF 110
#define TAU 290
#define PARK (36 + SPILL)
#define TRIGQ 20
#define TRIGS 54
#define AUX_Q1 0
#define SLPI 6
constexpr int AUX_NT = 2, AUX_SC1 = 16;
constexpr unsigned SENT = 0xFFFFFFFFu;
constexpr unsigned SPIN_CAP = 1u << 16;
constexpr int WS_HI = 1024;
constexpr int WS_T0 = 3072;
constexpr int WS_QG = 4096;
constexpr int WS_P1 = WS_QG + 524288;
constexpr int WS_ST = WS_P1 + 262144;
constexpr int WS_P2 = WS_ST + 8192;
constexpr int WS_END = WS_P2 + 524288;
constexpr int L_IMG = 0;
constexpr int L_RED = 16384;
constexpr int L_Q = 32768;
constexpr int L_ML = L_Q + 1024;
constexpr int L_OI = L_ML + 256;
constexpr int L_AL = L_OI + 8 * 272;
constexpr int L_FLAG = L_AL + 128;
constexpr int L_X = L_FLAG + 48;
constexpr int L_SP = L_X + 8 * 272;
constexpr int L_END = L_SP + 8 * SPILL * 1024;

constexpr float C2 = 0.08838834764831845f * 1.4426950408889634f;

#define STAMP(k) do { } while (0)
#define STAMP_DECL do { } while (0)
#define STAMP_OUT do { } while (0)

#define TEND do { } while (0)
__device__ __forceinline__ f32x4 mfma(u32x4 a, u32x4 b, f32x4 c) {
  return __builtin_amdgcn_mfma_f32_16x16x32_bf16(__builtin_bit_cast(bf16x8, a), __builtin_bit_cast(bf16x8, b), c, 0, 0, 0);
}
template <int CTRL> __device__ __forceinline__ float dppf(float x) {
  return __builtin_bit_cast(float, __builtin_amdgcn_mov_dpp(__builtin_bit_cast(int, x), CTRL, 0xf, 0xf, true));
}
template <int CTRL> __device__ __forceinline__ unsigned dppu(unsigned x) {
  return (unsigned)__builtin_amdgcn_mov_dpp((int)x, CTRL, 0xf, 0xf, true);
}
constexpr int ROR4 = 0x124, ROR8 = 0x128, ROR12 = 0x12C, SHL4 = 0x104, SHL8 = 0x108, SHL12 = 0x10C, QID = 0xE4, XOR1 = 0xB1, XOR2 = 0x4E, HMIR = 0x141;
template <int BANKS> __device__ __forceinline__ float bank_set(float old, float y) {
  return __builtin_bit_cast(float, __builtin_amdgcn_update_dpp(__builtin_bit_cast(int, old), __builtin_bit_cast(int, y), QID, 0xf, BANKS, false));
}
__device__ __forceinline__ float x16max(float x) {
  auto s = __builtin_amdgcn_permlane16_swap(__float_as_uint(x), __float_as_uint(x), false, false);
  return fmaxf(__uint_as_float(s[0]), __uint_as_float(s[1]));
}
__device__ __forceinline__ float x32max(float x) {
  auto s = __builtin_amdgcn_permlane32_swap(__float_as_uint(x), __float_as_uint(x), false, false);
  return fmaxf(__uint_as_float(s[0]), __uint_as_float(s[1]));
}
__device__ __forceinline__ float x16sum(float x) {
  auto s = __builtin_amdgcn_permlane16_swap(__float_as_uint(x), __float_as_uint(x), false, false);
  return __uint_as_float(s[0]) + __uint_as_float(s[1]);
}
__device__ __forceinline__ float x32sum(float x) {
  auto s = __builtin_amdgcn_permlane32_swap(__float_as_uint(x), __float_as_uint(x), false, false);
  return __uint_as_float(s[0]) + __uint_as_float(s[1]);
}
__device__ __forceinline__ float tsum16(float a, float b) {
  auto s = __builtin_amdgcn_permlane16_swap(__float_as_uint(a), __float_as_uint(b), false, false);
  return __uint_as_float(s[0]) + __uint_as_float(s[1]);
}
__device__ __forceinline__ float tsum32(float a, float b) {
  auto s = __builtin_amdgcn_permlane32_swap(__float_as_uint(a), __float_as_uint(b), false, false);
  return __uint_as_float(s[0]) + __uint_as_float(s[1]);
}
__device__ __forceinline__ unsigned pack_bf16(float lo, float hi) {
  f32x2 v = {lo, hi};
  return __builtin_bit_cast(unsigned, __builtin_convertvector(v, bf16x2));
}
__device__ __forceinline__ u32x4 no_sent(f32x4 v) {
  u32x4 u = __builtin_bit_cast(u32x4, v);
  u.x = min(u.x, SENT - 1u); u.y = min(u.y, SENT - 1u); u.z = min(u.z, SENT - 1u); u.w = min(u.w, SENT - 1u);
  return u;
}
__device__ __forceinline__ bool has_sent(u32x4 u) { return max(max(u.x, u.y), max(u.z, u.w)) == SENT; }
__device__ __forceinline__ void wg_barrier() {
  asm volatile("s_waitcnt lgkmcnt(0)\n\ts_barrier" ::: "memory");
}
__device__ __forceinline__ __amdgpu_buffer_rsrc_t mk_rsrc(const void* p, int bytes) {
  return __builtin_amdgcn_make_buffer_rsrc((void*)p, 0, bytes, 0x00020000);
}
template <int K> __device__ __forceinline__ void poll_issue(u32x4& r, int off, u64 mask, u32x4 srd) {
  asm volatile("s_mov_b64 exec, %2\n\tbuffer_load_dwordx4 %0, %1, %3, 0 offen offset:%4 " POLLFL "\n\ts_mov_b64 exec, -1"
               : "+v"(r) : "v"(off), "s"(mask), "s"(srd), "i"(K * (P2L ? 2048 : 1024)) : "memory");
}
template <int N, int S0, int PDN, int SG> __device__ __forceinline__ void poll_words(u32x4 srd, int off, u32x4 (&r)[2], u32x4 (&res)[2]) {
  r[0] = u32x4{SENT, SENT, SENT, SENT}; r[1] = r[0];
  if (S0) __builtin_amdgcn_s_sleep(S0);
  poll_issue<0>(r[0], off, ~0ull, srd);
  if (N > 1) poll_issue<1>(r[1], off, ~0ull, srd);
  asm volatile("s_waitcnt vmcnt(0)" : "+v"(r[0]), "+v"(r[1]) :: "memory");
#pragma unroll 1
  for (unsigned spins = 0; spins < SPIN_CAP; ++spins) {
    res[0] = r[0]; res[1] = r[1];
    asm volatile("" : "+v"(res[0]), "+v"(res[1]));
    const u64 m0 = __builtin_amdgcn_ballot_w64(has_sent(res[0]));
    const u64 m1 = N > 1 ? __builtin_amdgcn_ballot_w64(has_sent(res[1])) : 0ull;
    if ((m0 | m1) == 0ull) break;
    poll_issue<0>(r[0], off, m0 | 1ull, srd);
    if (N > 1) poll_issue<1>(r[1], off, m1 | 1ull, srd);
    if (PDN > 1 && spins < PDN - 1) __builtin_amdgcn_s_sleep(SG);
    asm volatile("s_waitcnt vmcnt(%2)" : "+v"(r[0]), "+v"(r[1]) : "i"((PDN - 1) * N) : "memory");
  }
}
#define POLL_KEEP(r) asm volatile("" :: "v"(r) : "memory")
__device__ __forceinline__ u32x4 mk_srd(u64 p, int bytes) { return u32x4{(unsigned)p, (unsigned)(p >> 32) & 0xFFFFu, (unsigned)bytes, 0x00020000u}; }
__device__ __forceinline__ u32x2 tr_read(LAS unsigned char* p) {
  return __builtin_bit_cast(u32x2, __builtin_amdgcn_ds_read_tr16_b64_v4i16((LAS s16x4*)p));
}
__device__ __forceinline__ int img_row(int k) { return (k & ~12) * 32 + ((k >> 2) & 1) * 256 + ((k >> 3) & 1) * 128; }

template <int SLP> __device__ __forceinline__ void lds_wait_ge(LAS unsigned char* p, unsigned want) {
#pragma unroll 1
  for (unsigned spins = 0; spins < (SPIN_CAP << 6); ++spins) {
    if ((unsigned)__builtin_amdgcn_readfirstlane(*(volatile LAS unsigned*)p) >= want) break;
    __builtin_amdgcn_s_sleep(SLP);
  }
  asm volatile("" ::: "memory");
}
typedef __attribute__((address_space(4))) const u64 karg_t;
__device__ __forceinline__ karg_t* kargs() { karg_t* p = (karg_t*)__builtin_amdgcn_kernarg_segment_ptr(); asm volatile("" : "+s"(p)); return p; }

__global__ void __launch_bounds__(NTHR, 3)
gqa_decode_kernel(const void* kc_, const void* vc_, void* ws_, const void* wq_, const void* x_, const void* wo_, void* out_) {
  karg_t* ka = kargs();
  const u64 a_k = ka[0], a_v = ka[1], a_ws = ka[2], a_wq = ka[3], a_x = ka[4];
  const unsigned t0 = (unsigned)__builtin_amdgcn_s_memrealtime();
  __builtin_amdgcn_sched_barrier(0);
  __shared__ __attribute__((aligned(16))) unsigned char smem[L_END];
  LAS unsigned char* L = (LAS unsigned char*)smem;
  const int tid = threadIdx.x, lane = tid & 63, w = __builtin_amdgcn_readfirstlane(tid >> 6);
  const int g = blockIdx.x & 7, i = blockIdx.x >> 3;
  const int lg = lane >> 4, ln = lane & 15;
  const int rb = i >> 3, cb = i & 7;
  const int gq = i & 7, kq = i >> 3, cq = (g + gq) & 7;
  const int img_w = L_IMG + ((lane & 7) >> 1) * 4096 + img_row(lane >> 3) + (lane & 1) * 16;
  STAMP_DECL;
  STAMP(0);
  int koff = ((lane >> 2) & 3) * 256 + lg * 64 + (lane & 3) * 16;
  int qoff = (lane >> 3) * 8192 + (lane & 7) * 16, xoff = kq * 256 + ln * 16;
  asm volatile("" : "+v"(koff), "+v"(qoff), "+v"(xoff));
  if (tid < 9) *(LAS unsigned*)(L + L_FLAG + tid * 4) = tid == 8 ? t0 : 0u;
  wg_barrier();

  if (__builtin_expect(w >= 8, 0)) {
    __builtin_amdgcn_s_setprio(3);
    const __amdgpu_buffer_rsrc_t rws = mk_rsrc((const void*)a_ws, WS_END);
    u32x4 wv[16];
    const u32x2 ep = __builtin_amdgcn_raw_buffer_load_b64(rws, 0, 0, AUX_SC1);
    {
      const int sl = cq * 4 + kq;
      const u32x4 sv = {SENT, SENT, SENT, SENT};
      __builtin_amdgcn_raw_buffer_store_b128(sv, rws, WS_P1 + gq * 32768 + sl * 1024 + lane * 16, 0, AUX_CLR);
      if (lane < 2) __builtin_amdgcn_raw_buffer_store_b128(sv, rws, WS_ST + gq * 1024 + sl * 32 + lane * 16, 0, AUX_SC1);
      const int o2 = P2L ? WS_P2 + lg * 65536 + (cb * 2 + ((lane >> 3) & 1)) * 4096 + (g * 4 + rb) * 128 + (lane & 7) * 16
                         : WS_P2 + lg * 65536 + (cb * 4 + ((lane >> 2) & 3)) * 2048 + (g * 4 + rb) * 64 + (lane & 3) * 16;
      __builtin_amdgcn_raw_buffer_store_b128(sv, rws, o2, 0, AUX_CLR);
      __builtin_amdgcn_raw_buffer_store_b128(sv, rws, o2 + 4 * 65536, 0, AUX_CLR);
    }
    __builtin_amdgcn_sched_barrier(0);
    lds_wait_ge<1>(L + L_FLAG + 16, 8u);
    STAMP(1);
    const u64 tag = (((u64)ep.y << 32) | ep.x) + 1ull;
    {
      LAS unsigned char* ip = L + L_IMG + (lg >> 1) * 512 + (lg & 1) * 128 + ln * 8;
      LAS unsigned char* xp = L + L_X + (ln & 7) * 272 + lg * 16;
      f32x4 d[4];
#pragma unroll
      for (int ct = 0; ct < 4; ++ct) d[ct] = f32x4{0.f, 0.f, 0.f, 0.f};
#pragma unroll
      for (int ks = 0; ks < 4; ++ks) {
        const u32x4 xb = *(LAS u32x4*)(xp + ks * 64);
#pragma unroll
        for (int ct = 0; ct < 4; ++ct) {
          const u32x2 a0 = tr_read(ip + ct * 4096 + ks * 1024), a1 = tr_read(ip + ct * 4096 + ks * 1024 + 256);
          d[ct] = mfma(u32x4{a0.x, a0.y, a1.x, a1.y}, xb, d[ct]);
        }
      }
      const int off = WS_QG + gq * 65536 + (ln & 7) * 8192 + kq * 2048 + cq * 256 + (ln >> 3) * 128 + lg * 16;
      __builtin_amdgcn_raw_buffer_store_b128(__builtin_bit_cast(u32x4, ln < 8 ? d[0] : d[2]), rws, off, 0, AUX_SC1);
      __builtin_amdgcn_raw_buffer_store_b128(__builtin_bit_cast(u32x4, ln < 8 ? d[1] : d[3]), rws, off + 64, 0, AUX_SC1);
      if (lane == 0) __builtin_amdgcn_raw_buffer_store_b32(t0, rws, WS_T0 + gq * 128 + (cq * 4 + kq) * 4, 0, AUX_SC1);
      asm volatile("s_waitcnt vmcnt(0)" ::: "memory");
      if (lane == 0) __builtin_amdgcn_raw_buffer_store_b64(u32x2{(unsigned)tag, (unsigned)(tag >> 32)}, rws, WS_HI + gq * 256 + (cq * 4 + kq) * 8, 0, AUX_SC1);
    }
    STAMP(2);
    lds_wait_ge<4>(L + L_FLAG + 8, TRIGQ);
    STAMP(9);
    {
#pragma unroll 1
      for (unsigned spins = 0; spins < SPIN_CAP; ++spins) {
        asm volatile("" ::: "memory");
        const u32x2 hi = __builtin_amdgcn_raw_buffer_load_b64(rws, WS_HI + g * 256 + (lane & 31) * 8, 0, AUX_SC1);
        if (!__any(((hi.x ^ (unsigned)tag) | (hi.y ^ (unsigned)(tag >> 32))) != 0u)) break;
        __builtin_amdgcn_s_sleep(8);
      }
      asm volatile("" ::: "memory");
      const int off = WS_QG + g * 65536 + (i >> 2) * 8192 + lane * 16;
#pragma unroll
      for (int t = 0; t < 8; ++t) wv[t] = __builtin_amdgcn_raw_buffer_load_b128(rws, off + (t & 1) * 1024, (t >> 1) * 2048, AUX_SC1);
      {
        int dt = (int)(__builtin_amdgcn_raw_buffer_load_b32(rws, WS_T0 + g * 128 + (lane & 31) * 4, 0, AUX_SC1) - t0);
        dt = min(dt, (int)dppu<XOR1>(dt)); dt = min(dt, (int)dppu<XOR2>(dt)); dt = min(dt, (int)dppu<ROR4>(dt)); dt = min(dt, (int)dppu<ROR8>(dt));
        dt = min(__builtin_amdgcn_readlane(dt, 0), __builtin_amdgcn_readlane(dt, 16));
        if (lane == 0) *(volatile LAS unsigned*)(L + L_FLAG + 32) = t0 + (unsigned)(min(dt + CADJ, 0));
      }
#pragma unroll
      for (int hh = 0; hh < 2; ++hh) {
        const f32x4 q4 = (__builtin_bit_cast(f32x4, wv[hh]) + __builtin_bit_cast(f32x4, wv[2 + hh])) + (__builtin_bit_cast(f32x4, wv[4 + hh]) + __builtin_bit_cast(f32x4, wv[6 + hh]));
        *(LAS u32x2*)(L + L_Q + hh * 512 + lane * 8) = u32x2{pack_bf16(q4.x, q4.y), pack_bf16(q4.z, q4.w)};
      }
      asm volatile("s_waitcnt lgkmcnt(0)" ::: "memory");
      *(volatile LAS unsigned*)(L + L_FLAG) = 1u;
    }
    STAMP(3);
    lds_wait_ge<4>(L + L_FLAG + 4, 8u);
    STAMP(4);
    const f32x4 m0 = *(LAS f32x4*)(L + L_ML + (lane & 3) * 32), m1 = *(LAS f32x4*)(L + L_ML + (lane & 3) * 32 + 16);
    {
      const f32x4 l0 = *(LAS f32x4*)(L + L_ML + 128 + (lane & 3) * 32), l1 = *(LAS f32x4*)(L + L_ML + 128 + (lane & 3) * 32 + 16);
      const float M = fmaxf(fmaxf(fmaxf(m0.x, m0.y), fmaxf(m0.z, m0.w)), fmaxf(fmaxf(m1.x, m1.y), fmaxf(m1.z, m1.w)));
      float Ls = 0.f;
#pragma unroll
      for (int k = 0; k < 4; ++k) Ls += l0[k] * __builtin_amdgcn_exp2f(m0[k] - M) + l1[k] * __builtin_amdgcn_exp2f(m1[k] - M);
      if (lane < 4) __builtin_amdgcn_raw_buffer_store_b64(u32x2{min(__float_as_uint(M), SENT - 1u), min(__float_as_uint(Ls), SENT - 1u)}, rws, WS_ST + g * 1024 + i * 32 + lane * 8, 0, AUX_SC1);
    }
    lds_wait_ge<4>(L + L_FLAG + 8, TRIGS);
    STAMP(10);
    {
      u32x2 st;
#pragma unroll 1
      for (unsigned spins = 0; spins < SPIN_CAP; ++spins) {
        asm volatile("" ::: "memory");
        st = __builtin_amdgcn_raw_buffer_load_b64(rws, WS_ST + g * 1024 + (i >> 2) * 128 + ln * 8, 0, AUX_SC1);
        if (!__any(max(st.x, st.y) == SENT)) break;
        __builtin_amdgcn_s_sleep(8);
      }
      const float Ms = __uint_as_float(st.x);
      float Mg = fmaxf(Ms, dppf<ROR4>(Ms)); Mg = fmaxf(Mg, dppf<ROR8>(Mg));
      float Lg = __uint_as_float(st.y) * __builtin_amdgcn_exp2f(Ms - Mg);
      Lg += dppf<ROR4>(Lg); Lg += dppf<ROR8>(Lg);
      const float inv = 1.0f / Lg;
      if (lane < 4) {
#pragma unroll
        for (int k = 0; k < 4; ++k) {
          *(LAS float*)(L + L_AL + k * 16 + lane * 4) = __builtin_amdgcn_exp2f(m0[k] - Mg) * inv;
          *(LAS float*)(L + L_AL + 64 + k * 16 + lane * 4) = __builtin_amdgcn_exp2f(m1[k] - Mg) * inv;
        }
      }
    }
    STAMP(5);
    const __amdgpu_buffer_rsrc_t ro = mk_rsrc((const unsigned char*)kargs()[5] + g * 524288 + cb * 128, 524288 - 1024 + 128);
    const __amdgpu_buffer_rsrc_t rout = mk_rsrc((const void*)kargs()[6], 8192);
    lds_wait_ge<2>(L + L_FLAG + 12, 8u);
    {
      const int kk = lane >> 3;
      const int woff = (kk & 3) * 131072 + (kk >> 2) * 8192 + rb * 2048 + (lane & 7) * 16;
#pragma unroll
      for (int t = 0; t < 16; ++t) wv[t] = __builtin_amdgcn_raw_buffer_load_b128(ro, woff, (t & 7) * 16384 + (t >> 3) * 1024, AUX_NT);
    }
    __builtin_amdgcn_sched_barrier(0);
    __builtin_amdgcn_s_setprio(0);
    wg_barrier();
    __builtin_amdgcn_s_sleep(SLPI);
#pragma unroll
    for (int t = 0; t < 16; ++t) *(LAS u32x4*)(L + img_w + img_row(8 * t)) = wv[t];
    STAMP(11);
    wg_barrier();
    STAMP(6);
    {
      const int off = P2L ? WS_P2 + (i >> 2) * 65536 + (2 * g + ((i >> 1) & 1)) * 4096 + (lane >> 2) * 128 + (i & 1) * 64 + (lane & 3) * 16 : WS_P2 + (i >> 2) * 65536 + (4 * g + (i & 3)) * 2048 + lane * 16;
      u32x4 pr[2], pw[2];
      poll_words<2, H2S0, H2PD, H2SG>(mk_srd(a_ws, WS_END), off, pr, pw);
      STAMP(7);
      const f32x4 o = __builtin_bit_cast(f32x4, pw[0]) + __builtin_bit_cast(f32x4, pw[1]);
      float v = tsum32(tsum16(o.x, o.y), tsum16(o.z, o.w));
      v += dppf<ROR4>(v); v += dppf<ROR8>(v);
      if (ln < 4) __builtin_amdgcn_raw_buffer_store_b16((unsigned short)pack_bf16(v, v), rout, (i >> 2) * 1024 + (4 * g + (i & 3)) * 32 + ln * 8 + lg * 2, 0, AUX_OUT);
      if (blockIdx.x == 0 && lane == 0) __builtin_amdgcn_raw_buffer_store_b64(u32x2{(unsigned)tag, (unsigned)(tag >> 32)}, rws, 0, 0, AUX_SC1);
      STAMP(8);
      TEND;
      POLL_KEEP(pr[0]); POLL_KEEP(pr[1]);
    }
  } else {
    const size_t base = ((size_t)((i >> 2) * 8 + g) * 4096 + (i & 3) * 1024) * 256;
#define CHUNK(c) ((c) * 32768 + ((w + (c)) & 7) * 4096)
    const int voff = lane * 16;
    const __amdgpu_buffer_rsrc_t rk = mk_rsrc((const unsigned char*)a_k + base, 262144), rv = mk_rsrc((const unsigned char*)a_v + base, 262144);
    const __amdgpu_buffer_rsrc_t rws = mk_rsrc((const void*)a_ws, WS_END);
    u32x4 kr[32], vr[32];
    const __amdgpu_buffer_rsrc_t rq = mk_rsrc((const void*)a_wq, 4194304), rx = mk_rsrc((const void*)a_x, 8192);
    const int qs = kq * 1048576 + gq * 1024 + cq * 128 + w * 131072;
    const u32x4 wq0 = __builtin_amdgcn_raw_buffer_load_b128(rq, qoff, qs, AUX_NT);
    const u32x4 wq1 = __builtin_amdgcn_raw_buffer_load_b128(rq, qoff, qs + 65536, AUX_NT);
    const u32x4 xq = __builtin_amdgcn_raw_buffer_load_b128(rx, xoff, w * 1024, 0);
#pragma unroll
    for (int n = 0; n < DINIT; ++n) kr[n] = __builtin_amdgcn_raw_buffer_load_b128(rk, koff + (n & 3) * 1024, CHUNK(n >> 2), AUX_NT);
    __builtin_amdgcn_sched_barrier(0);
    STAMP(8);
    unsigned tref = t0;
    int lag = 0;
#define ISSUE(n) do { \
      __builtin_amdgcn_s_waitcnt(0x0F70 | (DHI - 1)); \
      if ((n) < NBURST && lag < EHI) { __builtin_amdgcn_s_waitcnt(0x0F70 | (DMID - 1)); if ((n) >= NTH && (n) < NEND && lag < ELO) __builtin_amdgcn_s_waitcnt(0x0F70 | (DLO - 1)); } \
      if ((n) < 32) kr[(n) & 31] = __builtin_amdgcn_raw_buffer_load_b128(rk, koff + ((n) & 3) * 1024, CHUNK(((n) & 31) >> 2), AUX_NT); \
      else vr[(n) & 31] = __builtin_amdgcn_raw_buffer_load_b128(rv, voff + ((n) & 3) * 1024, CHUNK(((n) & 31) >> 2), AUX_NT); \
      __builtin_amdgcn_sched_barrier(0); \
      if (((n) & (EVAL - 1)) == 0) { \
        if (CREF && (n) >= 24 && (n) <= PARK + 4) tref = __builtin_amdgcn_readfirstlane(*(volatile LAS unsigned*)(L + L_FLAG + 32)); \
        lag = (int)((unsigned)__builtin_amdgcn_s_memrealtime() - tref) - (TOFF + ((n) * TAU) / 10); } \
      if ((n) == TRIGQ || (n) == TRIGS) *(volatile LAS unsigned*)(L + L_FLAG + 8) = (n); \
      if ((n) == 63 && lane == 0) __hip_atomic_fetch_add((LAS unsigned*)(L + L_FLAG + 12), 1u, __ATOMIC_RELAXED, __HIP_MEMORY_SCOPE_WORKGROUP); \
      __builtin_amdgcn_sched_barrier(0); } while (0)
#pragma unroll
    for (int n = DINIT; n < PARK; ++n) {
      if (n == DINIT) {
        *(LAS u32x4*)(L + img_w + w * 512) = wq0;
        *(LAS u32x4*)(L + img_w + w * 512 + 128) = wq1;
        *(LAS u32x4*)(L + L_X + w * 272 + ln * 16) = xq;
        asm volatile("s_waitcnt lgkmcnt(0)" ::: "memory");
        if (lane == 0) __hip_atomic_fetch_add((LAS unsigned*)(L + L_FLAG + 16), 1u, __ATOMIC_RELAXED, __HIP_MEMORY_SCOPE_WORKGROUP);
        __builtin_amdgcn_sched_barrier(0);
      }
      ISSUE(n);
      if (n >= 12 && n < 12 + SPILL) { *(LAS u32x4*)(L + L_SP + (w * SPILL + (n - 12)) * 1024 + voff) = kr[n - 12]; __builtin_amdgcn_sched_barrier(0); }
      if (n == 8) STAMP(10);
      if (n == 14) STAMP(11);
    }
    STAMP(1);
    lds_wait_ge<1>(L + L_FLAG, 1u);
    STAMP(2);
    const u32x4 qf = *(LAS u32x4*)(L + L_Q + (lane & 3) * 256 + (4 * lg + ((lane >> 2) & 3)) * 16);
    float sc[8];
    constexpr int NQ = PARK + 4;
#pragma unroll
    for (int uu = 0; uu < 32; uu += 4) {
      const int u0 = (uu + SPILL) & 31;
      const int t = u0 >> 3;
      f32x4 d[4];
      float y[4];
#pragma unroll
      for (int k = 0; k < 4; ++k) d[k] = mfma(u0 + k < SPILL ? *(LAS u32x4*)(L + L_SP + (w * SPILL + u0 + k) * 1024 + voff) : kr[u0 + k], qf, f32x4{0.f, 0.f, 0.f, 0.f});
      __builtin_amdgcn_sched_barrier(0);
#pragma unroll
      for (int k = 0; k < 4; ++k) y[k] = d[k][t] + dppf<ROR12>(d[k][(t + 1) & 3]);
      __builtin_amdgcn_sched_barrier(0);
#pragma unroll
      for (int k = 0; k < 4; ++k) y[k] += dppf<ROR8>(d[k][(t + 2) & 3]);
      __builtin_amdgcn_sched_barrier(0);
#pragma unroll
      for (int k = 0; k < 4; ++k) y[k] += dppf<ROR4>(d[k][(t + 3) & 3]);
      __builtin_amdgcn_sched_barrier(0);
#pragma unroll
      for (int k = 0; k < 4; ++k) {
        const int T = (u0 + k) & 7;
        sc[T] = uu < 8 ? y[k] : t == 0 ? bank_set<1>(sc[T], y[k]) : t == 1 ? bank_set<2>(sc[T], y[k]) : t == 2 ? bank_set<4>(sc[T], y[k]) : bank_set<8>(sc[T], y[k]);
      }
      __builtin_amdgcn_sched_barrier(0);
      if ((uu & 4) == 0) ISSUE(PARK + (uu >> 3));
    }
    float m = fmaxf(fmaxf(fmaxf(sc[0], sc[1]), fmaxf(sc[2], sc[3])), fmaxf(fmaxf(sc[4], sc[5]), fmaxf(sc[6], sc[7])));
    m = fmaxf(m, dppf<ROR4>(m)); m = fmaxf(m, dppf<ROR8>(m)); m = x32max(x16max(m));
    m *= C2;
    float ls = 0.f;
#pragma unroll
    for (int T = 0; T < 8; ++T) { sc[T] = __builtin_amdgcn_exp2f(fmaf(sc[T], C2, -m)); ls += sc[T]; }
    ls += dppf<ROR4>(ls); ls += dppf<ROR8>(ls); ls = x32sum(x16sum(ls));
    if (lane < 4) { *(LAS float*)(L + L_ML + lane * 32 + w * 4) = m; *(LAS float*)(L + L_ML + 128 + lane * 32 + w * 4) = ls; }
    asm volatile("s_waitcnt lgkmcnt(0)" ::: "memory");
    if (lane == 0) __hip_atomic_fetch_add((LAS unsigned*)(L + L_FLAG + 4), 1u, __ATOMIC_RELAXED, __HIP_MEMORY_SCOPE_WORKGROUP);
    STAMP(3);
    u32x4 pa;
#pragma unroll
    for (int k = 0; k < 4; ++k) pa[k] = pack_bf16(sc[2 * k], sc[2 * k + 1]);
    f32x4 acc[8];
#pragma unroll
    for (int j = 0; j < 8; ++j) acc[j] = f32x4{0.f, 0.f, 0.f, 0.f};
    __builtin_amdgcn_sched_barrier(0);
#pragma unroll
    for (int t = 0; t < 4; ++t) {
#pragma unroll
      for (int n = NQ; n < 64; ++n) if ((n > 31 + 8 * t + DHI && n <= 39 + 8 * t + DHI) || (t == 0 && n <= 31 + DHI)) ISSUE(n);
      u32x4 a, bb[8];
#pragma unroll
      for (int k = 0; k < 4; ++k) a[k] = t == 0 ? pa[k] : t == 1 ? dppu<SHL4>(pa[k]) : t == 2 ? dppu<SHL8>(pa[k]) : dppu<SHL12>(pa[k]);
#pragma unroll
      for (int k = 0; k < 4; ++k) {
#pragma unroll
        for (int j = 0; j < 8; ++j) bb[j][k] = __builtin_amdgcn_perm(vr[8 * t + 2 * k + 1][j >> 1], vr[8 * t + 2 * k][j >> 1], (j & 1) ? 0x07060302u : 0x05040100u);
        if (t == 3 && k < 3) __builtin_amdgcn_sched_barrier(0);
      }
#pragma unroll
      for (int j = 0; j < 8; ++j) acc[j] = mfma(a, bb[j], acc[j]);
      __builtin_amdgcn_sched_barrier(0);
    }
    if (lane < 16) {
#pragma unroll
      for (int j = 0; j < 8; ++j) *(LAS f32x4*)(L + L_RED + w * 2048 + j * 256 + lane * 16) = acc[j];
    }
    STAMP(4);
    wg_barrier();
    STAMP(5);
    if (w < 2) {
      f32x4 v = *(LAS f32x4*)(L + L_RED + tid * 16) * *(LAS f32x4*)(L + L_AL);
#pragma unroll
      for (int ww = 1; ww < 8; ++ww) v += *(LAS f32x4*)(L + L_RED + ww * 2048 + tid * 16) * *(LAS f32x4*)(L + L_AL + ww * 16);
      const unsigned h0 = min(pack_bf16(v.x, v.y), SENT - 1u), h1 = min(pack_bf16(v.z, v.w), SENT - 1u);
      const u32x4 pk = {h0, h1, dppu<XOR1>(h0), dppu<XOR1>(h1)};
      if (!(lane & 1)) __builtin_amdgcn_raw_buffer_store_b128(pk, rws, WS_P1 + g * 32768 + (tid >> 5) * 8192 + i * 256 + (tid & 31) * 8, 0, AUX_PUB);
    }
    STAMP(6);
    u32x4 pr[2];
    {
      u32x4 pw[2];
      poll_words<1, H1S0, H1PD, H1SG>(mk_srd(a_ws, WS_END), WS_P1 + g * 32768 + rb * 8192 + w * 1024 + lane * 16, pr, pw);
      const u32x4 p = pw[0];
      const float a0 = tsum16(__uint_as_float(p.x << 16), __uint_as_float(p.y << 16)), a1 = tsum16(__uint_as_float(p.x & 0xFFFF0000u), __uint_as_float(p.y & 0xFFFF0000u));
      const float a2 = tsum16(__uint_as_float(p.z << 16), __uint_as_float(p.w << 16)), a3 = tsum16(__uint_as_float(p.z & 0xFFFF0000u), __uint_as_float(p.w & 0xFFFF0000u));
      *(LAS unsigned*)(L + L_OI + w * 272 + ln * 16 + lg * 4) = pack_bf16(tsum32(a0, a2), tsum32(a1, a3));
    }
    STAMP(7);
    wg_barrier();
    if (w < 4) {
      LAS unsigned char* ip = L + L_IMG + w * 4096 + (lg >> 1) * 512 + (lg & 1) * 128 + ln * 8;
      f32x4 d0 = {0.f, 0.f, 0.f, 0.f}, d1 = {0.f, 0.f, 0.f, 0.f};
#pragma unroll
      for (int ks = 0; ks < 4; ks += 2) {
        const u32x2 a0 = tr_read(ip + ks * 1024), a1 = tr_read(ip + ks * 1024 + 256), a2 = tr_read(ip + ks * 1024 + 1024), a3 = tr_read(ip + ks * 1024 + 1280);
        d0 = mfma(u32x4{a0.x, a0.y, a1.x, a1.y}, *(LAS u32x4*)(L + L_OI + (ln & 7) * 272 + ks * 64 + lg * 16), d0);
        d1 = mfma(u32x4{a2.x, a2.y, a3.x, a3.y}, *(LAS u32x4*)(L + L_OI + (ln & 7) * 272 + ks * 64 + 64 + lg * 16), d1);
      }
      if (ln < 8) __builtin_amdgcn_raw_buffer_store_b128(no_sent(d0 + d1), rws, P2L ? WS_P2 + ln * 65536 + (cb * 2 + (w >> 1)) * 4096 + (g * 4 + rb) * 128 + (w & 1) * 64 + lg * 16 : WS_P2 + ln * 65536 + (cb * 4 + w) * 2048 + (g * 4 + rb) * 64 + lg * 16, 0, AUX_PUB);
    }
    STAMP(9);
    POLL_KEEP(pr[0]);
  }
  STAMP_OUT;
}

extern "C" void kernel_launch(void* const* d_in, const int* in_sizes, int n_in, void* d_out, int out_size, void* d_ws, size_t ws_size,
                              hipStream_t stream) {
  gqa_decode_kernel<<<256, NTHR, 0, stream>>>(d_in[1], d_in[2], d_ws, d_in[3], d_in[0], d_in[4], d_out);
}
```

```cpp
#define SPILL 0
#define CREF 0
#define PROBE 0
#define PK 0
#include <hip/hip_runtime.h>
#include <hip/hip_bf16.h>

#define GAS __attribute__((address_space(1)))
#define LAS __attribute__((address_space(3)))
typedef unsigned u32x4 __attribute__((ext_vector_type(4)));
typedef unsigned u32x2 __attribute__((ext_vector_type(2)));
typedef float f32x4 __attribute__((ext_vector_type(4)));
typedef short s16x4 __attribute__((ext_vector_type(4)));
typedef __bf16 bf16x8 __attribute__((ext_vector_type(8)));
typedef __bf16 bf16x2 __attribute__((ext_vector_type(2)));
typedef float f32x2 __attribute__((ext_vector_type(2)));
typedef unsigned long long u64;

constexpr int NTHR = 576;
#define DLO 1
#define DHI 6
#define DMID 3
#define ELO -40
#define EHI 30
#define EVAL 2
#define PD 1
#define PSLP 5
#define H1S0 24
#define H1S1 0
#define H2S0 24
#define H2S1 0
#define NTH 16
#define H1NEAR 0
#define NEARFL "nt"
#define NEND 64
#define NBURST 64
#define H1PD 1
#define H2PD 1
#define H1SG 10
#define H2SG 10
#define AUX_PUB 16
#define AUX_CLR 16
#define POLLFL "sc1"
#define P2L 0
#define AUX_OUT 0
#define CADJ 15
#define DINIT 4
#define TOFF 110
#define TAU 290
#define PARK (36 + SPILL)
#define TRIGQ 20
#define TRIGS 54
#define AUX_Q1 0
#define SLPI 6
constexpr int AUX_NT = 2, AUX_SC1 = 16;
constexpr unsigned SENT = 0xFFFFFFFFu;
constexpr unsigned SPIN_CAP = 1u << 16;
constexpr int WS_HI = 1024;
constexpr int WS_T0 = 3072;
constexpr int WS_QG = 4096;
constexpr int WS_P1 = WS_QG + 524288;
constexpr int WS_ST = WS_P1 + 262144;
constexpr int WS_P2 = WS_ST + 8192;
constexpr int WS_END = WS_P2 + 524288;
constexpr int L_IMG = 0;
constexpr int L_RED = 16384;
constexpr int L_Q = 32768;
constexpr int L_ML = L_Q + 1024;
constexpr int L_OI = L_ML + 256;
constexpr int L_AL = L_OI + 8 * 272;
constexpr int L_FLAG = L_AL + 128;
constexpr int L_X = L_FLAG + 48;
constexpr int L_SP = L_X + 8 * 272;
constexpr int L_END = L_SP + 8 * SPILL * 1024;

constexpr float C2 = 0.08838834764831845f * 1.4426950408889634f;

#define STAMP(k) do { __builtin_amdgcn_sched_barrier(0); asm volatile("s_memrealtime %0\n\ts_waitcnt lgkmcnt(0)" : "=s"(stamps_[k]) :: "memory"); __builtin_amdgcn_sched_barrier(0); } while (0)
#define STAMP_DECL u64 stamps_[12] = {0, 0, 0, 0, 0, 0, 0, 0, 0, 0, 0, 0}
#define STAMP_OUT do { if (w == 8) { const u64 until_ = stamps_[8] + (u64)probe_ * 8; while (__builtin_amdgcn_s_memrealtime() < until_) __builtin_amdgcn_s_sleep(1); } } while (0)

#define TEND do { } while (0)
__device__ __forceinline__ f32x4 mfma(u32x4 a, u32x4 b, f32x4 c) {
  return __builtin_amdgcn_mfma_f32_16x16x32_bf16(__builtin_bit_cast(bf16x8, a), __builtin_bit_cast(bf16x8, b), c, 0, 0, 0);
}
template <int CTRL> __device__ __forceinline__ float dppf(float x) {
  return __builtin_bit_cast(float, __builtin_amdgcn_mov_dpp(__builtin_bit_cast(int, x), CTRL, 0xf, 0xf, true));
}
template <int CTRL> __device__ __forceinline__ unsigned dppu(unsigned x) {
  return (unsigned)__builtin_amdgcn_mov_dpp((int)x, CTRL, 0xf, 0xf, true);
}
constexpr int ROR4 = 0x124, ROR8 = 0x128, ROR12 = 0x12C, SHL4 = 0x104, SHL8 = 0x108, SHL12 = 0x10C, QID = 0xE4, XOR1 = 0xB1, XOR2 = 0x4E, HMIR = 0x141;
template <int BANKS> __device__ __forceinline__ float bank_set(float old, float y) {
  return __builtin_bit_cast(float, __builtin_amdgcn_update_dpp(__builtin_bit_cast(int, old), __builtin_bit_cast(int, y), QID, 0xf, BANKS, false));
}
__device__ __forceinline__ float x16max(float x) {
  auto s = __builtin_amdgcn_permlane16_swap(__float_as_uint(x), __float_as_uint(x), false, false);
  return fmaxf(__uint_as_float(s[0]), __uint_as_float(s[1]));
}
__device__ __forceinline__ float x32max(float x) {
  auto s = __builtin_amdgcn_permlane32_swap(__float_as_uint(x), __float_as_uint(x), false, false);
  return fmaxf(__uint_as_float(s[0]), __uint_as_float(s[1]));
}
__device__ __forceinline__ float x16sum(float x) {
  auto s = __builtin_amdgcn_permlane16_swap(__float_as_uint(x), __float_as_uint(x), false, false);
  return __uint_as_float(s[0]) + __uint_as_float(s[1]);
}
__device__ __forceinline__ float x32sum(float x) {
  auto s = __builtin_amdgcn_permlane32_swap(__float_as_uint(x), __float_as_uint(x), false, false);
  return __uint_as_float(s[0]) + __uint_as_float(s[1]);
}
__device__ __forceinline__ float tsum16(float a, float b) {
  auto s = __builtin_amdgcn_permlane16_swap(__float_as_uint(a), __float_as_uint(b), false, false);
  return __uint_as_float(s[0]) + __uint_as_float(s[1]);
}
__device__ __forceinline__ float tsum32(float a, float b) {
  auto s = __builtin_amdgcn_permlane32_swap(__float_as_uint(a), __float_as_uint(b), false, false);
  return __uint_as_float(s[0]) + __uint_as_float(s[1]);
}
__device__ __forceinline__ unsigned pack_bf16(float lo, float hi) {
  f32x2 v = {lo, hi};
  return __builtin_bit_cast(unsigned, __builtin_convertvector(v, bf16x2));
}
__device__ __forceinline__ u32x4 no_sent(f32x4 v) {
  u32x4 u = __builtin_bit_cast(u32x4, v);
  u.x = min(u.x, SENT - 1u); u.y = min(u.y, SENT - 1u); u.z = min(u.z, SENT - 1u); u.w = min(u.w, SENT - 1u);
  return u;
}
__device__ __forceinline__ bool has_sent(u32x4 u) { return max(max(u.x, u.y), max(u.z, u.w)) == SENT; }
__device__ __forceinline__ void wg_barrier() {
  asm volatile("s_waitcnt lgkmcnt(0)\n\ts_barrier" ::: "memory");
}
__device__ __forceinline__ __amdgpu_buffer_rsrc_t mk_rsrc(const void* p, int bytes) {
  return __builtin_amdgcn_make_buffer_rsrc((void*)p, 0, bytes, 0x00020000);
}
template <int K> __device__ __forceinline__ void poll_issue(u32x4& r, int off, u64 mask, u32x4 srd) {
  asm volatile("s_mov_b64 exec, %2\n\tbuffer_load_dwordx4 %0, %1, %3, 0 offen offset:%4 " POLLFL "\n\ts_mov_b64 exec, -1"
               : "+v"(r) : "v"(off), "s"(mask), "s"(srd), "i"(K * (P2L ? 2048 : 1024)) : "memory");
}
template <int N, int S0, int PDN, int SG> __device__ __forceinline__ void poll_words(u32x4 srd, int off, u32x4 (&r)[2], u32x4 (&res)[2]) {
  r[0] = u32x4{SENT, SENT, SENT, SENT}; r[1] = r[0];
  if (S0) __builtin_amdgcn_s_sleep(S0);
  poll_issue<0>(r[0], off, ~0ull, srd);
  if (N > 1) poll_issue<1>(r[1], off, ~0ull, srd);
  asm volatile("s_waitcnt vmcnt(0)" : "+v"(r[0]), "+v"(r[1]) :: "memory");
#pragma unroll 1
  for (unsigned spins = 0; spins < SPIN_CAP; ++spins) {
    res[0] = r[0]; res[1] = r[1];
    asm volatile("" : "+v"(res[0]), "+v"(res[1]));
    const u64 m0 = __builtin_amdgcn_ballot_w64(has_sent(res[0]));
    const u64 m1 = N > 1 ? __builtin_amdgcn_ballot_w64(has_sent(res[1])) : 0ull;
    if ((m0 | m1) == 0ull) break;
    poll_issue<0>(r[0], off, m0 | 1ull, srd);
    if (N > 1) poll_issue<1>(r[1], off, m1 | 1ull, srd);
    if (PDN > 1 && spins < PDN - 1) __builtin_amdgcn_s_sleep(SG);
    asm volatile("s_waitcnt vmcnt(%2)" : "+v"(r[0]), "+v"(r[1]) : "i"((PDN - 1) * N) : "memory");
  }
}
#define POLL_KEEP(r) asm volatile("s_waitcnt vmcnt(0)" :: "v"(r) : "memory")
__device__ __forceinline__ u32x4 mk_srd(u64 p, int bytes) { return u32x4{(unsigned)p, (unsigned)(p >> 32) & 0xFFFFu, (unsigned)bytes, 0x00020000u}; }
__device__ __forceinline__ u32x2 tr_read(LAS unsigned char* p) {
  return __builtin_bit_cast(u32x2, __builtin_amdgcn_ds_read_tr16_b64_v4i16((LAS s16x4*)p));
}
__device__ __forceinline__ int img_row(int k) { return (k & ~12) * 32 + ((k >> 2) & 1) * 256 + ((k >> 3) & 1) * 128; }

template <int SLP> __device__ __forceinline__ void lds_wait_ge(LAS unsigned char* p, unsigned want) {
#pragma unroll 1
  for (unsigned spins = 0; spins < (SPIN_CAP << 6); ++spins) {
    if ((unsigned)__builtin_amdgcn_readfirstlane(*(volatile LAS unsigned*)p) >= want) break;
    __builtin_amdgcn_s_sleep(SLP);
  }
  asm volatile("" ::: "memory");
}
typedef __attribute__((address_space(4))) const u64 karg_t;
__device__ __forceinline__ karg_t* kargs() { karg_t* p = (karg_t*)__builtin_amdgcn_kernarg_segment_ptr(); asm volatile("" : "+s"(p)); return p; }

__global__ void __launch_bounds__(NTHR, 3)
gqa_decode_kernel(const void* kc_, const void* vc_, void* ws_, const void* wq_, const void* x_, const void* wo_, void* out_) {
  karg_t* ka = kargs();
  const u64 a_k = ka[0], a_v = ka[1], a_ws = ka[2], a_wq = ka[3], a_x = ka[4];
  const unsigned t0 = (unsigned)__builtin_amdgcn_s_memrealtime();
  __builtin_amdgcn_sched_barrier(0);
  __shared__ __attribute__((aligned(16))) unsigned char smem[L_END];
  LAS unsigned char* L = (LAS unsigned char*)smem;
  const int tid = threadIdx.x, lane = tid & 63, w = __builtin_amdgcn_readfirstlane(tid >> 6);
  const int g = blockIdx.x & 7, i = blockIdx.x >> 3;
  const int lg = lane >> 4, ln = lane & 15;
  const int rb = i >> 3, cb = i & 7;
  const int gq = i & 7, kq = i >> 3, cq = (g + gq) & 7;
  const int img_w = L_IMG + ((lane & 7) >> 1) * 4096 + img_row(lane >> 3) + (lane & 1) * 16;
  STAMP_DECL;
  int probe_ = 0;
  STAMP(0);
  int koff = ((lane >> 2) & 3) * 256 + lg * 64 + (lane & 3) * 16;
  int qoff = (lane >> 3) * 8192 + (lane & 7) * 16, xoff = kq * 256 + ln * 16;
  asm volatile("" : "+v"(koff), "+v"(qoff), "+v"(xoff));
  if (tid < 9) *(LAS unsigned*)(L + L_FLAG + tid * 4) = tid == 8 ? t0 : 0u;
  wg_barrier();

  if (__builtin_expect(w >= 8, 0)) {
    __builtin_amdgcn_s_setprio(3);
    const __amdgpu_buffer_rsrc_t rws = mk_rsrc((const void*)a_ws, WS_END);
    u32x4 wv[16];
    const u32x2 ep = __builtin_amdgcn_raw_buffer_load_b64(rws, 0, 0, AUX_SC1);
    {
      const int sl = cq * 4 + kq;
      const u32x4 sv = {SENT, SENT, SENT, SENT};
      __builtin_amdgcn_raw_buffer_store_b128(sv, rws, WS_P1 + gq * 32768 + sl * 1024 + lane * 16, 0, AUX_CLR);
      if (lane < 2) __builtin_amdgcn_raw_buffer_store_b128(sv, rws, WS_ST + gq * 1024 + sl * 32 + lane * 16, 0, AUX_SC1);
      const int o2 = P2L ? WS_P2 + lg * 65536 + (cb * 2 + ((lane >> 3) & 1)) * 4096 + (g * 4 + rb) * 128 + (lane & 7) * 16
                         : WS_P2 + lg * 65536 + (cb * 4 + ((lane >> 2) & 3)) * 2048 + (g * 4 + rb) * 64 + (lane & 3) * 16;
      __builtin_amdgcn_raw_buffer_store_b128(sv, rws, o2, 0, AUX_CLR);
      __builtin_amdgcn_raw_buffer_store_b128(sv, rws, o2 + 4 * 65536, 0, AUX_CLR);
    }
    __builtin_amdgcn_sched_barrier(0);
    lds_wait_ge<1>(L + L_FLAG + 16, 8u);
    STAMP(1);
    const u64 tag = (((u64)ep.y << 32) | ep.x) + 1ull;
    {
      LAS unsigned char* ip = L + L_IMG + (lg >> 1) * 512 + (lg & 1) * 128 + ln * 8;
      LAS unsigned char* xp = L + L_X + (ln & 7) * 272 + lg * 16;
      f32x4 d[4];
#pragma unroll
      for (int ct = 0; ct < 4; ++ct) d[ct] = f32x4{0.f, 0.f, 0.f, 0.f};
#pragma unroll
      for (int ks = 0; ks < 4; ++ks) {
        const u32x4 xb = *(LAS u32x4*)(xp + ks * 64);
#pragma unroll
        for (int ct = 0; ct < 4; ++ct) {
          const u32x2 a0 = tr_read(ip + ct * 4096 + ks * 1024), a1 = tr_read(ip + ct * 4096 + ks * 1024 + 256);
          d[ct] = mfma(u32x4{a0.x, a0.y, a1.x, a1.y}, xb, d[ct]);
        }
      }
      const int off = WS_QG + gq * 65536 + (ln & 7) * 8192 + kq * 2048 + cq * 256 + (ln >> 3) * 128 + lg * 16;
      __builtin_amdgcn_raw_buffer_store_b128(__builtin_bit_cast(u32x4, ln < 8 ? d[0] : d[2]), rws, off, 0, AUX_SC1);
      __builtin_amdgcn_raw_buffer_store_b128(__builtin_bit_cast(u32x4, ln < 8 ? d[1] : d[3]), rws, off + 64, 0, AUX_SC1);
      if (lane == 0) __builtin_amdgcn_raw_buffer_store_b32(t0, rws, WS_T0 + gq * 128 + (cq * 4 + kq) * 4, 0, AUX_SC1);
      asm volatile("s_waitcnt vmcnt(0)" ::: "memory");
      if (lane == 0) __builtin_amdgcn_raw_buffer_store_b64(u32x2{(unsigned)tag, (unsigned)(tag >> 32)}, rws, WS_HI + gq * 256 + (cq * 4 + kq) * 8, 0, AUX_SC1);
    }
    STAMP(2);
    lds_wait_ge<4>(L + L_FLAG + 8, TRIGQ);
    STAMP(9);
    {
#pragma unroll 1
      for (unsigned spins = 0; spins < SPIN_CAP; ++spins) {
        asm volatile("" ::: "memory");
        const u32x2 hi = __builtin_amdgcn_raw_buffer_load_b64(rws, WS_HI + g * 256 + (lane & 31) * 8, 0, AUX_SC1);
        if (!__any(((hi.x ^ (unsigned)tag) | (hi.y ^ (unsigned)(tag >> 32))) != 0u)) break;
        __builtin_amdgcn_s_sleep(8);
      }
      asm volatile("" ::: "memory");
      const int off = WS_QG + g * 65536 + (i >> 2) * 8192 + lane * 16;
#pragma unroll
      for (int t = 0; t < 8; ++t) wv[t] = __builtin_amdgcn_raw_buffer_load_b128(rws, off + (t & 1) * 1024, (t >> 1) * 2048, AUX_SC1);
      {
        int dt = (int)(__builtin_amdgcn_raw_buffer_load_b32(rws, WS_T0 + g * 128 + (lane & 31) * 4, 0, AUX_SC1) - t0);
        int sm_ = dt; sm_ += (int)dppu<XOR1>(sm_); sm_ += (int)dppu<XOR2>(sm_); sm_ += (int)dppu<ROR4>(sm_); sm_ += (int)dppu<ROR8>(sm_); sm_ = (__builtin_amdgcn_readlane(sm_, 0) + __builtin_amdgcn_readlane(sm_, 16)) / 32;
        int mx_ = dt; mx_ = max(mx_, (int)dppu<XOR1>(mx_)); mx_ = max(mx_, (int)dppu<XOR2>(mx_)); mx_ = max(mx_, (int)dppu<ROR4>(mx_)); mx_ = max(mx_, (int)dppu<ROR8>(mx_)); mx_ = max(__builtin_amdgcn_readlane(mx_, 0), __builtin_amdgcn_readlane(mx_, 16));
        dt = min(dt, (int)dppu<XOR1>(dt)); dt = min(dt, (int)dppu<XOR2>(dt)); dt = min(dt, (int)dppu<ROR4>(dt)); dt = min(dt, (int)dppu<ROR8>(dt));
        dt = min(__builtin_amdgcn_readlane(dt, 0), __builtin_amdgcn_readlane(dt, 16));
        probe_ = PK == 1 ? mx_ - dt : PK == 2 ? sm_ - dt : 0;
        if (lane == 0) *(volatile LAS unsigned*)(L + L_FLAG + 32) = t0 + (unsigned)(min(dt + CADJ, 0));
      }
#pragma unroll
      for (int hh = 0; hh < 2; ++hh) {
        const f32x4 q4 = (__builtin_bit_cast(f32x4, wv[hh]) + __builtin_bit_cast(f32x4, wv[2 + hh])) + (__builtin_bit_cast(f32x4, wv[4 + hh]) + __builtin_bit_cast(f32x4, wv[6 + hh]));
        *(LAS u32x2*)(L + L_Q + hh * 512 + lane * 8) = u32x2{pack_bf16(q4.x, q4.y), pack_bf16(q4.z, q4.w)};
      }
      asm volatile("s_waitcnt lgkmcnt(0)" ::: "memory");
      *(volatile LAS unsigned*)(L + L_FLAG) = 1u;
    }
    STAMP(3);
    lds_wait_ge<4>(L + L_FLAG + 4, 8u);
    STAMP(4);
    const f32x4 m0 = *(LAS f32x4*)(L + L_ML + (lane & 3) * 32), m1 = *(LAS f32x4*)(L + L_ML + (lane & 3) * 32 + 16);
    {
      const f32x4 l0 = *(LAS f32x4*)(L + L_ML + 128 + (lane & 3) * 32), l1 = *(LAS f32x4*)(L + L_ML + 128 + (lane & 3) * 32 + 16);
      const float M = fmaxf(fmaxf(fmaxf(m0.x, m0.y), fmaxf(m0.z, m0.w)), fmaxf(fmaxf(m1.x, m1.y), fmaxf(m1.z, m1.w)));
      float Ls = 0.f;
#pragma unroll
      for (int k = 0; k < 4; ++k) Ls += l0[k] * __builtin_amdgcn_exp2f(m0[k] - M) + l1[k] * __builtin_amdgcn_exp2f(m1[k] - M);
      if (lane < 4) __builtin_amdgcn_raw_buffer_store_b64(u32x2{min(__float_as_uint(M), SENT - 1u), min(__float_as_uint(Ls), SENT - 1u)}, rws, WS_ST + g * 1024 + i * 32 + lane * 8, 0, AUX_SC1);
    }
    lds_wait_ge<4>(L + L_FLAG + 8, TRIGS);
    STAMP(10);
    {
      u32x2 st;
#pragma unroll 1
      for (unsigned spins = 0; spins < SPIN_CAP; ++spins) {
        asm volatile("" ::: "memory");
        st = __builtin_amdgcn_raw_buffer_load_b64(rws, WS_ST + g * 1024 + (i >> 2) * 128 + ln * 8, 0, AUX_SC1);
        if (!__any(max(st.x, st.y) == SENT)) break;
        __builtin_amdgcn_s_sleep(8);
      }
      const float Ms = __uint_as_float(st.x);
      float Mg = fmaxf(Ms, dppf<ROR4>(Ms)); Mg = fmaxf(Mg, dppf<ROR8>(Mg));
      float Lg = __uint_as_float(st.y) * __builtin_amdgcn_exp2f(Ms - Mg);
      Lg += dppf<ROR4>(Lg); Lg += dppf<ROR8>(Lg);
      const float inv = 1.0f / Lg;
      if (lane < 4) {
#pragma unroll
        for (int k = 0; k < 4; ++k) {
          *(LAS float*)(L + L_AL + k * 16 + lane * 4) = __builtin_amdgcn_exp2f(m0[k] - Mg) * inv;
          *(LAS float*)(L + L_AL + 64 + k * 16 + lane * 4) = __builtin_amdgcn_exp2f(m1[k] - Mg) * inv;
        }
      }
    }
    STAMP(5);
    const __amdgpu_buffer_rsrc_t ro = mk_rsrc((const unsigned char*)kargs()[5] + g * 524288 + cb * 128, 524288 - 1024 + 128);
    const __amdgpu_buffer_rsrc_t rout = mk_rsrc((const void*)kargs()[6], 8192);
    lds_wait_ge<2>(L + L_FLAG + 12, 8u);
    {
      const int kk = lane >> 3;
      const int woff = (kk & 3) * 131072 + (kk >> 2) * 8192 + rb * 2048 + (lane & 7) * 16;
#pragma unroll
      for (int t = 0; t < 16; ++t) wv[t] = __builtin_amdgcn_raw_buffer_load_b128(ro, woff, (t & 7) * 16384 + (t >> 3) * 1024, AUX_NT);
    }
    __builtin_amdgcn_sched_barrier(0);
    __builtin_amdgcn_s_setprio(0);
    wg_barrier();
    __builtin_amdgcn_s_sleep(SLPI);
#pragma unroll
    for (int t = 0; t < 16; ++t) *(LAS u32x4*)(L + img_w + img_row(8 * t)) = wv[t];
    STAMP(11);
    wg_barrier();
    STAMP(6);
    {
      const int off = P2L ? WS_P2 + (i >> 2) * 65536 + (2 * g + ((i >> 1) & 1)) * 4096 + (lane >> 2) * 128 + (i & 1) * 64 + (lane & 3) * 16 : WS_P2 + (i >> 2) * 65536 + (4 * g + (i & 3)) * 2048 + lane * 16;
      u32x4 pr[2], pw[2];
      poll_words<2, H2S0, H2PD, H2SG>(mk_srd(a_ws, WS_END), off, pr, pw);
      STAMP(7);
      const f32x4 o = __builtin_bit_cast(f32x4, pw[0]) + __builtin_bit_cast(f32x4, pw[1]);
      float v = tsum32(tsum16(o.x, o.y), tsum16(o.z, o.w));
      v += dppf<ROR4>(v); v += dppf<ROR8>(v);
      if (ln < 4) __builtin_amdgcn_raw_buffer_store_b16((unsigned short)pack_bf16(v, v), rout, (i >> 2) * 1024 + (4 * g + (i & 3)) * 32 + ln * 8 + lg * 2, 0, AUX_OUT);
      if (blockIdx.x == 0 && lane == 0) __builtin_amdgcn_raw_buffer_store_b64(u32x2{(unsigned)tag, (unsigned)(tag >> 32)}, rws, 0, 0, AUX_SC1);
      STAMP(8);
      TEND;
      POLL_KEEP(pr[0]); POLL_KEEP(pr[1]);
    }
  } else {
    const size_t base = ((size_t)((i >> 2) * 8 + g) * 4096 + (i & 3) * 1024) * 256;
#define CHUNK(c) ((c) * 32768 + ((w + (c)) & 7) * 4096)
    const int voff = lane * 16;
    const __amdgpu_buffer_rsrc_t rk = mk_rsrc((const unsigned char*)a_k + base, 262144), rv = mk_rsrc((const unsigned char*)a_v + base, 262144);
    const __amdgpu_buffer_rsrc_t rws = mk_rsrc((const void*)a_ws, WS_END);
    u32x4 kr[32], vr[32];
    const __amdgpu_buffer_rsrc_t rq = mk_rsrc((const void*)a_wq, 4194304), rx = mk_rsrc((const void*)a_x, 8192);
    const int qs = kq * 1048576 + gq * 1024 + cq * 128 + w * 131072;
    const u32x4 wq0 = __builtin_amdgcn_raw_buffer_load_b128(rq, qoff, qs, AUX_NT);
    const u32x4 wq1 = __builtin_amdgcn_raw_buffer_load_b128(rq, qoff, qs + 65536, AUX_NT);
    const u32x4 xq = __builtin_amdgcn_raw_buffer_load_b128(rx, xoff, w * 1024, 0);
#pragma unroll
    for (int n = 0; n < DINIT; ++n) kr[n] = __builtin_amdgcn_raw_buffer_load_b128(rk, koff + (n & 3) * 1024, CHUNK(n >> 2), AUX_NT);
    __builtin_amdgcn_sched_barrier(0);
    STAMP(8);
    unsigned tref = t0;
    int lag = 0;
#define ISSUE(n) do { \
      __builtin_amdgcn_s_waitcnt(0x0F70 | (DHI - 1)); \
      if ((n) < NBURST && lag < EHI) { __builtin_amdgcn_s_waitcnt(0x0F70 | (DMID - 1)); if ((n) >= NTH && (n) < NEND && lag < ELO) __builtin_amdgcn_s_waitcnt(0x0F70 | (DLO - 1)); } \
      if ((n) < 32) kr[(n) & 31] = __builtin_amdgcn_raw_buffer_load_b128(rk, koff + ((n) & 3) * 1024, CHUNK(((n) & 31) >> 2), AUX_NT); \
      else vr[(n) & 31] = __builtin_amdgcn_raw_buffer_load_b128(rv, voff + ((n) & 3) * 1024, CHUNK(((n) & 31) >> 2), AUX_NT); \
      __builtin_amdgcn_sched_barrier(0); \
      if (((n) & (EVAL - 1)) == 0) { \
        if (CREF && (n) >= 24 && (n) <= PARK + 4) tref = __builtin_amdgcn_readfirstlane(*(volatile LAS unsigned*)(L + L_FLAG + 32)); \
        lag = (int)((unsigned)__builtin_amdgcn_s_memrealtime() - tref) - (TOFF + ((n) * TAU) / 10); } \
      if ((n) == TRIGQ || (n) == TRIGS) *(volatile LAS unsigned*)(L + L_FLAG + 8) = (n); \
      if ((n) == 63 && lane == 0) __hip_atomic_fetch_add((LAS unsigned*)(L + L_FLAG + 12), 1u, __ATOMIC_RELAXED, __HIP_MEMORY_SCOPE_WORKGROUP); \
      __builtin_amdgcn_sched_barrier(0); } while (0)
#pragma unroll
    for (int n = DINIT; n < PARK; ++n) {
      if (n == DINIT) {
        *(LAS u32x4*)(L + img_w + w * 512) = wq0;
        *(LAS u32x4*)(L + img_w + w * 512 + 128) = wq1;
        *(LAS u32x4*)(L + L_X + w * 272 + ln * 16) = xq;
        asm volatile("s_waitcnt lgkmcnt(0)" ::: "memory");
        if (lane == 0) __hip_atomic_fetch_add((LAS unsigned*)(L + L_FLAG + 16), 1u, __ATOMIC_RELAXED, __HIP_MEMORY_SCOPE_WORKGROUP);
        __builtin_amdgcn_sched_barrier(0);
      }
      ISSUE(n);
      if (n >= 12 && n < 12 + SPILL) { *(LAS u32x4*)(L + L_SP + (w * SPILL + (n - 12)) * 1024 + voff) = kr[n - 12]; __builtin_amdgcn_sched_barrier(0); }
      if (n == 8) STAMP(10);
      if (n == 14) STAMP(11);
    }
    STAMP(1);
    lds_wait_ge<1>(L + L_FLAG, 1u);
    STAMP(2);
    const u32x4 qf = *(LAS u32x4*)(L + L_Q + (lane & 3) * 256 + (4 * lg + ((lane >> 2) & 3)) * 16);
    float sc[8];
    constexpr int NQ = PARK + 4;
#pragma unroll
    for (int uu = 0; uu < 32; uu += 4) {
      const int u0 = (uu + SPILL) & 31;
      const int t = u0 >> 3;
      f32x4 d[4];
      float y[4];
#pragma unroll
      for (int k = 0; k < 4; ++k) d[k] = mfma(u0 + k < SPILL ? *(LAS u32x4*)(L + L_SP + (w * SPILL + u0 + k) * 1024 + voff) : kr[u0 + k], qf, f32x4{0.f, 0.f, 0.f, 0.f});
      __builtin_amdgcn_sched_barrier(0);
#pragma unroll
      for (int k = 0; k < 4; ++k) y[k] = d[k][t] + dppf<ROR12>(d[k][(t + 1) & 3]);
      __builtin_amdgcn_sched_barrier(0);
#pragma unroll
      for (int k = 0; k < 4; ++k) y[k] += dppf<ROR8>(d[k][(t + 2) & 3]);
      __builtin_amdgcn_sched_barrier(0);
#pragma unroll
      for (int k = 0; k < 4; ++k) y[k] += dppf<ROR4>(d[k][(t + 3) & 3]);
      __builtin_amdgcn_sched_barrier(0);
#pragma unroll
      for (int k = 0; k < 4; ++k) {
        const int T = (u0 + k) & 7;
        sc[T] = uu < 8 ? y[k] : t == 0 ? bank_set<1>(sc[T], y[k]) : t == 1 ? bank_set<2>(sc[T], y[k]) : t == 2 ? bank_set<4>(sc[T], y[k]) : bank_set<8>(sc[T], y[k]);
      }
      __builtin_amdgcn_sched_barrier(0);
      if ((uu & 4) == 0) ISSUE(PARK + (uu >> 3));
    }
    float m = fmaxf(fmaxf(fmaxf(sc[0], sc[1]), fmaxf(sc[2], sc[3])), fmaxf(fmaxf(sc[4], sc[5]), fmaxf(sc[6], sc[7])));
    m = fmaxf(m, dppf<ROR4>(m)); m = fmaxf(m, dppf<ROR8>(m)); m = x32max(x16max(m));
    m *= C2;
    float ls = 0.f;
#pragma unroll
    for (int T = 0; T < 8; ++T) { sc[T] = __builtin_amdgcn_exp2f(fmaf(sc[T], C2, -m)); ls += sc[T]; }
    ls += dppf<ROR4>(ls); ls += dppf<ROR8>(ls); ls = x32sum(x16sum(ls));
    if (lane < 4) { *(LAS float*)(L + L_ML + lane * 32 + w * 4) = m; *(LAS float*)(L + L_ML + 128 + lane * 32 + w * 4) = ls; }
    asm volatile("s_waitcnt lgkmcnt(0)" ::: "memory");
    if (lane == 0) __hip_atomic_fetch_add((LAS unsigned*)(L + L_FLAG + 4), 1u, __ATOMIC_RELAXED, __HIP_MEMORY_SCOPE_WORKGROUP);
    STAMP(3);
    u32x4 pa;
#pragma unroll
    for (int k = 0; k < 4; ++k) pa[k] = pack_bf16(sc[2 * k], sc[2 * k + 1]);
    f32x4 acc[8];
#pragma unroll
    for (int j = 0; j < 8; ++j) acc[j] = f32x4{0.f, 0.f, 0.f, 0.f};
    __builtin_amdgcn_sched_barrier(0);
#pragma unroll
    for (int t = 0; t < 4; ++t) {
#pragma unroll
      for (int n = NQ; n < 64; ++n) if ((n > 31 + 8 * t + DHI && n <= 39 + 8 * t + DHI) || (t == 0 && n <= 31 + DHI)) ISSUE(n);
      u32x4 a, bb[8];
#pragma unroll
      for (int k = 0; k < 4; ++k) a[k] = t == 0 ? pa[k] : t == 1 ? dppu<SHL4>(pa[k]) : t == 2 ? dppu<SHL8>(pa[k]) : dppu<SHL12>(pa[k]);
#pragma unroll
      for (int k = 0; k < 4; ++k) {
#pragma unroll
        for (int j = 0; j < 8; ++j) bb[j][k] = __builtin_amdgcn_perm(vr[8 * t + 2 * k + 1][j >> 1], vr[8 * t + 2 * k][j >> 1], (j & 1) ? 0x07060302u : 0x05040100u);
        if (t == 3 && k < 3) __builtin_amdgcn_sched_barrier(0);
      }
#pragma unroll
      for (int j = 0; j < 8; ++j) acc[j] = mfma(a, bb[j], acc[j]);
      __builtin_amdgcn_sched_barrier(0);
    }
    if (lane < 16) {
#pragma unroll
      for (int j = 0; j < 8; ++j) *(LAS f32x4*)(L + L_RED + w * 2048 + j * 256 + lane * 16) = acc[j];
    }
    STAMP(4);
    wg_barrier();
    STAMP(5);
    if (w < 2) {
      f32x4 v = *(LAS f32x4*)(L + L_RED + tid * 16) * *(LAS f32x4*)(L + L_AL);
#pragma unroll
      for (int ww = 1; ww < 8; ++ww) v += *(LAS f32x4*)(L + L_RED + ww * 2048 + tid * 16) * *(LAS f32x4*)(L + L_AL + ww * 16);
      const unsigned h0 = min(pack_bf16(v.x, v.y), SENT - 1u), h1 = min(pack_bf16(v.z, v.w), SENT - 1u);
      const u32x4 pk = {h0, h1, dppu<XOR1>(h0), dppu<XOR1>(h1)};
      if (!(lane & 1)) __builtin_amdgcn_raw_buffer_store_b128(pk, rws, WS_P1 + g * 32768 + (tid >> 5) * 8192 + i * 256 + (tid & 31) * 8, 0, AUX_PUB);
    }
    STAMP(6);
    u32x4 pr[2];
    {
      u32x4 pw[2];
      poll_words<1, H1S0, H1PD, H1SG>(mk_srd(a_ws, WS_END), WS_P1 + g * 32768 + rb * 8192 + w * 1024 + lane * 16, pr, pw);
      const u32x4 p = pw[0];
      const float a0 = tsum16(__uint_as_float(p.x << 16), __uint_as_float(p.y << 16)), a1 = tsum16(__uint_as_float(p.x & 0xFFFF0000u), __uint_as_float(p.y & 0xFFFF0000u));
      const float a2 = tsum16(__uint_as_float(p.z << 16), __uint_as_float(p.w << 16)), a3 = tsum16(__uint_as_float(p.z & 0xFFFF0000u), __uint_as_float(p.w & 0xFFFF0000u));
      *(LAS unsigned*)(L + L_OI + w * 272 + ln * 16 + lg * 4) = pack_bf16(tsum32(a0, a2), tsum32(a1, a3));
    }
    STAMP(7);
    wg_barrier();
    if (w < 4) {
      LAS unsigned char* ip = L + L_IMG + w * 4096 + (lg >> 1) * 512 + (lg & 1) * 128 + ln * 8;
      f32x4 d0 = {0.f, 0.f, 0.f, 0.f}, d1 = {0.f, 0.f, 0.f, 0.f};
#pragma unroll
      for (int ks = 0; ks < 4; ks += 2) {
        const u32x2 a0 = tr_read(ip + ks * 1024), a1 = tr_read(ip + ks * 1024 + 256), a2 = tr_read(ip + ks * 1024 + 1024), a3 = tr_read(ip + ks * 1024 + 1280);
        d0 = mfma(u32x4{a0.x, a0.y, a1.x, a1.y}, *(LAS u32x4*)(L + L_OI + (ln & 7) * 272 + ks * 64 + lg * 16), d0);
        d1 = mfma(u32x4{a2.x, a2.y, a3.x, a3.y}, *(LAS u32x4*)(L + L_OI + (ln & 7) * 272 + ks * 64 + 64 + lg * 16), d1);
      }
      if (ln < 8) __builtin_amdgcn_raw_buffer_store_b128(no_sent(d0 + d1), rws, P2L ? WS_P2 + ln * 65536 + (cb * 2 + (w >> 1)) * 4096 + (g * 4 + rb) * 128 + (w & 1) * 64 + lg * 16 : WS_P2 + ln * 65536 + (cb * 4 + w) * 2048 + (g * 4 + rb) * 64 + lg * 16, 0, AUX_PUB);
    }
    STAMP(9);
    POLL_KEEP(pr[0]);
  }
  STAMP_OUT;
}

extern "C" void kernel_launch(void* const* d_in, const int* in_sizes, int n_in, void* d_out, int out_size, void* d_ws, size_t ws_size,
                              hipStream_t stream) {
  gqa_decode_kernel<<<256, NTHR, 0, stream>>>(d_in[1], d_in[2], d_ws, d_in[3], d_in[0], d_in[4], d_out);
}
```

```cpp
#define SPILL 0
#define CREF 0
#include <hip/hip_runtime.h>
#include <hip/hip_bf16.h>

#define GAS __attribute__((address_space(1)))
#define LAS __attribute__((address_space(3)))
typedef unsigned u32x4 __attribute__((ext_vector_type(4)));
typedef unsigned u32x2 __attribute__((ext_vector_type(2)));
typedef float f32x4 __attribute__((ext_vector_type(4)));
typedef short s16x4 __attribute__((ext_vector_type(4)));
typedef __bf16 bf16x8 __attribute__((ext_vector_type(8)));
typedef __bf16 bf16x2 __attribute__((ext_vector_type(2)));
typedef float f32x2 __attribute__((ext_vector_type(2)));
typedef unsigned long long u64;

constexpr int NTHR = 576;
#define DLO 1
#define DHI 6
#define DMID 3
#define ELO -40
#define EHI 30
#define EVAL 2
#define PD 1
#define PSLP 5
#define H1S0 24
#define H1S1 0
#define H2S0 24
#define H2S1 0
#define NTH 16
#define H1NEAR 0
#define NEARFL "nt"
#define NEND 64
#define NBURST 64
#define H1PD 1
#define H2PD 1
#define H1SG 10
#define H2SG 10
#define AUX_PUB 16
#define AUX_CLR 16
#define POLLFL "sc1"
#define P2L 0
#define AUX_OUT 0
#define CADJ 0
#define LATEM 0
#define DINIT 4
#define TOFF 110
#define TAU 290
#define PARK (36 + SPILL)
#define TRIGQ 20
#define TRIGS 54
#define AUX_Q1 0
#define SLPI 6
constexpr int AUX_NT = 2, AUX_SC1 = 16;
constexpr unsigned SENT = 0xFFFFFFFFu;
constexpr unsigned SPIN_CAP = 1u << 16;
constexpr int WS_HI = 1024;
constexpr int WS_T0 = 3072;
constexpr int WS_QG = 4096;
constexpr int WS_P1 = WS_QG + 524288;
constexpr int WS_ST = WS_P1 + 262144;
constexpr int WS_P2 = WS_ST + 8192;
constexpr int WS_END = WS_P2 + 524288;
constexpr int L_IMG = 0;
constexpr int L_RED = 16384;
constexpr int L_Q = 32768;
constexpr int L_ML = L_Q + 1024;
constexpr int L_OI = L_ML + 256;
constexpr int L_AL = L_OI + 8 * 272;
constexpr int L_FLAG = L_AL + 128;
constexpr int L_X = L_FLAG + 48;
constexpr int L_SP = L_X + 8 * 272;
constexpr int L_END = L_SP + 8 * SPILL * 1024;

constexpr float C2 = 0.08838834764831845f * 1.4426950408889634f;

#define STAMP(k) do { } while (0)
#define STAMP_DECL do { } while (0)
#define STAMP_OUT do { } while (0)

#define TEND do { } while (0)
__device__ __forceinline__ f32x4 mfma(u32x4 a, u32x4 b, f32x4 c) {
  return __builtin_amdgcn_mfma_f32_16x16x32_bf16(__builtin_bit_cast(bf16x8, a), __builtin_bit_cast(bf16x8, b), c, 0, 0, 0);
}
template <int CTRL> __device__ __forceinline__ float dppf(float x) {
  return __builtin_bit_cast(float, __builtin_amdgcn_mov_dpp(__builtin_bit_cast(int, x), CTRL, 0xf, 0xf, true));
}
template <int CTRL> __device__ __forceinline__ unsigned dppu(unsigned x) {
  return (unsigned)__builtin_amdgcn_mov_dpp((int)x, CTRL, 0xf, 0xf, true);
}
constexpr int ROR4 = 0x124, ROR8 = 0x128, ROR12 = 0x12C, SHL4 = 0x104, SHL8 = 0x108, SHL12 = 0x10C, QID = 0xE4, XOR1 = 0xB1, XOR2 = 0x4E, HMIR = 0x141;
template <int BANKS> __device__ __forceinline__ float bank_set(float old, float y) {
  return __builtin_bit_cast(float, __builtin_amdgcn_update_dpp(__builtin_bit_cast(int, old), __builtin_bit_cast(int, y), QID, 0xf, BANKS, false));
}
__device__ __forceinline__ float x16max(float x) {
  auto s = __builtin_amdgcn_permlane16_swap(__float_as_uint(x), __float_as_uint(x), false, false);
  return fmaxf(__uint_as_float(s[0]), __uint_as_float(s[1]));
}
__device__ __forceinline__ float x32max(float x) {
  auto s = __builtin_amdgcn_permlane32_swap(__float_as_uint(x), __float_as_uint(x), false, false);
  return fmaxf(__uint_as_float(s[0]), __uint_as_float(s[1]));
}
__device__ __forceinline__ float x16sum(float x) {
  auto s = __builtin_amdgcn_permlane16_swap(__float_as_uint(x), __float_as_uint(x), false, false);
  return __uint_as_float(s[0]) + __uint_as_float(s[1]);
}
__device__ __forceinline__ float x32sum(float x) {
  auto s = __builtin_amdgcn_permlane32_swap(__float_as_uint(x), __float_as_uint(x), false, false);
  return __uint_as_float(s[0]) + __uint_as_float(s[1]);
}
__device__ __forceinline__ float tsum16(float a, float b) {
  auto s = __builtin_amdgcn_permlane16_swap(__float_as_uint(a), __float_as_uint(b), false, false);
  return __uint_as_float(s[0]) + __uint_as_float(s[1]);
}
__device__ __forceinline__ float tsum32(float a, float b) {
  auto s = __builtin_amdgcn_permlane32_swap(__float_as_uint(a), __float_as_uint(b), false, false);
  return __uint_as_float(s[0]) + __uint_as_float(s[1]);
}
__device__ __forceinline__ unsigned pack_bf16(float lo, float hi) {
  f32x2 v = {lo, hi};
  return __builtin_bit_cast(unsigned, __builtin_convertvector(v, bf16x2));
}
__device__ __forceinline__ u32x4 no_sent(f32x4 v) {
  u32x4 u = __builtin_bit_cast(u32x4, v);
  u.x = min(u.x, SENT - 1u); u.y = min(u.y, SENT - 1u); u.z = min(u.z, SENT - 1u); u.w = min(u.w, SENT - 1u);
  return u;
}
__device__ __forceinline__ bool has_sent(u32x4 u) { return max(max(u.x, u.y), max(u.z, u.w)) == SENT; }
__device__ __forceinline__ void wg_barrier() {
  asm volatile("s_waitcnt lgkmcnt(0)\n\ts_barrier" ::: "memory");
}
__device__ __forceinline__ __amdgpu_buffer_rsrc_t mk_rsrc(const void* p, int bytes) {
  return __builtin_amdgcn_make_buffer_rsrc((void*)p, 0, bytes, 0x00020000);
}
template <int K> __device__ __forceinline__ void poll_issue(u32x4& r, int off, u64 mask, u32x4 srd) {
  asm volatile("s_mov_b64 exec, %2\n\tbuffer_load_dwordx4 %0, %1, %3, 0 offen offset:%4 " POLLFL "\n\ts_mov_b64 exec, -1"
               : "+v"(r) : "v"(off), "s"(mask), "s"(srd), "i"(K * (P2L ? 2048 : 1024)) : "memory");
}
template <int N, int S0, int PDN, int SG> __device__ __forceinline__ void poll_words(u32x4 srd, int off, u32x4 (&r)[2], u32x4 (&res)[2]) {
  r[0] = u32x4{SENT, SENT, SENT, SENT}; r[1] = r[0];
  if (S0) __builtin_amdgcn_s_sleep(S0);
  poll_issue<0>(r[0], off, ~0ull, srd);
  if (N > 1) poll_issue<1>(r[1], off, ~0ull, srd);
  asm volatile("s_waitcnt vmcnt(0)" : "+v"(r[0]), "+v"(r[1]) :: "memory");
#pragma unroll 1
  for (unsigned spins = 0; spins < SPIN_CAP; ++spins) {
    res[0] = r[0]; res[1] = r[1];
    asm volatile("" : "+v"(res[0]), "+v"(res[1]));
    const u64 m0 = __builtin_amdgcn_ballot_w64(has_sent(res[0]));
    const u64 m1 = N > 1 ? __builtin_amdgcn_ballot_w64(has_sent(res[1])) : 0ull;
    if ((m0 | m1) == 0ull) break;
    poll_issue<0>(r[0], off, m0 | 1ull, srd);
    if (N > 1) poll_issue<1>(r[1], off, m1 | 1ull, srd);
    if (PDN > 1 && spins < PDN - 1) __builtin_amdgcn_s_sleep(SG);
    asm volatile("s_waitcnt vmcnt(%2)" : "+v"(r[0]), "+v"(r[1]) : "i"((PDN - 1) * N) : "memory");
  }
}
#define POLL_KEEP(r) asm volatile("" :: "v"(r) : "memory")
__device__ __forceinline__ u32x4 mk_srd(u64 p, int bytes) { return u32x4{(unsigned)p, (unsigned)(p >> 32) & 0xFFFFu, (unsigned)bytes, 0x00020000u}; }
__device__ __forceinline__ u32x2 tr_read(LAS unsigned char* p) {
  return __builtin_bit_cast(u32x2, __builtin_amdgcn_ds_read_tr16_b64_v4i16((LAS s16x4*)p));
}
__device__ __forceinline__ int img_row(int k) { return (k & ~12) * 32 + ((k >> 2) & 1) * 256 + ((k >> 3) & 1) * 128; }

template <int SLP> __device__ __forceinline__ void lds_wait_ge(LAS unsigned char* p, unsigned want) {
#pragma unroll 1
  for (unsigned spins = 0; spins < (SPIN_CAP << 6); ++spins) {
    if ((unsigned)__builtin_amdgcn_readfirstlane(*(volatile LAS unsigned*)p) >= want) break;
    __builtin_amdgcn_s_sleep(SLP);
  }
  asm volatile("" ::: "memory");
}
typedef __attribute__((address_space(4))) const u64 karg_t;
__device__ __forceinline__ karg_t* kargs() { karg_t* p = (karg_t*)__builtin_amdgcn_kernarg_segment_ptr(); asm volatile("" : "+s"(p)); return p; }

__global__ void __launch_bounds__(NTHR, 3)
gqa_decode_kernel(const void* kc_, const void* vc_, void* ws_, const void* wq_, const void* x_, const void* wo_, void* out_) {
  karg_t* ka = kargs();
  const u64 a_k = ka[0], a_v = ka[1], a_ws = ka[2], a_wq = ka[3], a_x = ka[4];
  const unsigned t0 = (unsigned)__builtin_amdgcn_s_memrealtime();
  __builtin_amdgcn_sched_barrier(0);
  __shared__ __attribute__((aligned(16))) unsigned char smem[L_END];
  LAS unsigned char* L = (LAS unsigned char*)smem;
  const int tid = threadIdx.x, lane = tid & 63, w = __builtin_amdgcn_readfirstlane(tid >> 6);
  const int g = blockIdx.x & 7, i = blockIdx.x >> 3;
  const int lg = lane >> 4, ln = lane & 15;
  const int rb = i >> 3, cb = i & 7;
  const int gq = i & 7, kq = i >> 3, cq = (g + gq) & 7;
  const int img_w = L_IMG + ((lane & 7) >> 1) * 4096 + img_row(lane >> 3) + (lane & 1) * 16;
  STAMP_DECL;
  STAMP(0);
  int koff = ((lane >> 2) & 3) * 256 + lg * 64 + (lane & 3) * 16;
  int qoff = (lane >> 3) * 8192 + (lane & 7) * 16, xoff = kq * 256 + ln * 16;
  asm volatile("" : "+v"(koff), "+v"(qoff), "+v"(xoff));
  if (tid < 9) *(LAS unsigned*)(L + L_FLAG + tid * 4) = tid == 8 ? t0 : 0u;
  wg_barrier();

  if (__builtin_expect(w >= 8, 0)) {
    __builtin_amdgcn_s_setprio(3);
    const __amdgpu_buffer_rsrc_t rws = mk_rsrc((const void*)a_ws, WS_END);
    u32x4 wv[16];
    const u32x2 ep = __builtin_amdgcn_raw_buffer_load_b64(rws, 0, 0, AUX_SC1);
    {
      const int sl = cq * 4 + kq;
      const u32x4 sv = {SENT, SENT, SENT, SENT};
      __builtin_amdgcn_raw_buffer_store_b128(sv, rws, WS_P1 + gq * 32768 + sl * 1024 + lane * 16, 0, AUX_CLR);
      if (lane < 2) __builtin_amdgcn_raw_buffer_store_b128(sv, rws, WS_ST + gq * 1024 + sl * 32 + lane * 16, 0, AUX_SC1);
      const int o2 = P2L ? WS_P2 + lg * 65536 + (cb * 2 + ((lane >> 3) & 1)) * 4096 + (g * 4 + rb) * 128 + (lane & 7) * 16
                         : WS_P2 + lg * 65536 + (cb * 4 + ((lane >> 2) & 3)) * 2048 + (g * 4 + rb) * 64 + (lane & 3) * 16;
      __builtin_amdgcn_raw_buffer_store_b128(sv, rws, o2, 0, AUX_CLR);
      __builtin_amdgcn_raw_buffer_store_b128(sv, rws, o2 + 4 * 65536, 0, AUX_CLR);
    }
    __builtin_amdgcn_sched_barrier(0);
    lds_wait_ge<1>(L + L_FLAG + 16, 8u);
    STAMP(1);
    const u64 tag = (((u64)ep.y << 32) | ep.x) + 1ull;
    {
      LAS unsigned char* ip = L + L_IMG + (lg >> 1) * 512 + (lg & 1) * 128 + ln * 8;
      LAS unsigned char* xp = L + L_X + (ln & 7) * 272 + lg * 16;
      f32x4 d[4];
#pragma unroll
      for (int ct = 0; ct < 4; ++ct) d[ct] = f32x4{0.f, 0.f, 0.f, 0.f};
#pragma unroll
      for (int ks = 0; ks < 4; ++ks) {
        const u32x4 xb = *(LAS u32x4*)(xp + ks * 64);
#pragma unroll
        for (int ct = 0; ct < 4; ++ct) {
          const u32x2 a0 = tr_read(ip + ct * 4096 + ks * 1024), a1 = tr_read(ip + ct * 4096 + ks * 1024 + 256);
          d[ct] = mfma(u32x4{a0.x, a0.y, a1.x, a1.y}, xb, d[ct]);
        }
      }
      const int off = WS_QG + gq * 65536 + (ln & 7) * 8192 + kq * 2048 + cq * 256 + (ln >> 3) * 128 + lg * 16;
      __builtin_amdgcn_raw_buffer_store_b128(__builtin_bit_cast(u32x4, ln < 8 ? d[0] : d[2]), rws, off, 0, AUX_SC1);
      __builtin_amdgcn_raw_buffer_store_b128(__builtin_bit_cast(u32x4, ln < 8 ? d[1] : d[3]), rws, off + 64, 0, AUX_SC1);
      if (lane == 0) __builtin_amdgcn_raw_buffer_store_b32(t0, rws, WS_T0 + gq * 128 + (cq * 4 + kq) * 4, 0, AUX_SC1);
      asm volatile("s_waitcnt vmcnt(0)" ::: "memory");
      if (lane == 0) __builtin_amdgcn_raw_buffer_store_b64(u32x2{(unsigned)tag, (unsigned)(tag >> 32)}, rws, WS_HI + gq * 256 + (cq * 4 + kq) * 8, 0, AUX_SC1);
    }
    STAMP(2);
    lds_wait_ge<4>(L + L_FLAG + 8, TRIGQ);
    STAMP(9);
    {
#pragma unroll 1
      for (unsigned spins = 0; spins < SPIN_CAP; ++spins) {
        asm volatile("" ::: "memory");
        const u32x2 hi = __builtin_amdgcn_raw_buffer_load_b64(rws, WS_HI + g * 256 + (lane & 31) * 8, 0, AUX_SC1);
        if (!__any(((hi.x ^ (unsigned)tag) | (hi.y ^ (unsigned)(tag >> 32))) != 0u)) break;
        __builtin_amdgcn_s_sleep(8);
      }
      asm volatile("" ::: "memory");
      const int off = WS_QG + g * 65536 + (i >> 2) * 8192 + lane * 16;
#pragma unroll
      for (int t = 0; t < 8; ++t) wv[t] = __builtin_amdgcn_raw_buffer_load_b128(rws, off + (t & 1) * 1024, (t >> 1) * 2048, AUX_SC1);
      {
        int dt = (int)(__builtin_amdgcn_raw_buffer_load_b32(rws, WS_T0 + g * 128 + (lane & 31) * 4, 0, AUX_SC1) - t0);
        dt += (int)dppu<XOR1>(dt); dt += (int)dppu<XOR2>(dt); dt += (int)dppu<ROR4>(dt); dt += (int)dppu<ROR8>(dt);
        dt = (__builtin_amdgcn_readlane(dt, 0) + __builtin_amdgcn_readlane(dt, 16)) >> 5;
        if (lane == 0) *(volatile LAS unsigned*)(L + L_FLAG + 32) = t0 + (unsigned)(dt + CADJ);
      }
#pragma unroll
      for (int hh = 0; hh < 2; ++hh) {
        const f32x4 q4 = (__builtin_bit_cast(f32x4, wv[hh]) + __builtin_bit_cast(f32x4, wv[2 + hh])) + (__builtin_bit_cast(f32x4, wv[4 + hh]) + __builtin_bit_cast(f32x4, wv[6 + hh]));
        *(LAS u32x2*)(L + L_Q + hh * 512 + lane * 8) = u32x2{pack_bf16(q4.x, q4.y), pack_bf16(q4.z, q4.w)};
      }
      asm volatile("s_waitcnt lgkmcnt(0)" ::: "memory");
      *(volatile LAS unsigned*)(L + L_FLAG) = 1u;
    }
    STAMP(3);
    lds_wait_ge<4>(L + L_FLAG + 4, 8u);
    STAMP(4);
    const f32x4 m0 = *(LAS f32x4*)(L + L_ML + (lane & 3) * 32), m1 = *(LAS f32x4*)(L + L_ML + (lane & 3) * 32 + 16);
    {
      const f32x4 l0 = *(LAS f32x4*)(L + L_ML + 128 + (lane & 3) * 32), l1 = *(LAS f32x4*)(L + L_ML + 128 + (lane & 3) * 32 + 16);
      const float M = fmaxf(fmaxf(fmaxf(m0.x, m0.y), fmaxf(m0.z, m0.w)), fmaxf(fmaxf(m1.x, m1.y), fmaxf(m1.z, m1.w)));
      float Ls = 0.f;
#pragma unroll
      for (int k = 0; k < 4; ++k) Ls += l0[k] * __builtin_amdgcn_exp2f(m0[k] - M) + l1[k] * __builtin_amdgcn_exp2f(m1[k] - M);
      if (lane < 4) __builtin_amdgcn_raw_buffer_store_b64(u32x2{min(__float_as_uint(M), SENT - 1u), min(__float_as_uint(Ls), SENT - 1u)}, rws, WS_ST + g * 1024 + i * 32 + lane * 8, 0, AUX_SC1);
    }
    lds_wait_ge<4>(L + L_FLAG + 8, TRIGS);
    STAMP(10);
    {
      u32x2 st;
#pragma unroll 1
      for (unsigned spins = 0; spins < SPIN_CAP; ++spins) {
        asm volatile("" ::: "memory");
        st = __builtin_amdgcn_raw_buffer_load_b64(rws, WS_ST + g * 1024 + (i >> 2) * 128 + ln * 8, 0, AUX_SC1);
        if (!__any(max(st.x, st.y) == SENT)) break;
        __builtin_amdgcn_s_sleep(8);
      }
      const float Ms = __uint_as_float(st.x);
      float Mg = fmaxf(Ms, dppf<ROR4>(Ms)); Mg = fmaxf(Mg, dppf<ROR8>(Mg));
      float Lg = __uint_as_float(st.y) * __builtin_amdgcn_exp2f(Ms - Mg);
      Lg += dppf<ROR4>(Lg); Lg += dppf<ROR8>(Lg);
      const float inv = 1.0f / Lg;
      if (lane < 4) {
#pragma unroll
        for (int k = 0; k < 4; ++k) {
          *(LAS float*)(L + L_AL + k * 16 + lane * 4) = __builtin_amdgcn_exp2f(m0[k] - Mg) * inv;
          *(LAS float*)(L + L_AL + 64 + k * 16 + lane * 4) = __builtin_amdgcn_exp2f(m1[k] - Mg) * inv;
        }
      }
    }
    STAMP(5);
    const __amdgpu_buffer_rsrc_t ro = mk_rsrc((const unsigned char*)kargs()[5] + g * 524288 + cb * 128, 524288 - 1024 + 128);
    const __amdgpu_buffer_rsrc_t rout = mk_rsrc((const void*)kargs()[6], 8192);
    lds_wait_ge<2>(L + L_FLAG + 12, 8u);
    {
      const int kk = lane >> 3;
      const int woff = (kk & 3) * 131072 + (kk >> 2) * 8192 + rb * 2048 + (lane & 7) * 16;
#pragma unroll
      for (int t = 0; t < 16; ++t) wv[t] = __builtin_amdgcn_raw_buffer_load_b128(ro, woff, (t & 7) * 16384 + (t >> 3) * 1024, AUX_NT);
    }
    __builtin_amdgcn_sched_barrier(0);
    __builtin_amdgcn_s_setprio(0);
    wg_barrier();
    __builtin_amdgcn_s_sleep(SLPI);
#pragma unroll
    for (int t = 0; t < 16; ++t) *(LAS u32x4*)(L + img_w + img_row(8 * t)) = wv[t];
    STAMP(11);
    wg_barrier();
    STAMP(6);
    {
      const int off = P2L ? WS_P2 + (i >> 2) * 65536 + (2 * g + ((i >> 1) & 1)) * 4096 + (lane >> 2) * 128 + (i & 1) * 64 + (lane & 3) * 16 : WS_P2 + (i >> 2) * 65536 + (4 * g + (i & 3)) * 2048 + lane * 16;
      u32x4 pr[2], pw[2];
      poll_words<2, H2S0, H2PD, H2SG>(mk_srd(a_ws, WS_END), off, pr, pw);
      STAMP(7);
      const f32x4 o = __builtin_bit_cast(f32x4, pw[0]) + __builtin_bit_cast(f32x4, pw[1]);
      float v = tsum32(tsum16(o.x, o.y), tsum16(o.z, o.w));
      v += dppf<ROR4>(v); v += dppf<ROR8>(v);
      if (ln < 4) __builtin_amdgcn_raw_buffer_store_b16((unsigned short)pack_bf16(v, v), rout, (i >> 2) * 1024 + (4 * g + (i & 3)) * 32 + ln * 8 + lg * 2, 0, AUX_OUT);
      if (blockIdx.x == 0 && lane == 0) __builtin_amdgcn_raw_buffer_store_b64(u32x2{(unsigned)tag, (unsigned)(tag >> 32)}, rws, 0, 0, AUX_SC1);
      STAMP(8);
      TEND;
      POLL_KEEP(pr[0]); POLL_KEEP(pr[1]);
    }
  } else {
    const size_t base = ((size_t)((i >> 2) * 8 + g) * 4096 + (i & 3) * 1024) * 256;
#define CHUNK(c) ((c) * 32768 + ((w + (c)) & 7) * 4096)
    const int voff = lane * 16;
    const __amdgpu_buffer_rsrc_t rk = mk_rsrc((const unsigned char*)a_k + base, 262144), rv = mk_rsrc((const unsigned char*)a_v + base, 262144);
    const __amdgpu_buffer_rsrc_t rws = mk_rsrc((const void*)a_ws, WS_END);
    u32x4 kr[32], vr[32];
    const __amdgpu_buffer_rsrc_t rq = mk_rsrc((const void*)a_wq, 4194304), rx = mk_rsrc((const void*)a_x, 8192);
    const int qs = kq * 1048576 + gq * 1024 + cq * 128 + w * 131072;
    const u32x4 wq0 = __builtin_amdgcn_raw_buffer_load_b128(rq, qoff, qs, AUX_NT);
    const u32x4 wq1 = __builtin_amdgcn_raw_buffer_load_b128(rq, qoff, qs + 65536, AUX_NT);
    const u32x4 xq = __builtin_amdgcn_raw_buffer_load_b128(rx, xoff, w * 1024, 0);
#pragma unroll
    for (int n = 0; n < DINIT; ++n) kr[n] = __builtin_amdgcn_raw_buffer_load_b128(rk, koff + (n & 3) * 1024, CHUNK(n >> 2), AUX_NT);
    __builtin_amdgcn_sched_barrier(0);
    STAMP(8);
    unsigned tref = t0;
    int lag = 0;
#define ISSUE(n) do { \
      __builtin_amdgcn_s_waitcnt(0x0F70 | (DHI - 1)); \
      if ((n) < NBURST && lag < EHI) { __builtin_amdgcn_s_waitcnt(0x0F70 | (DMID - 1)); if ((n) >= NTH && (n) < NEND && lag < ELO) __builtin_amdgcn_s_waitcnt(0x0F70 | (DLO - 1)); } \
      if ((n) < 32) kr[(n) & 31] = __builtin_amdgcn_raw_buffer_load_b128(rk, koff + ((n) & 3) * 1024, CHUNK(((n) & 31) >> 2), AUX_NT); \
      else vr[(n) & 31] = __builtin_amdgcn_raw_buffer_load_b128(rv, voff + ((n) & 3) * 1024, CHUNK(((n) & 31) >> 2), AUX_NT); \
      __builtin_amdgcn_sched_barrier(0); \
      if (((n) & (EVAL - 1)) == 0) { \
        if (CREF && (n) >= 24 && (n) <= PARK + 4) tref = __builtin_amdgcn_readfirstlane(*(volatile LAS unsigned*)(L + L_FLAG + 32)); \
        lag = (int)((unsigned)__builtin_amdgcn_s_memrealtime() - tref) - (TOFF + ((n) * TAU) / 10); } \
      if ((n) == TRIGQ || (n) == TRIGS) *(volatile LAS unsigned*)(L + L_FLAG + 8) = (n); \
      if ((n) == 63 && lane == 0) __hip_atomic_fetch_add((LAS unsigned*)(L + L_FLAG + 12), 1u, __ATOMIC_RELAXED, __HIP_MEMORY_SCOPE_WORKGROUP); \
      __builtin_amdgcn_sched_barrier(0); } while (0)
#pragma unroll
    for (int n = DINIT; n < PARK; ++n) {
      if (n == DINIT) {
        *(LAS u32x4*)(L + img_w + w * 512) = wq0;
        *(LAS u32x4*)(L + img_w + w * 512 + 128) = wq1;
        *(LAS u32x4*)(L + L_X + w * 272 + ln * 16) = xq;
        asm volatile("s_waitcnt lgkmcnt(0)" ::: "memory");
        if (lane == 0) __hip_atomic_fetch_add((LAS unsigned*)(L + L_FLAG + 16), 1u, __ATOMIC_RELAXED, __HIP_MEMORY_SCOPE_WORKGROUP);
        __builtin_amdgcn_sched_barrier(0);
      }
      ISSUE(n);
      if (n >= 12 && n < 12 + SPILL) { *(LAS u32x4*)(L + L_SP + (w * SPILL + (n - 12)) * 1024 + voff) = kr[n - 12]; __builtin_amdgcn_sched_barrier(0); }
      if (n == 8) STAMP(10);
      if (n == 14) STAMP(11);
    }
    STAMP(1);
    lds_wait_ge<1>(L + L_FLAG, 1u);
    STAMP(2);
    const u32x4 qf = *(LAS u32x4*)(L + L_Q + (lane & 3) * 256 + (4 * lg + ((lane >> 2) & 3)) * 16);
    float sc[8];
    constexpr int NQ = PARK + 4;
#pragma unroll
    for (int uu = 0; uu < 32; uu += 4) {
      const int u0 = (uu + SPILL) & 31;
      const int t = u0 >> 3;
      f32x4 d[4];
      float y[4];
#pragma unroll
      for (int k = 0; k < 4; ++k) d[k] = mfma(u0 + k < SPILL ? *(LAS u32x4*)(L + L_SP + (w * SPILL + u0 + k) * 1024 + voff) : kr[u0 + k], qf, f32x4{0.f, 0.f, 0.f, 0.f});
      __builtin_amdgcn_sched_barrier(0);
#pragma unroll
      for (int k = 0; k < 4; ++k) y[k] = d[k][t] + dppf<ROR12>(d[k][(t + 1) & 3]);
      __builtin_amdgcn_sched_barrier(0);
#pragma unroll
      for (int k = 0; k < 4; ++k) y[k] += dppf<ROR8>(d[k][(t + 2) & 3]);
      __builtin_amdgcn_sched_barrier(0);
#pragma unroll
      for (int k = 0; k < 4; ++k) y[k] += dppf<ROR4>(d[k][(t + 3) & 3]);
      __builtin_amdgcn_sched_barrier(0);
#pragma unroll
      for (int k = 0; k < 4; ++k) {
        const int T = (u0 + k) & 7;
        sc[T] = uu < 8 ? y[k] : t == 0 ? bank_set<1>(sc[T], y[k]) : t == 1 ? bank_set<2>(sc[T], y[k]) : t == 2 ? bank_set<4>(sc[T], y[k]) : bank_set<8>(sc[T], y[k]);
      }
      __builtin_amdgcn_sched_barrier(0);
      if ((uu & 4) == 0) ISSUE(PARK + (uu >> 3));
    }
    float m = fmaxf(fmaxf(fmaxf(sc[0], sc[1]), fmaxf(sc[2], sc[3])), fmaxf(fmaxf(sc[4], sc[5]), fmaxf(sc[6], sc[7])));
    m = fmaxf(m, dppf<ROR4>(m)); m = fmaxf(m, dppf<ROR8>(m)); m = x32max(x16max(m));
    m *= C2;
    float ls = 0.f;
#pragma unroll
    for (int T = 0; T < 8; ++T) { sc[T] = __builtin_amdgcn_exp2f(fmaf(sc[T], C2, -m)); ls += sc[T]; }
    ls += dppf<ROR4>(ls); ls += dppf<ROR8>(ls); ls = x32sum(x16sum(ls));
    if (lane < 4) { *(LAS float*)(L + L_ML + lane * 32 + w * 4) = m; *(LAS float*)(L + L_ML + 128 + lane * 32 + w * 4) = ls; }
    asm volatile("s_waitcnt lgkmcnt(0)" ::: "memory");
    if (lane == 0) __hip_atomic_fetch_add((LAS unsigned*)(L + L_FLAG + 4), 1u, __ATOMIC_RELAXED, __HIP_MEMORY_SCOPE_WORKGROUP);
    STAMP(3);
    u32x4 pa;
#pragma unroll
    for (int k = 0; k < 4; ++k) pa[k] = pack_bf16(sc[2 * k], sc[2 * k + 1]);
    f32x4 acc[8];
#pragma unroll
    for (int j = 0; j < 8; ++j) acc[j] = f32x4{0.f, 0.f, 0.f, 0.f};
    __builtin_amdgcn_sched_barrier(0);
#pragma unroll
    for (int t = 0; t < 4; ++t) {
#pragma unroll
      for (int n = NQ; n < 64; ++n) if ((n > 31 + 8 * t + DHI && n <= 39 + 8 * t + DHI) || (t == 0 && n <= 31 + DHI)) ISSUE(n);
      u32x4 a, bb[8];
#pragma unroll
      for (int k = 0; k < 4; ++k) a[k] = t == 0 ? pa[k] : t == 1 ? dppu<SHL4>(pa[k]) : t == 2 ? dppu<SHL8>(pa[k]) : dppu<SHL12>(pa[k]);
#pragma unroll
      for (int k = 0; k < 4; ++k) {
#pragma unroll
        for (int j = 0; j < 8; ++j) bb[j][k] = __builtin_amdgcn_perm(vr[8 * t + 2 * k + 1][j >> 1], vr[8 * t + 2 * k][j >> 1], (j & 1) ? 0x07060302u : 0x05040100u);
        if (t == 3 && k < 3) __builtin_amdgcn_sched_barrier(0);
      }
#pragma unroll
      for (int j = 0; j < 8; ++j) acc[j] = mfma(a, bb[j], acc[j]);
      __builtin_amdgcn_sched_barrier(0);
    }
    if (lane < 16) {
#pragma unroll
      for (int j = 0; j < 8; ++j) *(LAS f32x4*)(L + L_RED + w * 2048 + j * 256 + lane * 16) = acc[j];
    }
    STAMP(4);
    wg_barrier();
    STAMP(5);
    if (w < 2) {
      f32x4 v = *(LAS f32x4*)(L + L_RED + tid * 16) * *(LAS f32x4*)(L + L_AL);
#pragma unroll
      for (int ww = 1; ww < 8; ++ww) v += *(LAS f32x4*)(L + L_RED + ww * 2048 + tid * 16) * *(LAS f32x4*)(L + L_AL + ww * 16);
      const unsigned h0 = min(pack_bf16(v.x, v.y), SENT - 1u), h1 = min(pack_bf16(v.z, v.w), SENT - 1u);
      const u32x4 pk = {h0, h1, dppu<XOR1>(h0), dppu<XOR1>(h1)};
      if (!(lane & 1)) __builtin_amdgcn_raw_buffer_store_b128(pk, rws, WS_P1 + g * 32768 + (tid >> 5) * 8192 + i * 256 + (tid & 31) * 8, 0, AUX_PUB);
    }
    STAMP(6);
    u32x4 pr[2];
    {
      u32x4 pw[2];
      poll_words<1, H1S0, H1PD, H1SG>(mk_srd(a_ws, WS_END), WS_P1 + g * 32768 + rb * 8192 + w * 1024 + lane * 16, pr, pw);
      const u32x4 p = pw[0];
      const float a0 = tsum16(__uint_as_float(p.x << 16), __uint_as_float(p.y << 16)), a1 = tsum16(__uint_as_float(p.x & 0xFFFF0000u), __uint_as_float(p.y & 0xFFFF0000u));
      const float a2 = tsum16(__uint_as_float(p.z << 16), __uint_as_float(p.w << 16)), a3 = tsum16(__uint_as_float(p.z & 0xFFFF0000u), __uint_as_float(p.w & 0xFFFF0000u));
      *(LAS unsigned*)(L + L_OI + w * 272 + ln * 16 + lg * 4) = pack_bf16(tsum32(a0, a2), tsum32(a1, a3));
    }
    STAMP(7);
    wg_barrier();
    if (w < 4) {
      LAS unsigned char* ip = L + L_IMG + w * 4096 + (lg >> 1) * 512 + (lg & 1) * 128 + ln * 8;
      f32x4 d0 = {0.f, 0.f, 0.f, 0.f}, d1 = {0.f, 0.f, 0.f, 0.f};
#pragma unroll
      for (int ks = 0; ks < 4; ks += 2) {
        const u32x2 a0 = tr_read(ip + ks * 1024), a1 = tr_read(ip + ks * 1024 + 256), a2 = tr_read(ip + ks * 1024 + 1024), a3 = tr_read(ip + ks * 1024 + 1280);
        d0 = mfma(u32x4{a0.x, a0.y, a1.x, a1.y}, *(LAS u32x4*)(L + L_OI + (ln & 7) * 272 + ks * 64 + lg * 16), d0);
        d1 = mfma(u32x4{a2.x, a2.y, a3.x, a3.y}, *(LAS u32x4*)(L + L_OI + (ln & 7) * 272 + ks * 64 + 64 + lg * 16), d1);
      }
      if (ln < 8) __builtin_amdgcn_raw_buffer_store_b128(no_sent(d0 + d1), rws, P2L ? WS_P2 + ln * 65536 + (cb * 2 + (w >> 1)) * 4096 + (g * 4 + rb) * 128 + (w & 1) * 64 + lg * 16 : WS_P2 + ln * 65536 + (cb * 4 + w) * 2048 + (g * 4 + rb) * 64 + lg * 16, 0, AUX_PUB);
    }
    STAMP(9);
    POLL_KEEP(pr[0]);
  }
  STAMP_OUT;
}

extern "C" void kernel_launch(void* const* d_in, const int* in_sizes, int n_in, void* d_out, int out_size, void* d_ws, size_t ws_size,
                              hipStream_t stream) {
  gqa_decode_kernel<<<256, NTHR, 0, stream>>>(d_in[1], d_in[2], d_ws, d_in[3], d_in[0], d_in[4], d_out);
}
```

```cpp
#define EARLYT 1
#include <hip/hip_runtime.h>
#include <hip/hip_bf16.h>

#define GAS __attribute__((address_space(1)))
#define LAS __attribute__((address_space(3)))
typedef unsigned u32x4 __attribute__((ext_vector_type(4)));
typedef unsigned u32x2 __attribute__((ext_vector_type(2)));
typedef float f32x4 __attribute__((ext_vector_type(4)));
typedef short s16x4 __attribute__((ext_vector_type(4)));
typedef __bf16 bf16x8 __attribute__((ext_vector_type(8)));
typedef __bf16 bf16x2 __attribute__((ext_vector_type(2)));
typedef float f32x2 __attribute__((ext_vector_type(2)));
typedef unsigned long long u64;

constexpr int NTHR = 576;
#define DLO 1
#define DHI 6
#define DMID 3
#define ELO -40
#define EHI 30
#define EVAL 2
#define PD 1
#define PSLP 5
#define H1S0 24
#define H1S1 0
#define H2S0 24
#define H2S1 0
#define NTH 16
#define H1NEAR 0
#define NEARFL "nt"
#define NEND 64
#define NBURST 64
#define H1PD 1
#define H2PD 1
#define H1SG 10
#define H2SG 10
#define AUX_PUB 16
#define AUX_CLR 16
#define POLLFL "sc1"
#define P2L 0
#define AUX_OUT 0
#define SPILL 8
#define CREF 1
#define CADJ 0
#define LATEM 0
#define DINIT 4
#define TOFF 110
#define TAU 290
#define PARK (36 + SPILL)
#define TRIGQ 20
#define TRIGS 54
#define AUX_Q1 0
#define SLPI 6
constexpr int AUX_NT = 2, AUX_SC1 = 16;
constexpr unsigned SENT = 0xFFFFFFFFu;
constexpr unsigned SPIN_CAP = 1u << 16;
constexpr int WS_HI = 1024;
constexpr int WS_T0 = 3072;
constexpr int WS_QG = 4096;
constexpr int WS_P1 = WS_QG + 524288;
constexpr int WS_ST = WS_P1 + 262144;
constexpr int WS_P2 = WS_ST + 8192;
constexpr int WS_END = WS_P2 + 524288;
constexpr int L_IMG = 0;
constexpr int L_RED = 16384;
constexpr int L_Q = 32768;
constexpr int L_ML = L_Q + 1024;
constexpr int L_OI = L_ML + 256;
constexpr int L_AL = L_OI + 8 * 272;
constexpr int L_FLAG = L_AL + 128;
constexpr int L_X = L_FLAG + 48;
constexpr int L_SP = L_X + 8 * 272;
constexpr int L_END = L_SP + 8 * SPILL * 1024;

constexpr float C2 = 0.08838834764831845f * 1.4426950408889634f;

#define STAMP(k) do { } while (0)
#define STAMP_DECL do { } while (0)
#define STAMP_OUT do { } while (0)

#define TEND do { } while (0)
__device__ __forceinline__ f32x4 mfma(u32x4 a, u32x4 b, f32x4 c) {
  return __builtin_amdgcn_mfma_f32_16x16x32_bf16(__builtin_bit_cast(bf16x8, a), __builtin_bit_cast(bf16x8, b), c, 0, 0, 0);
}
template <int CTRL> __device__ __forceinline__ float dppf(float x) {
  return __builtin_bit_cast(float, __builtin_amdgcn_mov_dpp(__builtin_bit_cast(int, x), CTRL, 0xf, 0xf, true));
}
template <int CTRL> __device__ __forceinline__ unsigned dppu(unsigned x) {
  return (unsigned)__builtin_amdgcn_mov_dpp((int)x, CTRL, 0xf, 0xf, true);
}
constexpr int ROR4 = 0x124, ROR8 = 0x128, ROR12 = 0x12C, SHL4 = 0x104, SHL8 = 0x108, SHL12 = 0x10C, QID = 0xE4, XOR1 = 0xB1, XOR2 = 0x4E, HMIR = 0x141;
template <int BANKS> __device__ __forceinline__ float bank_set(float old, float y) {
  return __builtin_bit_cast(float, __builtin_amdgcn_update_dpp(__builtin_bit_cast(int, old), __builtin_bit_cast(int, y), QID, 0xf, BANKS, false));
}
__device__ __forceinline__ float x16max(float x) {
  auto s = __builtin_amdgcn_permlane16_swap(__float_as_uint(x), __float_as_uint(x), false, false);
  return fmaxf(__uint_as_float(s[0]), __uint_as_float(s[1]));
}
__device__ __forceinline__ float x32max(float x) {
  auto s = __builtin_amdgcn_permlane32_swap(__float_as_uint(x), __float_as_uint(x), false, false);
  return fmaxf(__uint_as_float(s[0]), __uint_as_float(s[1]));
}
__device__ __forceinline__ float x16sum(float x) {
  auto s = __builtin_amdgcn_permlane16_swap(__float_as_uint(x), __float_as_uint(x), false, false);
  return __uint_as_float(s[0]) + __uint_as_float(s[1]);
}
__device__ __forceinline__ float x32sum(float x) {
  auto s = __builtin_amdgcn_permlane32_swap(__float_as_uint(x), __float_as_uint(x), false, false);
  return __uint_as_float(s[0]) + __uint_as_float(s[1]);
}
__device__ __forceinline__ float tsum16(float a, float b) {
  auto s = __builtin_amdgcn_permlane16_swap(__float_as_uint(a), __float_as_uint(b), false, false);
  return __uint_as_float(s[0]) + __uint_as_float(s[1]);
}
__device__ __forceinline__ float tsum32(float a, float b) {
  auto s = __builtin_amdgcn_permlane32_swap(__float_as_uint(a), __float_as_uint(b), false, false);
  return __uint_as_float(s[0]) + __uint_as_float(s[1]);
}
__device__ __forceinline__ unsigned pack_bf16(float lo, float hi) {
  f32x2 v = {lo, hi};
  return __builtin_bit_cast(unsigned, __builtin_convertvector(v, bf16x2));
}
__device__ __forceinline__ u32x4 no_sent(f32x4 v) {
  u32x4 u = __builtin_bit_cast(u32x4, v);
  u.x = min(u.x, SENT - 1u); u.y = min(u.y, SENT - 1u); u.z = min(u.z, SENT - 1u); u.w = min(u.w, SENT - 1u);
  return u;
}
__device__ __forceinline__ bool has_sent(u32x4 u) { return max(max(u.x, u.y), max(u.z, u.w)) == SENT; }
__device__ __forceinline__ void wg_barrier() {
  asm volatile("s_waitcnt lgkmcnt(0)\n\ts_barrier" ::: "memory");
}
__device__ __forceinline__ __amdgpu_buffer_rsrc_t mk_rsrc(const void* p, int bytes) {
  return __builtin_amdgcn_make_buffer_rsrc((void*)p, 0, bytes, 0x00020000);
}
template <int K> __device__ __forceinline__ void poll_issue(u32x4& r, int off, u64 mask, u32x4 srd) {
  asm volatile("s_mov_b64 exec, %2\n\tbuffer_load_dwordx4 %0, %1, %3, 0 offen offset:%4 " POLLFL "\n\ts_mov_b64 exec, -1"
               : "+v"(r) : "v"(off), "s"(mask), "s"(srd), "i"(K * (P2L ? 2048 : 1024)) : "memory");
}
template <int N, int S0, int PDN, int SG> __device__ __forceinline__ void poll_words(u32x4 srd, int off, u32x4 (&r)[2], u32x4 (&res)[2]) {
  r[0] = u32x4{SENT, SENT, SENT, SENT}; r[1] = r[0];
  if (S0) __builtin_amdgcn_s_sleep(S0);
  poll_issue<0>(r[0], off, ~0ull, srd);
  if (N > 1) poll_issue<1>(r[1], off, ~0ull, srd);
  asm volatile("s_waitcnt vmcnt(0)" : "+v"(r[0]), "+v"(r[1]) :: "memory");
#pragma unroll 1
  for (unsigned spins = 0; spins < SPIN_CAP; ++spins) {
    res[0] = r[0]; res[1] = r[1];
    asm volatile("" : "+v"(res[0]), "+v"(res[1]));
    const u64 m0 = __builtin_amdgcn_ballot_w64(has_sent(res[0]));
    const u64 m1 = N > 1 ? __builtin_amdgcn_ballot_w64(has_sent(res[1])) : 0ull;
    if ((m0 | m1) == 0ull) break;
    poll_issue<0>(r[0], off, m0 | 1ull, srd);
    if (N > 1) poll_issue<1>(r[1], off, m1 | 1ull, srd);
    if (PDN > 1 && spins < PDN - 1) __builtin_amdgcn_s_sleep(SG);
    asm volatile("s_waitcnt vmcnt(%2)" : "+v"(r[0]), "+v"(r[1]) : "i"((PDN - 1) * N) : "memory");
  }
}
#define POLL_KEEP(r) asm volatile("" :: "v"(r) : "memory")
__device__ __forceinline__ u32x4 mk_srd(u64 p, int bytes) { return u32x4{(unsigned)p, (unsigned)(p >> 32) & 0xFFFFu, (unsigned)bytes, 0x00020000u}; }
__device__ __forceinline__ u32x2 tr_read(LAS unsigned char* p) {
  return __builtin_bit_cast(u32x2, __builtin_amdgcn_ds_read_tr16_b64_v4i16((LAS s16x4*)p));
}
__device__ __forceinline__ int img_row(int k) { return (k & ~12) * 32 + ((k >> 2) & 1) * 256 + ((k >> 3) & 1) * 128; }

template <int SLP> __device__ __forceinline__ void lds_wait_ge(LAS unsigned char* p, unsigned want) {
#pragma unroll 1
  for (unsigned spins = 0; spins < (SPIN_CAP << 6); ++spins) {
    if ((unsigned)__builtin_amdgcn_readfirstlane(*(volatile LAS unsigned*)p) >= want) break;
    __builtin_amdgcn_s_sleep(SLP);
  }
  asm volatile("" ::: "memory");
}
typedef __attribute__((address_space(4))) const u64 karg_t;
__device__ __forceinline__ karg_t* kargs() { karg_t* p = (karg_t*)__builtin_amdgcn_kernarg_segment_ptr(); asm volatile("" : "+s"(p)); return p; }

__global__ void __launch_bounds__(NTHR, 3)
gqa_decode_kernel(const void* kc_, const void* vc_, void* ws_, const void* wq_, const void* x_, const void* wo_, void* out_) {
  karg_t* ka = kargs();
  const u64 a_k = ka[0], a_v = ka[1], a_ws = ka[2], a_wq = ka[3], a_x = ka[4];
  const unsigned t0 = (unsigned)__builtin_amdgcn_s_memrealtime();
  __builtin_amdgcn_sched_barrier(0);
  __shared__ __attribute__((aligned(16))) unsigned char smem[L_END];
  LAS unsigned char* L = (LAS unsigned char*)smem;
  const int tid = threadIdx.x, lane = tid & 63, w = __builtin_amdgcn_readfirstlane(tid >> 6);
  const int g = blockIdx.x & 7, i = blockIdx.x >> 3;
  const int lg = lane >> 4, ln = lane & 15;
  const int rb = i >> 3, cb = i & 7;
  const int gq = i & 7, kq = i >> 3, cq = (g + gq) & 7;
  const int img_w = L_IMG + ((lane & 7) >> 1) * 4096 + img_row(lane >> 3) + (lane & 1) * 16;
  STAMP_DECL;
  STAMP(0);
  int koff = ((lane >> 2) & 3) * 256 + lg * 64 + (lane & 3) * 16;
  int qoff = (lane >> 3) * 8192 + (lane & 7) * 16, xoff = kq * 256 + ln * 16;
  asm volatile("" : "+v"(koff), "+v"(qoff), "+v"(xoff));
  if (tid < 9) *(LAS unsigned*)(L + L_FLAG + tid * 4) = tid == 8 ? t0 : 0u;
  wg_barrier();

  if (__builtin_expect(w >= 8, 0)) {
    __builtin_amdgcn_s_setprio(3);
    const __amdgpu_buffer_rsrc_t rws = mk_rsrc((const void*)a_ws, WS_END);
    u32x4 wv[16];
    const u32x2 ep = __builtin_amdgcn_raw_buffer_load_b64(rws, 0, 0, AUX_SC1);
    {
      const int sl = cq * 4 + kq;
      const u32x4 sv = {SENT, SENT, SENT, SENT};
      __builtin_amdgcn_raw_buffer_store_b128(sv, rws, WS_P1 + gq * 32768 + sl * 1024 + lane * 16, 0, AUX_CLR);
      if (lane < 2) __builtin_amdgcn_raw_buffer_store_b128(sv, rws, WS_ST + gq * 1024 + sl * 32 + lane * 16, 0, AUX_SC1);
      const int o2 = P2L ? WS_P2 + lg * 65536 + (cb * 2 + ((lane >> 3) & 1)) * 4096 + (g * 4 + rb) * 128 + (lane & 7) * 16
                         : WS_P2 + lg * 65536 + (cb * 4 + ((lane >> 2) & 3)) * 2048 + (g * 4 + rb) * 64 + (lane & 3) * 16;
      __builtin_amdgcn_raw_buffer_store_b128(sv, rws, o2, 0, AUX_CLR);
      __builtin_amdgcn_raw_buffer_store_b128(sv, rws, o2 + 4 * 65536, 0, AUX_CLR);
    }
    __builtin_amdgcn_sched_barrier(0);
    lds_wait_ge<1>(L + L_FLAG + 16, 8u);
    STAMP(1);
    const u64 tag = (((u64)ep.y << 32) | ep.x) + 1ull;
    {
      LAS unsigned char* ip = L + L_IMG + (lg >> 1) * 512 + (lg & 1) * 128 + ln * 8;
      LAS unsigned char* xp = L + L_X + (ln & 7) * 272 + lg * 16;
      f32x4 d[4];
#pragma unroll
      for (int ct = 0; ct < 4; ++ct) d[ct] = f32x4{0.f, 0.f, 0.f, 0.f};
#pragma unroll
      for (int ks = 0; ks < 4; ++ks) {
        const u32x4 xb = *(LAS u32x4*)(xp + ks * 64);
#pragma unroll
        for (int ct = 0; ct < 4; ++ct) {
          const u32x2 a0 = tr_read(ip + ct * 4096 + ks * 1024), a1 = tr_read(ip + ct * 4096 + ks * 1024 + 256);
          d[ct] = mfma(u32x4{a0.x, a0.y, a1.x, a1.y}, xb, d[ct]);
        }
      }
      const int off = WS_QG + gq * 65536 + (ln & 7) * 8192 + kq * 2048 + cq * 256 + (ln >> 3) * 128 + lg * 16;
      __builtin_amdgcn_raw_buffer_store_b128(__builtin_bit_cast(u32x4, ln < 8 ? d[0] : d[2]), rws, off, 0, AUX_SC1);
      __builtin_amdgcn_raw_buffer_store_b128(__builtin_bit_cast(u32x4, ln < 8 ? d[1] : d[3]), rws, off + 64, 0, AUX_SC1);
      if (lane == 0) __builtin_amdgcn_raw_buffer_store_b32(t0, rws, WS_T0 + gq * 128 + (cq * 4 + kq) * 4, 0, AUX_SC1);
      asm volatile("s_waitcnt vmcnt(0)" ::: "memory");
      if (lane == 0) __builtin_amdgcn_raw_buffer_store_b64(u32x2{(unsigned)tag, (unsigned)(tag >> 32)}, rws, WS_HI + gq * 256 + (cq * 4 + kq) * 8, 0, AUX_SC1);
    }
    STAMP(2);
    const __amdgpu_buffer_rsrc_t ro = mk_rsrc((const unsigned char*)kargs()[5] + g * 524288 + cb * 128, 524288 - 1024 + 128);
    const __amdgpu_buffer_rsrc_t rout = mk_rsrc((const void*)kargs()[6], 8192);
    if (EARLYT) {
      const size_t vb = ((size_t)((i >> 2) * 8 + g) * 4096 + (i & 3) * 1024) * 256;
      const unsigned c0 = __builtin_amdgcn_raw_buffer_load_b32(mk_rsrc((const unsigned char*)a_v + vb, 262144), 0, 0, AUX_NT);
      const unsigned c1 = __builtin_amdgcn_raw_buffer_load_b32(ro, rb * 2048, 0, AUX_NT);
      const unsigned c2 = __builtin_amdgcn_raw_buffer_load_b32(rout, (i >> 2) * 1024 + (4 * g + (i & 3)) * 32, 0, AUX_NT);
      asm volatile("" :: "v"(c0), "v"(c1), "v"(c2));
    }
    lds_wait_ge<4>(L + L_FLAG + 8, TRIGQ);
    STAMP(9);
    {
#pragma unroll 1
      for (unsigned spins = 0; spins < SPIN_CAP; ++spins) {
        asm volatile("" ::: "memory");
        const u32x2 hi = __builtin_amdgcn_raw_buffer_load_b64(rws, WS_HI + g * 256 + (lane & 31) * 8, 0, AUX_SC1);
        if (!__any(((hi.x ^ (unsigned)tag) | (hi.y ^ (unsigned)(tag >> 32))) != 0u)) break;
        __builtin_amdgcn_s_sleep(8);
      }
      asm volatile("" ::: "memory");
      const int off = WS_QG + g * 65536 + (i >> 2) * 8192 + lane * 16;
#pragma unroll
      for (int t = 0; t < 8; ++t) wv[t] = __builtin_amdgcn_raw_buffer_load_b128(rws, off + (t & 1) * 1024, (t >> 1) * 2048, AUX_SC1);
      {
        int dt = (int)(__builtin_amdgcn_raw_buffer_load_b32(rws, WS_T0 + g * 128 + (lane & 31) * 4, 0, AUX_SC1) - t0);
        dt += (int)dppu<XOR1>(dt); dt += (int)dppu<XOR2>(dt); dt += (int)dppu<ROR4>(dt); dt += (int)dppu<ROR8>(dt);
        dt = (__builtin_amdgcn_readlane(dt, 0) + __builtin_amdgcn_readlane(dt, 16)) >> 5;
        if (lane == 0) *(volatile LAS unsigned*)(L + L_FLAG + 32) = t0 + (unsigned)(dt + CADJ);
      }
#pragma unroll
      for (int hh = 0; hh < 2; ++hh) {
        const f32x4 q4 = (__builtin_bit_cast(f32x4, wv[hh]) + __builtin_bit_cast(f32x4, wv[2 + hh])) + (__builtin_bit_cast(f32x4, wv[4 + hh]) + __builtin_bit_cast(f32x4, wv[6 + hh]));
        *(LAS u32x2*)(L + L_Q + hh * 512 + lane * 8) = u32x2{pack_bf16(q4.x, q4.y), pack_bf16(q4.z, q4.w)};
      }
      asm volatile("s_waitcnt lgkmcnt(0)" ::: "memory");
      *(volatile LAS unsigned*)(L + L_FLAG) = 1u;
    }
    STAMP(3);
    lds_wait_ge<4>(L + L_FLAG + 4, 8u);
    STAMP(4);
    const f32x4 m0 = *(LAS f32x4*)(L + L_ML + (lane & 3) * 32), m1 = *(LAS f32x4*)(L + L_ML + (lane & 3) * 32 + 16);
    {
      const f32x4 l0 = *(LAS f32x4*)(L + L_ML + 128 + (lane & 3) * 32), l1 = *(LAS f32x4*)(L + L_ML + 128 + (lane & 3) * 32 + 16);
      const float M = fmaxf(fmaxf(fmaxf(m0.x, m0.y), fmaxf(m0.z, m0.w)), fmaxf(fmaxf(m1.x, m1.y), fmaxf(m1.z, m1.w)));
      float Ls = 0.f;
#pragma unroll
      for (int k = 0; k < 4; ++k) Ls += l0[k] * __builtin_amdgcn_exp2f(m0[k] - M) + l1[k] * __builtin_amdgcn_exp2f(m1[k] - M);
      if (lane < 4) __builtin_amdgcn_raw_buffer_store_b64(u32x2{min(__float_as_uint(M), SENT - 1u), min(__float_as_uint(Ls), SENT - 1u)}, rws, WS_ST + g * 1024 + i * 32 + lane * 8, 0, AUX_SC1);
    }
    lds_wait_ge<4>(L + L_FLAG + 8, TRIGS);
    STAMP(10);
    {
      u32x2 st;
#pragma unroll 1
      for (unsigned spins = 0; spins < SPIN_CAP; ++spins) {
        asm volatile("" ::: "memory");
        st = __builtin_amdgcn_raw_buffer_load_b64(rws, WS_ST + g * 1024 + (i >> 2) * 128 + ln * 8, 0, AUX_SC1);
        if (!__any(max(st.x, st.y) == SENT)) break;
        __builtin_amdgcn_s_sleep(8);
      }
      const float Ms = __uint_as_float(st.x);
      float Mg = fmaxf(Ms, dppf<ROR4>(Ms)); Mg = fmaxf(Mg, dppf<ROR8>(Mg));
      float Lg = __uint_as_float(st.y) * __builtin_amdgcn_exp2f(Ms - Mg);
      Lg += dppf<ROR4>(Lg); Lg += dppf<ROR8>(Lg);
      const float inv = 1.0f / Lg;
      if (lane < 4) {
#pragma unroll
        for (int k = 0; k < 4; ++k) {
          *(LAS float*)(L + L_AL + k * 16 + lane * 4) = __builtin_amdgcn_exp2f(m0[k] - Mg) * inv;
          *(LAS float*)(L + L_AL + 64 + k * 16 + lane * 4) = __builtin_amdgcn_exp2f(m1[k] - Mg) * inv;
        }
      }
    }
    STAMP(5);
    lds_wait_ge<2>(L + L_FLAG + 12, 8u);
    {
      const int kk = lane >> 3;
      const int woff = (kk & 3) * 131072 + (kk >> 2) * 8192 + rb * 2048 + (lane & 7) * 16;
#pragma unroll
      for (int t = 0; t < 16; ++t) wv[t] = __builtin_amdgcn_raw_buffer_load_b128(ro, woff, (t & 7) * 16384 + (t >> 3) * 1024, AUX_NT);
    }
    __builtin_amdgcn_sched_barrier(0);
    __builtin_amdgcn_s_setprio(0);
    wg_barrier();
    __builtin_amdgcn_s_sleep(SLPI);
#pragma unroll
    for (int t = 0; t < 16; ++t) *(LAS u32x4*)(L + img_w + img_row(8 * t)) = wv[t];
    STAMP(11);
    wg_barrier();
    STAMP(6);
    {
      const int off = P2L ? WS_P2 + (i >> 2) * 65536 + (2 * g + ((i >> 1) & 1)) * 4096 + (lane >> 2) * 128 + (i & 1) * 64 + (lane & 3) * 16 : WS_P2 + (i >> 2) * 65536 + (4 * g + (i & 3)) * 2048 + lane * 16;
      u32x4 pr[2], pw[2];
      poll_words<2, H2S0, H2PD, H2SG>(mk_srd(a_ws, WS_END), off, pr, pw);
      STAMP(7);
      const f32x4 o = __builtin_bit_cast(f32x4, pw[0]) + __builtin_bit_cast(f32x4, pw[1]);
      float v = tsum32(tsum16(o.x, o.y), tsum16(o.z, o.w));
      v += dppf<ROR4>(v); v += dppf<ROR8>(v);
      if (ln < 4) __builtin_amdgcn_raw_buffer_store_b16((unsigned short)pack_bf16(v, v), rout, (i >> 2) * 1024 + (4 * g + (i & 3)) * 32 + ln * 8 + lg * 2, 0, AUX_OUT);
      if (blockIdx.x == 0 && lane == 0) __builtin_amdgcn_raw_buffer_store_b64(u32x2{(unsigned)tag, (unsigned)(tag >> 32)}, rws, 0, 0, AUX_SC1);
      STAMP(8);
      TEND;
      POLL_KEEP(pr[0]); POLL_KEEP(pr[1]);
    }
  } else {
    const size_t base = ((size_t)((i >> 2) * 8 + g) * 4096 + (i & 3) * 1024) * 256;
#define CHUNK(c) ((c) * 32768 + ((w + (c)) & 7) * 4096)
    const int voff = lane * 16;
    const __amdgpu_buffer_rsrc_t rk = mk_rsrc((const unsigned char*)a_k + base, 262144), rv = mk_rsrc((const unsigned char*)a_v + base, 262144);
    const __amdgpu_buffer_rsrc_t rws = mk_rsrc((const void*)a_ws, WS_END);
    u32x4 kr[32], vr[32];
    const __amdgpu_buffer_rsrc_t rq = mk_rsrc((const void*)a_wq, 4194304), rx = mk_rsrc((const void*)a_x, 8192);
    const int qs = kq * 1048576 + gq * 1024 + cq * 128 + w * 131072;
    const u32x4 wq0 = __builtin_amdgcn_raw_buffer_load_b128(rq, qoff, qs, AUX_NT);
    const u32x4 wq1 = __builtin_amdgcn_raw_buffer_load_b128(rq, qoff, qs + 65536, AUX_NT);
    const u32x4 xq = __builtin_amdgcn_raw_buffer_load_b128(rx, xoff, w * 1024, 0);
#pragma unroll
    for (int n = 0; n < DINIT; ++n) kr[n] = __builtin_amdgcn_raw_buffer_load_b128(rk, koff + (n & 3) * 1024, CHUNK(n >> 2), AUX_NT);
    __builtin_amdgcn_sched_barrier(0);
    STAMP(8);
    unsigned tref = t0;
    int lag = 0;
#define ISSUE(n) do { \
      __builtin_amdgcn_s_waitcnt(0x0F70 | (DHI - 1)); \
      if ((n) < NBURST && lag < EHI) { __builtin_amdgcn_s_waitcnt(0x0F70 | (DMID - 1)); if ((n) >= NTH && (n) < NEND && lag < ELO) __builtin_amdgcn_s_waitcnt(0x0F70 | (DLO - 1)); } \
      if ((n) < 32) kr[(n) & 31] = __builtin_amdgcn_raw_buffer_load_b128(rk, koff + ((n) & 3) * 1024, CHUNK(((n) & 31) >> 2), AUX_NT); \
      else vr[(n) & 31] = __builtin_amdgcn_raw_buffer_load_b128(rv, voff + ((n) & 3) * 1024, CHUNK(((n) & 31) >> 2), AUX_NT); \
      __builtin_amdgcn_sched_barrier(0); \
      if (((n) & (EVAL - 1)) == 0) { \
        if (CREF && (n) >= 24 && (n) <= PARK + 4) tref = __builtin_amdgcn_readfirstlane(*(volatile LAS unsigned*)(L + L_FLAG + 32)); \
        lag = (int)((unsigned)__builtin_amdgcn_s_memrealtime() - tref) - (TOFF + ((n) * TAU) / 10); } \
      if ((n) == TRIGQ || (n) == TRIGS) *(volatile LAS unsigned*)(L + L_FLAG + 8) = (n); \
      if ((n) == 63 && lane == 0) __hip_atomic_fetch_add((LAS unsigned*)(L + L_FLAG + 12), 1u, __ATOMIC_RELAXED, __HIP_MEMORY_SCOPE_WORKGROUP); \
      __builtin_amdgcn_sched_barrier(0); } while (0)
#pragma unroll
    for (int n = DINIT; n < PARK; ++n) {
      if (n == DINIT) {
        *(LAS u32x4*)(L + img_w + w * 512) = wq0;
        *(LAS u32x4*)(L + img_w + w * 512 + 128) = wq1;
        *(LAS u32x4*)(L + L_X + w * 272 + ln * 16) = xq;
        asm volatile("s_waitcnt lgkmcnt(0)" ::: "memory");
        if (lane == 0) __hip_atomic_fetch_add((LAS unsigned*)(L + L_FLAG + 16), 1u, __ATOMIC_RELAXED, __HIP_MEMORY_SCOPE_WORKGROUP);
        __builtin_amdgcn_sched_barrier(0);
      }
      ISSUE(n);
      if (n >= 12 && n < 12 + SPILL) { *(LAS u32x4*)(L + L_SP + (w * SPILL + (n - 12)) * 1024 + voff) = kr[n - 12]; __builtin_amdgcn_sched_barrier(0); }
      if (n == 8) STAMP(10);
      if (n == 14) STAMP(11);
    }
    STAMP(1);
    lds_wait_ge<1>(L + L_FLAG, 1u);
    STAMP(2);
    const u32x4 qf = *(LAS u32x4*)(L + L_Q + (lane & 3) * 256 + (4 * lg + ((lane >> 2) & 3)) * 16);
    float sc[8];
    constexpr int NQ = PARK + 4;
#pragma unroll
    for (int uu = 0; uu < 32; uu += 4) {
      const int u0 = (uu + SPILL) & 31;
      const int t = u0 >> 3;
      f32x4 d[4];
      float y[4];
#pragma unroll
      for (int k = 0; k < 4; ++k) d[k] = mfma(u0 + k < SPILL ? *(LAS u32x4*)(L + L_SP + (w * SPILL + u0 + k) * 1024 + voff) : kr[u0 + k], qf, f32x4{0.f, 0.f, 0.f, 0.f});
      __builtin_amdgcn_sched_barrier(0);
#pragma unroll
      for (int k = 0; k < 4; ++k) y[k] = d[k][t] + dppf<ROR12>(d[k][(t + 1) & 3]);
      __builtin_amdgcn_sched_barrier(0);
#pragma unroll
      for (int k = 0; k < 4; ++k) y[k] += dppf<ROR8>(d[k][(t + 2) & 3]);
      __builtin_amdgcn_sched_barrier(0);
#pragma unroll
      for (int k = 0; k < 4; ++k) y[k] += dppf<ROR4>(d[k][(t + 3) & 3]);
      __builtin_amdgcn_sched_barrier(0);
#pragma unroll
      for (int k = 0; k < 4; ++k) {
        const int T = (u0 + k) & 7;
        sc[T] = uu < 8 ? y[k] : t == 0 ? bank_set<1>(sc[T], y[k]) : t == 1 ? bank_set<2>(sc[T], y[k]) : t == 2 ? bank_set<4>(sc[T], y[k]) : bank_set<8>(sc[T], y[k]);
      }
      __builtin_amdgcn_sched_barrier(0);
      if ((uu & 4) == 0) ISSUE(PARK + (uu >> 3));
    }
    float m = fmaxf(fmaxf(fmaxf(sc[0], sc[1]), fmaxf(sc[2], sc[3])), fmaxf(fmaxf(sc[4], sc[5]), fmaxf(sc[6], sc[7])));
    m = fmaxf(m, dppf<ROR4>(m)); m = fmaxf(m, dppf<ROR8>(m)); m = x32max(x16max(m));
    m *= C2;
    float ls = 0.f;
#pragma unroll
    for (int T = 0; T < 8; ++T) { sc[T] = __builtin_amdgcn_exp2f(fmaf(sc[T], C2, -m)); ls += sc[T]; }
    ls += dppf<ROR4>(ls); ls += dppf<ROR8>(ls); ls = x32sum(x16sum(ls));
    if (lane < 4) { *(LAS float*)(L + L_ML + lane * 32 + w * 4) = m; *(LAS float*)(L + L_ML + 128 + lane * 32 + w * 4) = ls; }
    asm volatile("s_waitcnt lgkmcnt(0)" ::: "memory");
    if (lane == 0) __hip_atomic_fetch_add((LAS unsigned*)(L + L_FLAG + 4), 1u, __ATOMIC_RELAXED, __HIP_MEMORY_SCOPE_WORKGROUP);
    STAMP(3);
    u32x4 pa;
#pragma unroll
    for (int k = 0; k < 4; ++k) pa[k] = pack_bf16(sc[2 * k], sc[2 * k + 1]);
    f32x4 acc[8];
#pragma unroll
    for (int j = 0; j < 8; ++j) acc[j] = f32x4{0.f, 0.f, 0.f, 0.f};
    __builtin_amdgcn_sched_barrier(0);
#pragma unroll
    for (int t = 0; t < 4; ++t) {
#pragma unroll
      for (int n = NQ; n < 64; ++n) if ((n > 31 + 8 * t + DHI && n <= 39 + 8 * t + DHI) || (t == 0 && n <= 31 + DHI)) ISSUE(n);
      u32x4 a, bb[8];
#pragma unroll
      for (int k = 0; k < 4; ++k) a[k] = t == 0 ? pa[k] : t == 1 ? dppu<SHL4>(pa[k]) : t == 2 ? dppu<SHL8>(pa[k]) : dppu<SHL12>(pa[k]);
#pragma unroll
      for (int k = 0; k < 4; ++k) {
#pragma unroll
        for (int j = 0; j < 8; ++j) bb[j][k] = __builtin_amdgcn_perm(vr[8 * t + 2 * k + 1][j >> 1], vr[8 * t + 2 * k][j >> 1], (j & 1) ? 0x07060302u : 0x05040100u);
        if (t == 3 && k < 3) __builtin_amdgcn_sched_barrier(0);
      }
#pragma unroll
      for (int j = 0; j < 8; ++j) acc[j] = mfma(a, bb[j], acc[j]);
      __builtin_amdgcn_sched_barrier(0);
    }
    if (lane < 16) {
#pragma unroll
      for (int j = 0; j < 8; ++j) *(LAS f32x4*)(L + L_RED + w * 2048 + j * 256 + lane * 16) = acc[j];
    }
    STAMP(4);
    wg_barrier();
    STAMP(5);
    if (w < 2) {
      f32x4 v = *(LAS f32x4*)(L + L_RED + tid * 16) * *(LAS f32x4*)(L + L_AL);
#pragma unroll
      for (int ww = 1; ww < 8; ++ww) v += *(LAS f32x4*)(L + L_RED + ww * 2048 + tid * 16) * *(LAS f32x4*)(L + L_AL + ww * 16);
      const unsigned h0 = min(pack_bf16(v.x, v.y), SENT - 1u), h1 = min(pack_bf16(v.z, v.w), SENT - 1u);
      const u32x4 pk = {h0, h1, dppu<XOR1>(h0), dppu<XOR1>(h1)};
      if (!(lane & 1)) __builtin_amdgcn_raw_buffer_store_b128(pk, rws, WS_P1 + g * 32768 + (tid >> 5) * 8192 + i * 256 + (tid & 31) * 8, 0, AUX_PUB);
    }
    STAMP(6);
    u32x4 pr[2];
    {
      u32x4 pw[2];
      poll_words<1, H1S0, H1PD, H1SG>(mk_srd(a_ws, WS_END), WS_P1 + g * 32768 + rb * 8192 + w * 1024 + lane * 16, pr, pw);
      const u32x4 p = pw[0];
      const float a0 = tsum16(__uint_as_float(p.x << 16), __uint_as_float(p.y << 16)), a1 = tsum16(__uint_as_float(p.x & 0xFFFF0000u), __uint_as_float(p.y & 0xFFFF0000u));
      const float a2 = tsum16(__uint_as_float(p.z << 16), __uint_as_float(p.w << 16)), a3 = tsum16(__uint_as_float(p.z & 0xFFFF0000u), __uint_as_float(p.w & 0xFFFF0000u));
      *(LAS unsigned*)(L + L_OI + w * 272 + ln * 16 + lg * 4) = pack_bf16(tsum32(a0, a2), tsum32(a1, a3));
    }
    STAMP(7);
    wg_barrier();
    if (w < 4) {
      LAS unsigned char* ip = L + L_IMG + w * 4096 + (lg >> 1) * 512 + (lg & 1) * 128 + ln * 8;
      f32x4 d0 = {0.f, 0.f, 0.f, 0.f}, d1 = {0.f, 0.f, 0.f, 0.f};
#pragma unroll
      for (int ks = 0; ks < 4; ks += 2) {
        const u32x2 a0 = tr_read(ip + ks * 1024), a1 = tr_read(ip + ks * 1024 + 256), a2 = tr_read(ip + ks * 1024 + 1024), a3 = tr_read(ip + ks * 1024 + 1280);
        d0 = mfma(u32x4{a0.x, a0.y, a1.x, a1.y}, *(LAS u32x4*)(L + L_OI + (ln & 7) * 272 + ks * 64 + lg * 16), d0);
        d1 = mfma(u32x4{a2.x, a2.y, a3.x, a3.y}, *(LAS u32x4*)(L + L_OI + (ln & 7) * 272 + ks * 64 + 64 + lg * 16), d1);
      }
      if (ln < 8) __builtin_amdgcn_raw_buffer_store_b128(no_sent(d0 + d1), rws, P2L ? WS_P2 + ln * 65536 + (cb * 2 + (w >> 1)) * 4096 + (g * 4 + rb) * 128 + (w & 1) * 64 + lg * 16 : WS_P2 + ln * 65536 + (cb * 4 + w) * 2048 + (g * 4 + rb) * 64 + lg * 16, 0, AUX_PUB);
    }
    STAMP(9);
    POLL_KEEP(pr[0]);
  }
  STAMP_OUT;
}

extern "C" void kernel_launch(void* const* d_in, const int* in_sizes, int n_in, void* d_out, int out_size, void* d_ws, size_t ws_size,
                              hipStream_t stream) {
  gqa_decode_kernel<<<256, NTHR, 0, stream>>>(d_in[1], d_in[2], d_ws, d_in[3], d_in[0], d_in[4], d_out);
}
```

```cpp
#include <hip/hip_runtime.h>
#include <hip/hip_bf16.h>

#define LAS __attribute__((address_space(3)))
typedef unsigned u32x4 __attribute__((ext_vector_type(4)));
typedef unsigned u32x2 __attribute__((ext_vector_type(2)));
typedef float f32x4 __attribute__((ext_vector_type(4)));
typedef short s16x4 __attribute__((ext_vector_type(4)));
typedef __bf16 bf16x8 __attribute__((ext_vector_type(8)));
typedef __bf16 bf16x2 __attribute__((ext_vector_type(2)));
typedef float f32x2 __attribute__((ext_vector_type(2)));
typedef unsigned long long u64;

constexpr int NTHR = 576;
constexpr int TOFF = 130, TAU = 284;
constexpr int DINIT = 4;
constexpr int DLO = 1, DMID = 3, DHI = 6;
constexpr int ELO = -40, EHI = 30;
constexpr int NTH = 16, NBOOST = 28;
constexpr int NCAL = 12, CADJ = -10;
constexpr int SPILL = 4, PARK = 36 + SPILL;
constexpr int TRIGQ = 20, TRIGS = 54;
constexpr int H1S0 = 22, H2S0 = 21;
constexpr int AUX_NT = 2, AUX_SC1 = 16;
constexpr unsigned SENT = 0xFFFFFFFFu;
constexpr unsigned SPIN_CAP = 1u << 16;
constexpr int WS_HI = 1024;
constexpr int WS_T0 = 4096;
constexpr int WS_QG = 8192;
constexpr int WS_P1 = WS_QG + 524288;
constexpr int WS_ST = WS_P1 + 262144;
constexpr int WS_P2 = WS_ST + 8192;
constexpr int WS_END = WS_P2 + 524288;
constexpr int L_IMG = 0;
constexpr int L_RED = 16384;
constexpr int L_Q = 32768;
constexpr int L_ML = L_Q + 1024;
constexpr int L_OI = L_ML + 256;
constexpr int L_AL = L_OI + 8 * 272;
constexpr int L_FLAG = L_AL + 128;
constexpr int L_X = L_FLAG + 48;
constexpr int L_SP = L_X + 8 * 272;
constexpr int L_END = L_SP + 8 * SPILL * 1024;
constexpr int F_Q = L_FLAG, F_ML = L_FLAG + 4, F_POS = L_FLAG + 8, F_LAST = L_FLAG + 12, F_WQ = L_FLAG + 16, F_AL = L_FLAG + 20, F_WO = L_FLAG + 24,
              F_REF = L_FLAG + 32, F_CAL = L_FLAG + 40;

constexpr float C2 = 0.08838834764831845f * 1.4426950408889634f;

__device__ __forceinline__ f32x4 mfma(u32x4 a, u32x4 b, f32x4 c) {
  return __builtin_amdgcn_mfma_f32_16x16x32_bf16(__builtin_bit_cast(bf16x8, a), __builtin_bit_cast(bf16x8, b), c, 0, 0, 0);
}
template <int CTRL> __device__ __forceinline__ float dppf(float x) {
  return __builtin_bit_cast(float, __builtin_amdgcn_mov_dpp(__builtin_bit_cast(int, x), CTRL, 0xf, 0xf, true));
}
template <int CTRL> __device__ __forceinline__ unsigned dppu(unsigned x) {
  return (unsigned)__builtin_amdgcn_mov_dpp((int)x, CTRL, 0xf, 0xf, true);
}
constexpr int ROR4 = 0x124, ROR8 = 0x128, ROR12 = 0x12C, QID = 0xE4, XOR1 = 0xB1, XOR2 = 0x4E;
template <int BANKS> __device__ __forceinline__ float bank_set(float old, float y) {
  return __builtin_bit_cast(float, __builtin_amdgcn_update_dpp(__builtin_bit_cast(int, old), __builtin_bit_cast(int, y), QID, 0xf, BANKS, false));
}
template <int SRC, int DST> __device__ __forceinline__ unsigned bank_move(unsigned old, unsigned y) {
  return (unsigned)__builtin_amdgcn_update_dpp((int)old, (int)y, SRC == DST ? QID : 0x120 + 4 * ((DST - SRC) & 3), 0xf, 1 << DST, false);
}
template <int SRC> __device__ __forceinline__ void spread_rows(u32x4 (&av)[4], u32x4 pa) {
#pragma unroll
  for (int k = 0; k < 4; ++k) {
    av[0][k] = bank_move<SRC, 0>(av[0][k], pa[k]); av[1][k] = bank_move<SRC, 1>(av[1][k], pa[k]);
    av[2][k] = bank_move<SRC, 2>(av[2][k], pa[k]); av[3][k] = bank_move<SRC, 3>(av[3][k], pa[k]);
  }
}
__device__ __forceinline__ float x16max(float x) {
  auto s = __builtin_amdgcn_permlane16_swap(__float_as_uint(x), __float_as_uint(x), false, false);
  return fmaxf(__uint_as_float(s[0]), __uint_as_float(s[1]));
}
__device__ __forceinline__ float x32max(float x) {
  auto s = __builtin_amdgcn_permlane32_swap(__float_as_uint(x), __float_as_uint(x), false, false);
  return fmaxf(__uint_as_float(s[0]), __uint_as_float(s[1]));
}
__device__ __forceinline__ float tsum16(float a, float b) {
  auto s = __builtin_amdgcn_permlane16_swap(__float_as_uint(a), __float_as_uint(b), false, false);
  return __uint_as_float(s[0]) + __uint_as_float(s[1]);
}
__device__ __forceinline__ float tsum32(float a, float b) {
  auto s = __builtin_amdgcn_permlane32_swap(__float_as_uint(a), __float_as_uint(b), false, false);
  return __uint_as_float(s[0]) + __uint_as_float(s[1]);
}
__device__ __forceinline__ unsigned pack_bf16(float lo, float hi) {
  f32x2 v = {lo, hi};
  return __builtin_bit_cast(unsigned, __builtin_convertvector(v, bf16x2));
}
__device__ __forceinline__ unsigned no_sent(unsigned u) { return min(u, SENT - 1u); }
__device__ __forceinline__ bool has_sent(u32x4 u) { return max(max(u.x, u.y), max(u.z, u.w)) == SENT; }
__device__ __forceinline__ void wg_barrier() { asm volatile("s_waitcnt lgkmcnt(0)\n\ts_barrier" ::: "memory"); }
__device__ __forceinline__ void lds_done() { asm volatile("s_waitcnt lgkmcnt(0)" ::: "memory"); }
__device__ __forceinline__ __amdgpu_buffer_rsrc_t mk_rsrc(u64 p, int bytes) { return __builtin_amdgcn_make_buffer_rsrc((void*)p, 0, bytes, 0x00020000); }
__device__ __forceinline__ u32x4 mk_srd(u64 p, int bytes) { return u32x4{(unsigned)p, (unsigned)(p >> 32) & 0xFFFFu, (unsigned)bytes, 0x00020000u}; }
__device__ __forceinline__ void poll1(u32x4 srd, int off, u32x4& r) {
  u64 m = ~0ull;
#pragma unroll 1
  for (unsigned spins = 0; spins < SPIN_CAP; ++spins) {
    asm volatile("s_mov_b64 exec, %2\n\tbuffer_load_dwordx4 %0, %1, %3, 0 offen sc1\n\ts_mov_b64 exec, -1\n\ts_waitcnt vmcnt(0)"
                 : "+v"(r) : "v"(off), "s"(m), "s"(srd) : "memory");
    m = __builtin_amdgcn_ballot_w64(has_sent(r));
    if (m == 0ull) break;
  }
}
__device__ __forceinline__ void poll2(u32x4 srd, int off, u32x4& r0, u32x4& r1) {
  u64 m0 = ~0ull, m1 = ~0ull;
#pragma unroll 1
  for (unsigned spins = 0; spins < SPIN_CAP; ++spins) {
    asm volatile("s_mov_b64 exec, %3\n\tbuffer_load_dwordx4 %0, %2, %5, 0 offen sc1\n\ts_mov_b64 exec, %4\n\tbuffer_load_dwordx4 %1, %2, %5, 0 offen offset:1024 sc1\n\t"
                 "s_mov_b64 exec, -1\n\ts_waitcnt vmcnt(0)"
                 : "+v"(r0), "+v"(r1) : "v"(off), "s"(m0 | 1ull), "s"(m1 | 1ull), "s"(srd) : "memory");
    m0 = __builtin_amdgcn_ballot_w64(has_sent(r0));
    m1 = __builtin_amdgcn_ballot_w64(has_sent(r1));
    if ((m0 | m1) == 0ull) break;
  }
}
__device__ __forceinline__ u32x4 tr_frag(LAS unsigned char* p) {
  const u32x2 lo = __builtin_bit_cast(u32x2, __builtin_amdgcn_ds_read_tr16_b64_v4i16((LAS s16x4*)p));
  const u32x2 hi = __builtin_bit_cast(u32x2, __builtin_amdgcn_ds_read_tr16_b64_v4i16((LAS s16x4*)(p + 256)));
  return u32x4{lo.x, lo.y, hi.x, hi.y};
}
__device__ __forceinline__ int img_row(int k) { return (k & ~12) * 32 + ((k >> 2) & 1) * 256 + ((k >> 3) & 1) * 128; }
template <int SLP> __device__ __forceinline__ void lds_wait_ge(LAS unsigned char* p, unsigned want) {
#pragma unroll 1
  for (unsigned spins = 0; spins < (SPIN_CAP << 6); ++spins) {
    if ((unsigned)__builtin_amdgcn_readfirstlane(*(volatile LAS unsigned*)p) >= want) break;
    __builtin_amdgcn_s_sleep(SLP);
  }
  asm volatile("" ::: "memory");
}
__device__ __forceinline__ void lds_count(LAS unsigned char* p) { __hip_atomic_fetch_add((LAS unsigned*)p, 1u, __ATOMIC_RELAXED, __HIP_MEMORY_SCOPE_WORKGROUP); }
typedef __attribute__((address_space(4))) const u64 karg_t;
__device__ __forceinline__ karg_t* kargs() { karg_t* p = (karg_t*)__builtin_amdgcn_kernarg_segment_ptr(); asm volatile("" : "+s"(p)); return p; }
#define SB() __builtin_amdgcn_sched_barrier(0)

__global__ void __launch_bounds__(NTHR, 3)
gqa_decode_kernel(const void* kc_, const void* vc_, void* ws_, const void* wq_, const void* x_, const void* wo_, void* out_) {
  karg_t* ka = kargs();
  const u64 a_k = ka[0], a_v = ka[1], a_ws = ka[2], a_wq = ka[3], a_x = ka[4];
  const unsigned t0 = (unsigned)__builtin_amdgcn_s_memrealtime();
  SB();
  __shared__ __attribute__((aligned(16))) unsigned char smem[L_END];
  LAS unsigned char* L = (LAS unsigned char*)smem;
  const int tid = threadIdx.x, lane = tid & 63, w = __builtin_amdgcn_readfirstlane(tid >> 6);
  const int g = blockIdx.x & 7, i = blockIdx.x >> 3;
  const int lg = lane >> 4, ln = lane & 15;
  const int rb = i >> 3, cb = i & 7;
  const int gq = i & 7, kq = i >> 3, cq = (g + gq) & 7;
  const int img_w = L_IMG + ((lane & 7) >> 1) * 4096 + img_row(lane >> 3) + (lane & 1) * 16;
  int koff = ((lane >> 2) & 3) * 256 + lg * 64 + (lane & 3) * 16;
  int qoff = (lane >> 3) * 8192 + (lane & 7) * 16, xoff = kq * 256 + ln * 16;
  asm volatile("" : "+v"(koff), "+v"(qoff), "+v"(xoff));
  if (tid < 12) *(LAS unsigned*)(L + L_FLAG + tid * 4) = tid == 8 ? t0 : 0u;
  wg_barrier();

  if (__builtin_expect(w >= 8, 0)) {
    __builtin_amdgcn_s_setprio(3);
    const __amdgpu_buffer_rsrc_t rws = mk_rsrc(a_ws, WS_END);
    u32x4 wv[16];
    const u32x2 ep = __builtin_amdgcn_raw_buffer_load_b64(rws, 0, 0, AUX_SC1);
    const int sl = cq * 4 + kq;
    {
      const u32x4 sv = {SENT, SENT, SENT, SENT};
      __builtin_amdgcn_raw_buffer_store_b128(sv, rws, WS_P1 + gq * 32768 + sl * 1024 + lane * 16, 0, AUX_SC1);
      if (lane < 2) __builtin_amdgcn_raw_buffer_store_b128(sv, rws, WS_ST + gq * 1024 + sl * 32 + lane * 16, 0, AUX_SC1);
      const int o2 = WS_P2 + lg * 65536 + (cb * 4 + ((lane >> 2) & 3)) * 2048 + (g * 4 + rb) * 64 + (lane & 3) * 16;
      __builtin_amdgcn_raw_buffer_store_b128(sv, rws, o2, 0, AUX_SC1);
      __builtin_amdgcn_raw_buffer_store_b128(sv, rws, o2 + 4 * 65536, 0, AUX_SC1);
    }
    SB();
    lds_wait_ge<1>(L + F_WQ, 8u);
    const u64 tag = (((u64)ep.y << 32) | ep.x) + 1ull;
    const u32x2 tagv = {(unsigned)tag, (unsigned)(tag >> 32)};
    {
      LAS unsigned char* ip = L + L_IMG + (lg >> 1) * 512 + (lg & 1) * 128 + ln * 8;
      LAS unsigned char* xp = L + L_X + (ln & 7) * 272 + lg * 16;
      f32x4 d[4];
#pragma unroll
      for (int ct = 0; ct < 4; ++ct) d[ct] = f32x4{0.f, 0.f, 0.f, 0.f};
#pragma unroll
      for (int ks = 0; ks < 4; ++ks) {
        const u32x4 xb = *(LAS u32x4*)(xp + ks * 64);
#pragma unroll
        for (int ct = 0; ct < 4; ++ct) d[ct] = mfma(tr_frag(ip + ct * 4096 + ks * 1024), xb, d[ct]);
      }
      const int off = WS_QG + gq * 65536 + (ln & 7) * 8192 + kq * 2048 + cq * 256 + (ln >> 3) * 128 + lg * 16;
      __builtin_amdgcn_raw_buffer_store_b128(__builtin_bit_cast(u32x4, ln < 8 ? d[0] : d[2]), rws, off, 0, AUX_SC1);
      __builtin_amdgcn_raw_buffer_store_b128(__builtin_bit_cast(u32x4, ln < 8 ? d[1] : d[3]), rws, off + 64, 0, AUX_SC1);
      asm volatile("s_waitcnt vmcnt(0)" ::: "memory");
      if (lane == 0) __builtin_amdgcn_raw_buffer_store_b64(tagv, rws, WS_HI + gq * 256 + sl * 8, 0, AUX_SC1);
    }
    lds_wait_ge<4>(L + F_CAL, 1u);
    if (lane == 0) __builtin_amdgcn_raw_buffer_store_b64(u32x2{(*(volatile LAS unsigned*)(L + F_CAL + 4) << 16) | (t0 & 0xFFFFu), tagv.x}, rws, WS_T0 + gq * 256 + sl * 8, 0, AUX_SC1);
    const __amdgpu_buffer_rsrc_t ro = mk_rsrc(kargs()[5] + g * 524288 + cb * 128, 524288 - 1024 + 128), rout = mk_rsrc(kargs()[6], 8192);
    lds_wait_ge<4>(L + F_POS, TRIGQ);
    {
#pragma unroll 1
      for (unsigned spins = 0; spins < SPIN_CAP; ++spins) {
        asm volatile("" ::: "memory");
        const u32x2 hi = __builtin_amdgcn_raw_buffer_load_b64(rws, WS_HI + g * 256 + (lane & 31) * 8, 0, AUX_SC1);
        if (!__any(((hi.x ^ tagv.x) | (hi.y ^ tagv.y)) != 0u)) break;
        __builtin_amdgcn_s_sleep(8);
      }
      asm volatile("" ::: "memory");
      const int off = WS_QG + g * 65536 + (i >> 2) * 8192 + lane * 16;
#pragma unroll
      for (int t = 0; t < 8; ++t) wv[t] = __builtin_amdgcn_raw_buffer_load_b128(rws, off + (t & 1) * 1024, (t >> 1) * 2048, AUX_SC1);
      const u32x2 o = __builtin_amdgcn_raw_buffer_load_b64(rws, WS_T0 + g * 256 + (lane & 31) * 8, 0, AUX_SC1);
#pragma unroll
      for (int hh = 0; hh < 2; ++hh) {
        const f32x4 q4 = (__builtin_bit_cast(f32x4, wv[hh]) + __builtin_bit_cast(f32x4, wv[2 + hh])) + (__builtin_bit_cast(f32x4, wv[4 + hh]) + __builtin_bit_cast(f32x4, wv[6 + hh]));
        *(LAS u32x2*)(L + L_Q + hh * 512 + lane * 8) = u32x2{pack_bf16(q4.x, q4.y), pack_bf16(q4.z, q4.w)};
      }
      const bool ok = o.y == tagv.x;
      int dt = ok ? (int)(short)((lane < 32 ? o.x : o.x >> 16) - t0) : 0;
      dt += (int)dppu<XOR1>(dt); dt += (int)dppu<XOR2>(dt); dt += (int)dppu<ROR4>(dt); dt += (int)dppu<ROR8>(dt);
      const int cn = __builtin_popcount((unsigned)__builtin_amdgcn_ballot_w64(ok));
      const int s0 = __builtin_amdgcn_readlane(dt, 0) + __builtin_amdgcn_readlane(dt, 16), s1 = __builtin_amdgcn_readlane(dt, 32) + __builtin_amdgcn_readlane(dt, 48);
      const float mean = (float)max(s0, s1 + CADJ * cn) * __builtin_amdgcn_rcpf((float)cn);
      if (lane == 0 && cn >= 16) *(volatile LAS unsigned*)(L + F_REF) = t0 + (unsigned)(int)mean;
      lds_done();
      *(volatile LAS unsigned*)(L + F_Q) = 1u;
    }
    lds_wait_ge<4>(L + F_ML, 8u);
    const f32x4 m0 = *(LAS f32x4*)(L + L_ML + (lane & 3) * 32), m1 = *(LAS f32x4*)(L + L_ML + (lane & 3) * 32 + 16);
    {
      const f32x4 l0 = *(LAS f32x4*)(L + L_ML + 128 + (lane & 3) * 32), l1 = *(LAS f32x4*)(L + L_ML + 128 + (lane & 3) * 32 + 16);
      const float M = fmaxf(fmaxf(fmaxf(m0.x, m0.y), fmaxf(m0.z, m0.w)), fmaxf(fmaxf(m1.x, m1.y), fmaxf(m1.z, m1.w)));
      float Ls = 0.f;
#pragma unroll
      for (int k = 0; k < 4; ++k) Ls += l0[k] * __builtin_amdgcn_exp2f(m0[k] - M) + l1[k] * __builtin_amdgcn_exp2f(m1[k] - M);
      if (lane < 4) __builtin_amdgcn_raw_buffer_store_b64(u32x2{no_sent(__float_as_uint(M)), no_sent(__float_as_uint(Ls))}, rws, WS_ST + g * 1024 + i * 32 + lane * 8, 0, AUX_SC1);
    }
    lds_wait_ge<4>(L + F_POS, TRIGS);
    {
      u32x2 st;
#pragma unroll 1
      for (unsigned spins = 0; spins < SPIN_CAP; ++spins) {
        asm volatile("" ::: "memory");
        st = __builtin_amdgcn_raw_buffer_load_b64(rws, WS_ST + g * 1024 + (i >> 2) * 128 + ln * 8, 0, AUX_SC1);
        if (!__any(max(st.x, st.y) == SENT)) break;
        __builtin_amdgcn_s_sleep(8);
      }
      const float Ms = __uint_as_float(st.x);
      float Mg = fmaxf(Ms, dppf<ROR4>(Ms)); Mg = fmaxf(Mg, dppf<ROR8>(Mg));
      float Lg = __uint_as_float(st.y) * __builtin_amdgcn_exp2f(Ms - Mg);
      Lg += dppf<ROR4>(Lg); Lg += dppf<ROR8>(Lg);
      const float inv = 1.0f / Lg;
      if (lane < 4) {
#pragma unroll
        for (int k = 0; k < 4; ++k) {
          *(LAS float*)(L + L_AL + k * 16 + lane * 4) = __builtin_amdgcn_exp2f(m0[k] - Mg) * inv;
          *(LAS float*)(L + L_AL + 64 + k * 16 + lane * 4) = __builtin_amdgcn_exp2f(m1[k] - Mg) * inv;
        }
      }
      lds_done();
      *(volatile LAS unsigned*)(L + F_AL) = 1u;
    }
    lds_wait_ge<2>(L + F_LAST, 8u);
    {
      const int kk = lane >> 3;
      const int woff = (kk & 3) * 131072 + (kk >> 2) * 8192 + rb * 2048 + (lane & 7) * 16;
#pragma unroll
      for (int t = 0; t < 16; ++t) wv[t] = __builtin_amdgcn_raw_buffer_load_b128(ro, woff, (t & 7) * 16384 + (t >> 3) * 1024, AUX_NT);
    }
    int off2 = WS_P2 + (i >> 2) * 65536 + (4 * g + (i & 3)) * 2048 + lane * 16, offo = (i >> 2) * 1024 + (4 * g + (i & 3)) * 32 + ln * 8 + lg * 2;
    asm volatile("" : "+v"(off2), "+v"(offo));
    SB();
    __builtin_amdgcn_s_setprio(0);
    wg_barrier();
    __builtin_amdgcn_s_sleep(6);
#pragma unroll
    for (int t = 0; t < 16; ++t) *(LAS u32x4*)(L + img_w + img_row(8 * t)) = wv[t];
    lds_done();
    *(volatile LAS unsigned*)(L + F_WO) = 1u;
    wg_barrier();
    {
      u32x4 p0 = {SENT, SENT, SENT, SENT}, p1 = p0;
      __builtin_amdgcn_s_sleep(H2S0);
      poll2(mk_srd(a_ws, WS_END), off2, p0, p1);
      const f32x4 o = __builtin_bit_cast(f32x4, p0) + __builtin_bit_cast(f32x4, p1);
      float v = tsum32(tsum16(o.x, o.y), tsum16(o.z, o.w));
      v += dppf<ROR4>(v); v += dppf<ROR8>(v);
      if (ln < 4) __builtin_amdgcn_raw_buffer_store_b16((unsigned short)pack_bf16(v, v), rout, offo, 0, 0);
      if (blockIdx.x == 0 && lane == 0) __builtin_amdgcn_raw_buffer_store_b64(tagv, rws, 0, 0, AUX_SC1);
    }
  } else {
    const size_t base = ((size_t)((i >> 2) * 8 + g) * 4096 + (i & 3) * 1024) * 256;
#define CHUNK(c) ((c) * 32768 + ((w + (c)) & 7) * 4096)
    const int voff = lane * 16;
    const __amdgpu_buffer_rsrc_t rk = mk_rsrc(a_k + base, 262144), rv = mk_rsrc(a_v + base, 262144), rws = mk_rsrc(a_ws, WS_END);
    u32x4 kr[32], vr[32];
    const __amdgpu_buffer_rsrc_t rq = mk_rsrc(a_wq, 4194304), rx = mk_rsrc(a_x, 8192);
    const int qs = kq * 1048576 + gq * 1024 + cq * 128 + w * 131072;
    const u32x4 wq0 = __builtin_amdgcn_raw_buffer_load_b128(rq, qoff, qs, AUX_NT);
    const u32x4 wq1 = __builtin_amdgcn_raw_buffer_load_b128(rq, qoff, qs + 65536, AUX_NT);
    const u32x4 xq = __builtin_amdgcn_raw_buffer_load_b128(rx, xoff, w * 1024, 0);
#pragma unroll
    for (int n = 0; n < DINIT; ++n) kr[n] = __builtin_amdgcn_raw_buffer_load_b128(rk, koff + (n & 3) * 1024, CHUNK(n >> 2), AUX_NT);
    SB();
    *(LAS u32x4*)(L + img_w + w * 512) = wq0;
    *(LAS u32x4*)(L + img_w + w * 512 + 128) = wq1;
    *(LAS u32x4*)(L + L_X + w * 272 + ln * 16) = xq;
    lds_done();
    if (lane == 0) lds_count(L + F_WQ);
    SB();
    unsigned tref = t0;
    int lag = 0;
#define ISSUE(n) do { \
      __builtin_amdgcn_s_waitcnt(0x0F70 | (DHI - 1)); \
      if ((n) < NBOOST || lag < EHI) { __builtin_amdgcn_s_waitcnt(0x0F70 | (DMID - 1)); if ((n) >= NTH && lag < ELO) __builtin_amdgcn_s_waitcnt(0x0F70 | (DLO - 1)); } \
      if ((n) < 32) kr[(n) & 31] = __builtin_amdgcn_raw_buffer_load_b128(rk, koff + ((n) & 3) * 1024, CHUNK(((n) & 31) >> 2), AUX_NT); \
      else vr[(n) & 31] = __builtin_amdgcn_raw_buffer_load_b128(rv, voff + ((n) & 3) * 1024, CHUNK(((n) & 31) >> 2), AUX_NT); \
      SB(); \
      if (((n) & 1) == 0 && (n) >= NCAL) { \
        if ((n) >= 24 && (n) <= PARK + 4) tref = __builtin_amdgcn_readfirstlane(*(volatile LAS unsigned*)(L + F_REF)); \
        lag = (int)((unsigned)__builtin_amdgcn_s_memrealtime() - tref) - (TOFF + ((n) * TAU) / 10); \
        if ((n) == NCAL && w == 3) *(volatile LAS u32x2*)(L + F_CAL) = u32x2{1u, tref + (unsigned)lag}; } \
      if ((n) == TRIGQ || (n) == TRIGS) *(volatile LAS unsigned*)(L + F_POS) = (n); \
      if ((n) == 63 && lane == 0) lds_count(L + F_LAST); \
      SB(); } while (0)
#pragma unroll
    for (int n = DINIT; n < PARK; ++n) {
      ISSUE(n);
      if (n >= 12 && n < 12 + SPILL) { *(LAS u32x4*)(L + L_SP + (w * SPILL + (n - 12)) * 1024 + voff) = kr[n - 12]; SB(); }
    }
    lds_wait_ge<1>(L + F_Q, 1u);
    const u32x4 qf = *(LAS u32x4*)(L + L_Q + (lane & 3) * 256 + (4 * lg + ((lane >> 2) & 3)) * 16);
    float sc[8];
    constexpr int NQ = PARK + 4;
#pragma unroll
    for (int uu = 0; uu < 32; uu += 4) {
      const int u0 = (uu + SPILL) & 31, t = u0 >> 3;
      f32x4 d[4];
      float y[4];
#pragma unroll
      for (int k = 0; k < 4; ++k) d[k] = mfma(u0 + k < SPILL ? *(LAS u32x4*)(L + L_SP + (w * SPILL + u0 + k) * 1024 + voff) : kr[u0 + k], qf, f32x4{0.f, 0.f, 0.f, 0.f});
      SB();
#pragma unroll
      for (int k = 0; k < 4; ++k) y[k] = d[k][t] + dppf<ROR12>(d[k][(t + 1) & 3]);
      SB();
#pragma unroll
      for (int k = 0; k < 4; ++k) y[k] += dppf<ROR8>(d[k][(t + 2) & 3]);
      SB();
#pragma unroll
      for (int k = 0; k < 4; ++k) y[k] += dppf<ROR4>(d[k][(t + 3) & 3]);
      SB();
#pragma unroll
      for (int k = 0; k < 4; ++k) {
        const int T = (u0 + k) & 7;
        sc[T] = uu < 8 ? y[k] : t == 0 ? bank_set<1>(sc[T], y[k]) : t == 1 ? bank_set<2>(sc[T], y[k]) : t == 2 ? bank_set<4>(sc[T], y[k]) : bank_set<8>(sc[T], y[k]);
      }
      SB();
      if ((uu & 4) == 0) ISSUE(PARK + (uu >> 3));
    }
    float m = fmaxf(fmaxf(fmaxf(sc[0], sc[1]), fmaxf(sc[2], sc[3])), fmaxf(fmaxf(sc[4], sc[5]), fmaxf(sc[6], sc[7])));
    m = fmaxf(m, dppf<ROR4>(m)); m = fmaxf(m, dppf<ROR8>(m)); m = x32max(x16max(m));
    m *= C2;
    float ls = 0.f;
#pragma unroll
    for (int T = 0; T < 8; ++T) { sc[T] = __builtin_amdgcn_exp2f(fmaf(sc[T], C2, -m)); ls += sc[T]; }
    ls += dppf<ROR4>(ls); ls += dppf<ROR8>(ls); ls = tsum16(ls, ls); ls = tsum32(ls, ls);
    if (lane < 4) { *(LAS float*)(L + L_ML + lane * 32 + w * 4) = m; *(LAS float*)(L + L_ML + 128 + lane * 32 + w * 4) = ls; }
    lds_done();
    if (lane == 0) lds_count(L + F_ML);
    u32x4 pa;
#pragma unroll
    for (int k = 0; k < 4; ++k) pa[k] = pack_bf16(sc[2 * k], sc[2 * k + 1]);
    f32x4 acc[2] = {{0.f, 0.f, 0.f, 0.f}, {0.f, 0.f, 0.f, 0.f}}, al[8];
    u32x4 av[4] = {{0u, 0u, 0u, 0u}, {0u, 0u, 0u, 0u}, {0u, 0u, 0u, 0u}, {0u, 0u, 0u, 0u}};
    int off1 = 0, offp = 0, offd = 0, offr = 0;
    SB();
#pragma unroll
    for (int t = 0; t < 4; ++t) {
#pragma unroll
      for (int n = NQ; n < 64; ++n) if ((n > 31 + 8 * t + DHI && n <= 39 + 8 * t + DHI) || (t == 0 && n <= 31 + DHI)) ISSUE(n);
      if (t == 3) {
        off1 = WS_P1 + g * 32768 + (tid >> 5) * 8192 + i * 256 + (tid & 31) * 8;
        offp = WS_P1 + g * 32768 + rb * 8192 + w * 1024 + lane * 16;
        offd = WS_P2 + ln * 65536 + (cb * 4 + w) * 2048 + (g * 4 + rb) * 64 + lg * 16;
        offr = L_RED + w * 2048 + voff;
        asm volatile("" : "+v"(off1), "+v"(offp), "+v"(offd), "+v"(offr));
        if (w < 2) {
          lds_wait_ge<1>(L + F_AL, 1u);
#pragma unroll
          for (int ww = 0; ww < 8; ++ww) al[ww] = *(LAS f32x4*)(L + L_AL + ww * 16);
          asm volatile("" : "+v"(al[0]), "+v"(al[1]), "+v"(al[2]), "+v"(al[3]), "+v"(al[4]), "+v"(al[5]), "+v"(al[6]), "+v"(al[7]));
        }
      }
      u32x4 bb[8];
      if (t == 0) spread_rows<0>(av, pa); else if (t == 1) spread_rows<1>(av, pa); else if (t == 2) spread_rows<2>(av, pa); else spread_rows<3>(av, pa);
      if (t < 3) {
#pragma unroll
        for (int j = 0; j < 8; ++j) {
#pragma unroll
          for (int k = 0; k < 4; ++k) bb[j][k] = __builtin_amdgcn_perm(vr[8 * t + 2 * k + 1][j >> 1], vr[8 * t + 2 * k][j >> 1], (j & 1) ? 0x07060302u : 0x05040100u);
          acc[j >> 2] = mfma(av[j & 3], bb[j], acc[j >> 2]);
        }
        SB();
      } else {
        const unsigned e0 = __builtin_amdgcn_perm(av[1][3], av[0][3], 0x05040100u), e1 = __builtin_amdgcn_perm(av[3][3], av[2][3], 0x05040100u);
        const unsigned f0 = __builtin_amdgcn_perm(av[1][3], av[0][3], 0x07060302u), f1 = __builtin_amdgcn_perm(av[3][3], av[2][3], 0x07060302u);
        u32x4 s0 = {e0, e1, 0u, 0u}, s1 = {0u, 0u, e0, e1}, s2 = {f0, f1, 0u, 0u}, s3 = {0u, 0u, f0, f1};
        asm volatile("" : "+v"(s0), "+v"(s1), "+v"(s2), "+v"(s3));
#pragma unroll
        for (int k = 0; k < 3; ++k) {
#pragma unroll
          for (int j = 0; j < 8; ++j) bb[j][k] = __builtin_amdgcn_perm(vr[24 + 2 * k + 1][j >> 1], vr[24 + 2 * k][j >> 1], (j & 1) ? 0x07060302u : 0x05040100u);
          SB();
        }
#pragma unroll
        for (int j = 0; j < 8; ++j) { bb[j][3] = 0u; acc[j >> 2] = mfma(av[j & 3], bb[j], acc[j >> 2]); }
        SB();
        acc[0] = mfma(s0, vr[30], acc[0]); acc[1] = mfma(s1, vr[30], acc[1]);
        SB();
        acc[0] = mfma(s2, vr[31], acc[0]); acc[1] = mfma(s3, vr[31], acc[1]);
        SB();
      }
    }
    *(LAS f32x4*)(L + offr) = acc[0];
    *(LAS f32x4*)(L + offr + 1024) = acc[1];
    wg_barrier();
    if (w < 2) {
      f32x4 rd[8];
#pragma unroll
      for (int ww = 0; ww < 8; ++ww) rd[ww] = *(LAS f32x4*)(L + L_RED + ww * 2048 + tid * 16);
      SB();
      f32x4 v = rd[0] * al[0];
#pragma unroll
      for (int ww = 1; ww < 8; ++ww) v = __builtin_elementwise_fma(rd[ww], al[ww], v);
      const unsigned h0 = no_sent(pack_bf16(v.x, v.y)), h1 = no_sent(pack_bf16(v.z, v.w));
      const u32x4 pk = {h0, h1, dppu<XOR1>(h0), dppu<XOR1>(h1)};
      if (!(lane & 1)) __builtin_amdgcn_raw_buffer_store_b128(pk, rws, off1, 0, AUX_SC1);
    }
    u32x4 wa[4];
    __builtin_amdgcn_s_sleep(H1S0 - 4);
    lds_wait_ge<1>(L + F_WO, 1u);
#pragma unroll
    for (int ks = 0; ks < 4; ++ks) wa[ks] = tr_frag(L + L_IMG + (w & 3) * 4096 + (lg >> 1) * 512 + (lg & 1) * 128 + ln * 8 + ks * 1024);
    int oiw = L_OI + w * 272 + ln * 16 + lg * 4, oir = L_OI + (ln & 7) * 272 + lg * 16;
    asm volatile("" : "+v"(wa[0]), "+v"(wa[1]), "+v"(wa[2]), "+v"(wa[3]), "+v"(oiw), "+v"(oir));
    {
      u32x4 p = {SENT, SENT, SENT, SENT};
      poll1(mk_srd(a_ws, WS_END), offp, p);
      const float e0 = __uint_as_float(p.x << 16), e1 = __uint_as_float(p.x & 0xFFFF0000u), e2 = __uint_as_float(p.y << 16), e3 = __uint_as_float(p.y & 0xFFFF0000u);
      const float e4 = __uint_as_float(p.z << 16), e5 = __uint_as_float(p.z & 0xFFFF0000u), e6 = __uint_as_float(p.w << 16), e7 = __uint_as_float(p.w & 0xFFFF0000u);
      SB();
      const float a0 = tsum16(e0, e2), a1 = tsum16(e1, e3), a2 = tsum16(e4, e6), a3 = tsum16(e5, e7);
      *(LAS unsigned*)(L + oiw) = pack_bf16(tsum32(a0, a2), tsum32(a1, a3));
    }
    wg_barrier();
    if (w < 4) {
      u32x4 ob[4];
#pragma unroll
      for (int ks = 0; ks < 4; ++ks) ob[ks] = *(LAS u32x4*)(L + oir + ks * 64);
      SB();
      f32x4 d = {0.f, 0.f, 0.f, 0.f};
#pragma unroll
      for (int ks = 0; ks < 4; ++ks) d = mfma(wa[ks], ob[ks], d);
      if (ln < 8) __builtin_amdgcn_raw_buffer_store_b128(u32x4{no_sent(__float_as_uint(d.x)), no_sent(__float_as_uint(d.y)), no_sent(__float_as_uint(d.z)), no_sent(__float_as_uint(d.w))}, rws, offd, 0, AUX_SC1);
    }
  }
}

extern "C" void kernel_launch(void* const* d_in, const int* in_sizes, int n_in, void* d_out, int out_size, void* d_ws, size_t ws_size,
                              hipStream_t stream) {
  gqa_decode_kernel<<<256, NTHR, 0, stream>>>(d_in[1], d_in[2], d_ws, d_in[3], d_in[0], d_in[4], d_out);
}
```

```cpp
#define PK 1
#include <hip/hip_runtime.h>
#include <hip/hip_bf16.h>

#define LAS __attribute__((address_space(3)))
typedef unsigned u32x4 __attribute__((ext_vector_type(4)));
typedef unsigned u32x2 __attribute__((ext_vector_type(2)));
typedef float f32x4 __attribute__((ext_vector_type(4)));
typedef short s16x4 __attribute__((ext_vector_type(4)));
typedef __bf16 bf16x8 __attribute__((ext_vector_type(8)));
typedef __bf16 bf16x2 __attribute__((ext_vector_type(2)));
typedef float f32x2 __attribute__((ext_vector_type(2)));
typedef unsigned long long u64;

constexpr int NTHR = 576;
constexpr int TOFF = 100, TAU = 289;
constexpr int DINIT = 4;
constexpr int DLO = 1, DMID = 3, DHI = 6;
constexpr int ELO = -40, EHI = 30;
constexpr int NTH = 16, NBOOST = 28;
constexpr int NCAL = 12, CADJ = -10;
constexpr int SPILL = 4, PARK = 36 + SPILL;
constexpr int TRIGQ = 20, TRIGS = 54;
constexpr int H1S0 = 22, H2S0 = 21;
constexpr int AUX_NT = 2, AUX_SC1 = 16;
constexpr unsigned SENT = 0xFFFFFFFFu;
constexpr unsigned SPIN_CAP = 1u << 16;
constexpr int WS_HI = 1024;
constexpr int WS_T0 = 4096;
constexpr int WS_QG = 8192;
constexpr int WS_P1 = WS_QG + 524288;
constexpr int WS_ST = WS_P1 + 262144;
constexpr int WS_P2 = WS_ST + 8192;
constexpr int WS_END = WS_P2 + 524288;
constexpr int L_IMG = 0;
constexpr int L_RED = 16384;
constexpr int L_Q = 32768;
constexpr int L_ML = L_Q + 1024;
constexpr int L_OI = L_ML + 256;
constexpr int L_AL = L_OI + 8 * 272;
constexpr int L_FLAG = L_AL + 128;
constexpr int L_X = L_FLAG + 48;
constexpr int L_SP = L_X + 8 * 272;
constexpr int L_END = L_SP + 8 * SPILL * 1024;
constexpr int F_Q = L_FLAG, F_ML = L_FLAG + 4, F_POS = L_FLAG + 8, F_LAST = L_FLAG + 12, F_WQ = L_FLAG + 16, F_AL = L_FLAG + 20, F_WO = L_FLAG + 24,
              F_REF = L_FLAG + 32, F_CAL = L_FLAG + 40;

constexpr float C2 = 0.08838834764831845f * 1.4426950408889634f;

__device__ __forceinline__ f32x4 mfma(u32x4 a, u32x4 b, f32x4 c) {
  return __builtin_amdgcn_mfma_f32_16x16x32_bf16(__builtin_bit_cast(bf16x8, a), __builtin_bit_cast(bf16x8, b), c, 0, 0, 0);
}
template <int CTRL> __device__ __forceinline__ float dppf(float x) {
  return __builtin_bit_cast(float, __builtin_amdgcn_mov_dpp(__builtin_bit_cast(int, x), CTRL, 0xf, 0xf, true));
}
template <int CTRL> __device__ __forceinline__ unsigned dppu(unsigned x) {
  return (unsigned)__builtin_amdgcn_mov_dpp((int)x, CTRL, 0xf, 0xf, true);
}
constexpr int ROR4 = 0x124, ROR8 = 0x128, ROR12 = 0x12C, QID = 0xE4, XOR1 = 0xB1, XOR2 = 0x4E;
template <int BANKS> __device__ __forceinline__ float bank_set(float old, float y) {
  return __builtin_bit_cast(float, __builtin_amdgcn_update_dpp(__builtin_bit_cast(int, old), __builtin_bit_cast(int, y), QID, 0xf, BANKS, false));
}
template <int SRC, int DST> __device__ __forceinline__ unsigned bank_move(unsigned old, unsigned y) {
  return (unsigned)__builtin_amdgcn_update_dpp((int)old, (int)y, SRC == DST ? QID : 0x120 + 4 * ((DST - SRC) & 3), 0xf, 1 << DST, false);
}
template <int SRC> __device__ __forceinline__ void spread_rows(u32x4 (&av)[4], u32x4 pa) {
#pragma unroll
  for (int k = 0; k < 4; ++k) {
    av[0][k] = bank_move<SRC, 0>(av[0][k], pa[k]); av[1][k] = bank_move<SRC, 1>(av[1][k], pa[k]);
    av[2][k] = bank_move<SRC, 2>(av[2][k], pa[k]); av[3][k] = bank_move<SRC, 3>(av[3][k], pa[k]);
  }
}
__device__ __forceinline__ float x16max(float x) {
  auto s = __builtin_amdgcn_permlane16_swap(__float_as_uint(x), __float_as_uint(x), false, false);
  return fmaxf(__uint_as_float(s[0]), __uint_as_float(s[1]));
}
__device__ __forceinline__ float x32max(float x) {
  auto s = __builtin_amdgcn_permlane32_swap(__float_as_uint(x), __float_as_uint(x), false, false);
  return fmaxf(__uint_as_float(s[0]), __uint_as_float(s[1]));
}
__device__ __forceinline__ float tsum16(float a, float b) {
  auto s = __builtin_amdgcn_permlane16_swap(__float_as_uint(a), __float_as_uint(b), false, false);
  return __uint_as_float(s[0]) + __uint_as_float(s[1]);
}
__device__ __forceinline__ float tsum32(float a, float b) {
  auto s = __builtin_amdgcn_permlane32_swap(__float_as_uint(a), __float_as_uint(b), false, false);
  return __uint_as_float(s[0]) + __uint_as_float(s[1]);
}
__device__ __forceinline__ unsigned pack_bf16(float lo, float hi) {
  f32x2 v = {lo, hi};
  return __builtin_bit_cast(unsigned, __builtin_convertvector(v, bf16x2));
}
__device__ __forceinline__ unsigned no_sent(unsigned u) { return min(u, SENT - 1u); }
__device__ __forceinline__ bool has_sent(u32x4 u) { return max(max(u.x, u.y), max(u.z, u.w)) == SENT; }
__device__ __forceinline__ void wg_barrier() { asm volatile("s_waitcnt lgkmcnt(0)\n\ts_barrier" ::: "memory"); }
__device__ __forceinline__ void lds_done() { asm volatile("s_waitcnt lgkmcnt(0)" ::: "memory"); }
__device__ __forceinline__ __amdgpu_buffer_rsrc_t mk_rsrc(u64 p, int bytes) { return __builtin_amdgcn_make_buffer_rsrc((void*)p, 0, bytes, 0x00020000); }
__device__ __forceinline__ u32x4 mk_srd(u64 p, int bytes) { return u32x4{(unsigned)p, (unsigned)(p >> 32) & 0xFFFFu, (unsigned)bytes, 0x00020000u}; }
__device__ __forceinline__ void poll1(u32x4 srd, int off, u32x4& r) {
  u64 m = ~0ull;
#pragma unroll 1
  for (unsigned spins = 0; spins < SPIN_CAP; ++spins) {
    asm volatile("s_mov_b64 exec, %2\n\tbuffer_load_dwordx4 %0, %1, %3, 0 offen sc1\n\ts_mov_b64 exec, -1\n\ts_waitcnt vmcnt(0)"
                 : "+v"(r) : "v"(off), "s"(m), "s"(srd) : "memory");
    m = __builtin_amdgcn_ballot_w64(has_sent(r));
    if (m == 0ull) break;
  }
}
__device__ __forceinline__ void poll2(u32x4 srd, int off, u32x4& r0, u32x4& r1) {
  u64 m0 = ~0ull, m1 = ~0ull;
#pragma unroll 1
  for (unsigned spins = 0; spins < SPIN_CAP; ++spins) {
    asm volatile("s_mov_b64 exec, %3\n\tbuffer_load_dwordx4 %0, %2, %5, 0 offen sc1\n\ts_mov_b64 exec, %4\n\tbuffer_load_dwordx4 %1, %2, %5, 0 offen offset:1024 sc1\n\t"
                 "s_mov_b64 exec, -1\n\ts_waitcnt vmcnt(0)"
                 : "+v"(r0), "+v"(r1) : "v"(off), "s"(m0 | 1ull), "s"(m1 | 1ull), "s"(srd) : "memory");
    m0 = __builtin_amdgcn_ballot_w64(has_sent(r0));
    m1 = __builtin_amdgcn_ballot_w64(has_sent(r1));
    if ((m0 | m1) == 0ull) break;
  }
}
__device__ __forceinline__ u32x4 tr_frag(LAS unsigned char* p) {
  const u32x2 lo = __builtin_bit_cast(u32x2, __builtin_amdgcn_ds_read_tr16_b64_v4i16((LAS s16x4*)p));
  const u32x2 hi = __builtin_bit_cast(u32x2, __builtin_amdgcn_ds_read_tr16_b64_v4i16((LAS s16x4*)(p + 256)));
  return u32x4{lo.x, lo.y, hi.x, hi.y};
}
__device__ __forceinline__ int img_row(int k) { return (k & ~12) * 32 + ((k >> 2) & 1) * 256 + ((k >> 3) & 1) * 128; }
template <int SLP> __device__ __forceinline__ void lds_wait_ge(LAS unsigned char* p, unsigned want) {
#pragma unroll 1
  for (unsigned spins = 0; spins < (SPIN_CAP << 6); ++spins) {
    if ((unsigned)__builtin_amdgcn_readfirstlane(*(volatile LAS unsigned*)p) >= want) break;
    __builtin_amdgcn_s_sleep(SLP);
  }
  asm volatile("" ::: "memory");
}
__device__ __forceinline__ void lds_count(LAS unsigned char* p) { __hip_atomic_fetch_add((LAS unsigned*)p, 1u, __ATOMIC_RELAXED, __HIP_MEMORY_SCOPE_WORKGROUP); }
typedef __attribute__((address_space(4))) const u64 karg_t;
__device__ __forceinline__ karg_t* kargs() { karg_t* p = (karg_t*)__builtin_amdgcn_kernarg_segment_ptr(); asm volatile("" : "+s"(p)); return p; }
#define SB() __builtin_amdgcn_sched_barrier(0)

__global__ void __launch_bounds__(NTHR, 3)
gqa_decode_kernel(const void* kc_, const void* vc_, void* ws_, const void* wq_, const void* x_, const void* wo_, void* out_) {
  karg_t* ka = kargs();
  const u64 a_k = ka[0], a_v = ka[1], a_ws = ka[2], a_wq = ka[3], a_x = ka[4];
  const unsigned t0 = (unsigned)__builtin_amdgcn_s_memrealtime();
  SB();
  __shared__ __attribute__((aligned(16))) unsigned char smem[L_END];
  LAS unsigned char* L = (LAS unsigned char*)smem;
  const int tid = threadIdx.x, lane = tid & 63, w = __builtin_amdgcn_readfirstlane(tid >> 6);
  const int g = blockIdx.x & 7, i = blockIdx.x >> 3;
  const int lg = lane >> 4, ln = lane & 15;
  const int rb = i >> 3, cb = i & 7;
  const int gq = i & 7, kq = i >> 3, cq = (g + gq) & 7;
  const int img_w = L_IMG + ((lane & 7) >> 1) * 4096 + img_row(lane >> 3) + (lane & 1) * 16;
  int koff = ((lane >> 2) & 3) * 256 + lg * 64 + (lane & 3) * 16;
  int qoff = (lane >> 3) * 8192 + (lane & 7) * 16, xoff = kq * 256 + ln * 16;
  asm volatile("" : "+v"(koff), "+v"(qoff), "+v"(xoff));
  if (tid < 12) *(LAS unsigned*)(L + L_FLAG + tid * 4) = tid == 8 ? t0 : 0u;
  wg_barrier();
  const unsigned tb0_ = (unsigned)__builtin_amdgcn_s_memrealtime();

  if (__builtin_expect(w >= 8, 0)) {
    int pv_ = 0;
    if (PK == 5) pv_ = 8 * (int)(tb0_ - t0);
    __builtin_amdgcn_s_setprio(3);
    const __amdgpu_buffer_rsrc_t rws = mk_rsrc(a_ws, WS_END);
    u32x4 wv[16];
    const u32x2 ep = __builtin_amdgcn_raw_buffer_load_b64(rws, 0, 0, AUX_SC1);
    const int sl = cq * 4 + kq;
    {
      const u32x4 sv = {SENT, SENT, SENT, SENT};
      __builtin_amdgcn_raw_buffer_store_b128(sv, rws, WS_P1 + gq * 32768 + sl * 1024 + lane * 16, 0, AUX_SC1);
      if (lane < 2) __builtin_amdgcn_raw_buffer_store_b128(sv, rws, WS_ST + gq * 1024 + sl * 32 + lane * 16, 0, AUX_SC1);
      const int o2 = WS_P2 + lg * 65536 + (cb * 4 + ((lane >> 2) & 3)) * 2048 + (g * 4 + rb) * 64 + (lane & 3) * 16;
      __builtin_amdgcn_raw_buffer_store_b128(sv, rws, o2, 0, AUX_SC1);
      __builtin_amdgcn_raw_buffer_store_b128(sv, rws, o2 + 4 * 65536, 0, AUX_SC1);
    }
    SB();
    lds_wait_ge<1>(L + F_WQ, 8u);
    if (PK == 6) pv_ = 4 * (int)((unsigned)__builtin_amdgcn_s_memrealtime() - t0);
    const u64 tag = (((u64)ep.y << 32) | ep.x) + 1ull;
    const u32x2 tagv = {(unsigned)tag, (unsigned)(tag >> 32)};
    {
      LAS unsigned char* ip = L + L_IMG + (lg >> 1) * 512 + (lg & 1) * 128 + ln * 8;
      LAS unsigned char* xp = L + L_X + (ln & 7) * 272 + lg * 16;
      f32x4 d[4];
#pragma unroll
      for (int ct = 0; ct < 4; ++ct) d[ct] = f32x4{0.f, 0.f, 0.f, 0.f};
#pragma unroll
      for (int ks = 0; ks < 4; ++ks) {
        const u32x4 xb = *(LAS u32x4*)(xp + ks * 64);
#pragma unroll
        for (int ct = 0; ct < 4; ++ct) d[ct] = mfma(tr_frag(ip + ct * 4096 + ks * 1024), xb, d[ct]);
      }
      const int off = WS_QG + gq * 65536 + (ln & 7) * 8192 + kq * 2048 + cq * 256 + (ln >> 3) * 128 + lg * 16;
      __builtin_amdgcn_raw_buffer_store_b128(__builtin_bit_cast(u32x4, ln < 8 ? d[0] : d[2]), rws, off, 0, AUX_SC1);
      __builtin_amdgcn_raw_buffer_store_b128(__builtin_bit_cast(u32x4, ln < 8 ? d[1] : d[3]), rws, off + 64, 0, AUX_SC1);
      asm volatile("s_waitcnt vmcnt(0)" ::: "memory");
      if (lane == 0) __builtin_amdgcn_raw_buffer_store_b64(tagv, rws, WS_HI + gq * 256 + sl * 8, 0, AUX_SC1);
    }
    lds_wait_ge<4>(L + F_CAL, 1u);
    if (lane == 0) __builtin_amdgcn_raw_buffer_store_b64(u32x2{(*(volatile LAS unsigned*)(L + F_CAL + 4) << 16) | (t0 & 0xFFFFu), tagv.x}, rws, WS_T0 + gq * 256 + sl * 8, 0, AUX_SC1);
    const __amdgpu_buffer_rsrc_t ro = mk_rsrc(kargs()[5] + g * 524288 + cb * 128, 524288 - 1024 + 128), rout = mk_rsrc(kargs()[6], 8192);
    lds_wait_ge<4>(L + F_POS, TRIGQ);
    {
#pragma unroll 1
      for (unsigned spins = 0; spins < SPIN_CAP; ++spins) {
        asm volatile("" ::: "memory");
        const u32x2 hi = __builtin_amdgcn_raw_buffer_load_b64(rws, WS_HI + g * 256 + (lane & 31) * 8, 0, AUX_SC1);
        if (!__any(((hi.x ^ tagv.x) | (hi.y ^ tagv.y)) != 0u)) break;
        __builtin_amdgcn_s_sleep(8);
      }
      asm volatile("" ::: "memory");
      const int off = WS_QG + g * 65536 + (i >> 2) * 8192 + lane * 16;
#pragma unroll
      for (int t = 0; t < 8; ++t) wv[t] = __builtin_amdgcn_raw_buffer_load_b128(rws, off + (t & 1) * 1024, (t >> 1) * 2048, AUX_SC1);
      const u32x2 o = __builtin_amdgcn_raw_buffer_load_b64(rws, WS_T0 + g * 256 + (lane & 31) * 8, 0, AUX_SC1);
#pragma unroll
      for (int hh = 0; hh < 2; ++hh) {
        const f32x4 q4 = (__builtin_bit_cast(f32x4, wv[hh]) + __builtin_bit_cast(f32x4, wv[2 + hh])) + (__builtin_bit_cast(f32x4, wv[4 + hh]) + __builtin_bit_cast(f32x4, wv[6 + hh]));
        *(LAS u32x2*)(L + L_Q + hh * 512 + lane * 8) = u32x2{pack_bf16(q4.x, q4.y), pack_bf16(q4.z, q4.w)};
      }
      const bool ok = o.y == tagv.x;
      int dt = ok ? (int)(short)((lane < 32 ? o.x : o.x >> 16) - t0) : 0;
      dt += (int)dppu<XOR1>(dt); dt += (int)dppu<XOR2>(dt); dt += (int)dppu<ROR4>(dt); dt += (int)dppu<ROR8>(dt);
      const int cn = __builtin_popcount((unsigned)__builtin_amdgcn_ballot_w64(ok));
      const int s0 = __builtin_amdgcn_readlane(dt, 0) + __builtin_amdgcn_readlane(dt, 16), s1 = __builtin_amdgcn_readlane(dt, 32) + __builtin_amdgcn_readlane(dt, 48);
      const float mean = (float)max(s0, s1 + CADJ * cn) * __builtin_amdgcn_rcpf((float)cn);
      if (PK == 3) pv_ = 8 * max(0, (s1 - s0) / max(cn, 1) + 50);
      if (PK == 4) pv_ = 8 * max(0, s0 / max(cn, 1));
      if (lane == 0 && cn >= 16) *(volatile LAS unsigned*)(L + F_REF) = t0 + (unsigned)(int)mean;
      lds_done();
      *(volatile LAS unsigned*)(L + F_Q) = 1u;
    }
    lds_wait_ge<4>(L + F_ML, 8u);
    const f32x4 m0 = *(LAS f32x4*)(L + L_ML + (lane & 3) * 32), m1 = *(LAS f32x4*)(L + L_ML + (lane & 3) * 32 + 16);
    {
      const f32x4 l0 = *(LAS f32x4*)(L + L_ML + 128 + (lane & 3) * 32), l1 = *(LAS f32x4*)(L + L_ML + 128 + (lane & 3) * 32 + 16);
      const float M = fmaxf(fmaxf(fmaxf(m0.x, m0.y), fmaxf(m0.z, m0.w)), fmaxf(fmaxf(m1.x, m1.y), fmaxf(m1.z, m1.w)));
      float Ls = 0.f;
#pragma unroll
      for (int k = 0; k < 4; ++k) Ls += l0[k] * __builtin_amdgcn_exp2f(m0[k] - M) + l1[k] * __builtin_amdgcn_exp2f(m1[k] - M);
      if (lane < 4) __builtin_amdgcn_raw_buffer_store_b64(u32x2{no_sent(__float_as_uint(M)), no_sent(__float_as_uint(Ls))}, rws, WS_ST + g * 1024 + i * 32 + lane * 8, 0, AUX_SC1);
    }
    lds_wait_ge<4>(L + F_POS, TRIGS);
    {
      u32x2 st;
#pragma unroll 1
      for (unsigned spins = 0; spins < SPIN_CAP; ++spins) {
        asm volatile("" ::: "memory");
        st = __builtin_amdgcn_raw_buffer_load_b64(rws, WS_ST + g * 1024 + (i >> 2) * 128 + ln * 8, 0, AUX_SC1);
        if (!__any(max(st.x, st.y) == SENT)) break;
        __builtin_amdgcn_s_sleep(8);
      }
      const float Ms = __uint_as_float(st.x);
      float Mg = fmaxf(Ms, dppf<ROR4>(Ms)); Mg = fmaxf(Mg, dppf<ROR8>(Mg));
      float Lg = __uint_as_float(st.y) * __builtin_amdgcn_exp2f(Ms - Mg);
      Lg += dppf<ROR4>(Lg); Lg += dppf<ROR8>(Lg);
      const float inv = 1.0f / Lg;
      if (lane < 4) {
#pragma unroll
        for (int k = 0; k < 4; ++k) {
          *(LAS float*)(L + L_AL + k * 16 + lane * 4) = __builtin_amdgcn_exp2f(m0[k] - Mg) * inv;
          *(LAS float*)(L + L_AL + 64 + k * 16 + lane * 4) = __builtin_amdgcn_exp2f(m1[k] - Mg) * inv;
        }
      }
      lds_done();
      *(volatile LAS unsigned*)(L + F_AL) = 1u;
    }
    lds_wait_ge<2>(L + F_LAST, 8u);
    {
      const int kk = lane >> 3;
      const int woff = (kk & 3) * 131072 + (kk >> 2) * 8192 + rb * 2048 + (lane & 7) * 16;
#pragma unroll
      for (int t = 0; t < 16; ++t) wv[t] = __builtin_amdgcn_raw_buffer_load_b128(ro, woff, (t & 7) * 16384 + (t >> 3) * 1024, AUX_NT);
    }
    int off2 = WS_P2 + (i >> 2) * 65536 + (4 * g + (i & 3)) * 2048 + lane * 16, offo = (i >> 2) * 1024 + (4 * g + (i & 3)) * 32 + ln * 8 + lg * 2;
    asm volatile("" : "+v"(off2), "+v"(offo));
    SB();
    __builtin_amdgcn_s_setprio(0);
    wg_barrier();
    __builtin_amdgcn_s_sleep(6);
#pragma unroll
    for (int t = 0; t < 16; ++t) *(LAS u32x4*)(L + img_w + img_row(8 * t)) = wv[t];
    lds_done();
    *(volatile LAS unsigned*)(L + F_WO) = 1u;
    wg_barrier();
    {
      u32x4 p0 = {SENT, SENT, SENT, SENT}, p1 = p0;
      __builtin_amdgcn_s_sleep(H2S0);
      poll2(mk_srd(a_ws, WS_END), off2, p0, p1);
      const f32x4 o = __builtin_bit_cast(f32x4, p0) + __builtin_bit_cast(f32x4, p1);
      float v = tsum32(tsum16(o.x, o.y), tsum16(o.z, o.w));
      v += dppf<ROR4>(v); v += dppf<ROR8>(v);
      if (ln < 4) __builtin_amdgcn_raw_buffer_store_b16((unsigned short)pack_bf16(v, v), rout, offo, 0, 0);
      if (blockIdx.x == 0 && lane == 0) __builtin_amdgcn_raw_buffer_store_b64(tagv, rws, 0, 0, AUX_SC1);
      { const unsigned te_ = (unsigned)__builtin_amdgcn_s_memrealtime(); if (PK == 2) pv_ = 2 * (int)(te_ - t0); while ((int)((unsigned)__builtin_amdgcn_s_memrealtime() - te_) < pv_) __builtin_amdgcn_s_sleep(1); }
    }
  } else {
    const size_t base = ((size_t)((i >> 2) * 8 + g) * 4096 + (i & 3) * 1024) * 256;
#define CHUNK(c) ((c) * 32768 + ((w + (c)) & 7) * 4096)
    const int voff = lane * 16;
    const __amdgpu_buffer_rsrc_t rk = mk_rsrc(a_k + base, 262144), rv = mk_rsrc(a_v + base, 262144), rws = mk_rsrc(a_ws, WS_END);
    u32x4 kr[32], vr[32];
    const __amdgpu_buffer_rsrc_t rq = mk_rsrc(a_wq, 4194304), rx = mk_rsrc(a_x, 8192);
    const int qs = kq * 1048576 + gq * 1024 + cq * 128 + w * 131072;
    const u32x4 wq0 = __builtin_amdgcn_raw_buffer_load_b128(rq, qoff, qs, AUX_NT);
    const u32x4 wq1 = __builtin_amdgcn_raw_buffer_load_b128(rq, qoff, qs + 65536, AUX_NT);
    const u32x4 xq = __builtin_amdgcn_raw_buffer_load_b128(rx, xoff, w * 1024, 0);
#pragma unroll
    for (int n = 0; n < DINIT; ++n) kr[n] = __builtin_amdgcn_raw_buffer_load_b128(rk, koff + (n & 3) * 1024, CHUNK(n >> 2), AUX_NT);
    SB();
    *(LAS u32x4*)(L + img_w + w * 512) = wq0;
    *(LAS u32x4*)(L + img_w + w * 512 + 128) = wq1;
    *(LAS u32x4*)(L + L_X + w * 272 + ln * 16) = xq;
    lds_done();
    if (lane == 0) lds_count(L + F_WQ);
    SB();
    unsigned tref = t0;
    int lag = 0;
#define ISSUE(n) do { \
      __builtin_amdgcn_s_waitcnt(0x0F70 | (DHI - 1)); \
      if ((n) < NBOOST || lag < EHI) { __builtin_amdgcn_s_waitcnt(0x0F70 | (DMID - 1)); if ((n) >= NTH && lag < ELO) __builtin_amdgcn_s_waitcnt(0x0F70 | (DLO - 1)); } \
      if ((n) < 32) kr[(n) & 31] = __builtin_amdgcn_raw_buffer_load_b128(rk, koff + ((n) & 3) * 1024, CHUNK(((n) & 31) >> 2), AUX_NT); \
      else vr[(n) & 31] = __builtin_amdgcn_raw_buffer_load_b128(rv, voff + ((n) & 3) * 1024, CHUNK(((n) & 31) >> 2), AUX_NT); \
      SB(); \
      if (((n) & 1) == 0 && (n) >= NCAL) { \
        if ((n) >= 24 && (n) <= PARK + 4) tref = __builtin_amdgcn_readfirstlane(*(volatile LAS unsigned*)(L + F_REF)); \
        lag = (int)((unsigned)__builtin_amdgcn_s_memrealtime() - tref) - (TOFF + ((n) * TAU) / 10); \
        if ((n) == NCAL && w == 3) *(volatile LAS u32x2*)(L + F_CAL) = u32x2{1u, tref + (unsigned)lag}; } \
      if ((n) == TRIGQ || (n) == TRIGS) *(volatile LAS unsigned*)(L + F_POS) = (n); \
      if ((n) == 63 && lane == 0) lds_count(L + F_LAST); \
      SB(); } while (0)
#pragma unroll
    for (int n = DINIT; n < PARK; ++n) {
      ISSUE(n);
      if (n >= 12 && n < 12 + SPILL) { *(LAS u32x4*)(L + L_SP + (w * SPILL + (n - 12)) * 1024 + voff) = kr[n - 12]; SB(); }
    }
    lds_wait_ge<1>(L + F_Q, 1u);
    const u32x4 qf = *(LAS u32x4*)(L + L_Q + (lane & 3) * 256 + (4 * lg + ((lane >> 2) & 3)) * 16);
    float sc[8];
    constexpr int NQ = PARK + 4;
#pragma unroll
    for (int uu = 0; uu < 32; uu += 4) {
      const int u0 = (uu + SPILL) & 31, t = u0 >> 3;
      f32x4 d[4];
      float y[4];
#pragma unroll
      for (int k = 0; k < 4; ++k) d[k] = mfma(u0 + k < SPILL ? *(LAS u32x4*)(L + L_SP + (w * SPILL + u0 + k) * 1024 + voff) : kr[u0 + k], qf, f32x4{0.f, 0.f, 0.f, 0.f});
      SB();
#pragma unroll
      for (int k = 0; k < 4; ++k) y[k] = d[k][t] + dppf<ROR12>(d[k][(t + 1) & 3]);
      SB();
#pragma unroll
      for (int k = 0; k < 4; ++k) y[k] += dppf<ROR8>(d[k][(t + 2) & 3]);
      SB();
#pragma unroll
      for (int k = 0; k < 4; ++k) y[k] += dppf<ROR4>(d[k][(t + 3) & 3]);
      SB();
#pragma unroll
      for (int k = 0; k < 4; ++k) {
        const int T = (u0 + k) & 7;
        sc[T] = uu < 8 ? y[k] : t == 0 ? bank_set<1>(sc[T], y[k]) : t == 1 ? bank_set<2>(sc[T], y[k]) : t == 2 ? bank_set<4>(sc[T], y[k]) : bank_set<8>(sc[T], y[k]);
      }
      SB();
      if ((uu & 4) == 0) ISSUE(PARK + (uu >> 3));
    }
    float m = fmaxf(fmaxf(fmaxf(sc[0], sc[1]), fmaxf(sc[2], sc[3])), fmaxf(fmaxf(sc[4], sc[5]), fmaxf(sc[6], sc[7])));
    m = fmaxf(m, dppf<ROR4>(m)); m = fmaxf(m, dppf<ROR8>(m)); m = x32max(x16max(m));
    m *= C2;
    float ls = 0.f;
#pragma unroll
    for (int T = 0; T < 8; ++T) { sc[T] = __builtin_amdgcn_exp2f(fmaf(sc[T], C2, -m)); ls += sc[T]; }
    ls += dppf<ROR4>(ls); ls += dppf<ROR8>(ls); ls = tsum16(ls, ls); ls = tsum32(ls, ls);
    if (lane < 4) { *(LAS float*)(L + L_ML + lane * 32 + w * 4) = m; *(LAS float*)(L + L_ML + 128 + lane * 32 + w * 4) = ls; }
    lds_done();
    if (lane == 0) lds_count(L + F_ML);
    u32x4 pa;
#pragma unroll
    for (int k = 0; k < 4; ++k) pa[k] = pack_bf16(sc[2 * k], sc[2 * k + 1]);
    f32x4 acc[2] = {{0.f, 0.f, 0.f, 0.f}, {0.f, 0.f, 0.f, 0.f}}, al[8];
    u32x4 av[4] = {{0u, 0u, 0u, 0u}, {0u, 0u, 0u, 0u}, {0u, 0u, 0u, 0u}, {0u, 0u, 0u, 0u}};
    int off1 = 0, offp = 0, offd = 0, offr = 0;
    SB();
#pragma unroll
    for (int t = 0; t < 4; ++t) {
#pragma unroll
      for (int n = NQ; n < 64; ++n) if ((n > 31 + 8 * t + DHI && n <= 39 + 8 * t + DHI) || (t == 0 && n <= 31 + DHI)) ISSUE(n);
      if (t == 3) {
        off1 = WS_P1 + g * 32768 + (tid >> 5) * 8192 + i * 256 + (tid & 31) * 8;
        offp = WS_P1 + g * 32768 + rb * 8192 + w * 1024 + lane * 16;
        offd = WS_P2 + ln * 65536 + (cb * 4 + w) * 2048 + (g * 4 + rb) * 64 + lg * 16;
        offr = L_RED + w * 2048 + voff;
        asm volatile("" : "+v"(off1), "+v"(offp), "+v"(offd), "+v"(offr));
        if (w < 2) {
          lds_wait_ge<1>(L + F_AL, 1u);
#pragma unroll
          for (int ww = 0; ww < 8; ++ww) al[ww] = *(LAS f32x4*)(L + L_AL + ww * 16);
          asm volatile("" : "+v"(al[0]), "+v"(al[1]), "+v"(al[2]), "+v"(al[3]), "+v"(al[4]), "+v"(al[5]), "+v"(al[6]), "+v"(al[7]));
        }
      }
      u32x4 bb[8];
      if (t == 0) spread_rows<0>(av, pa); else if (t == 1) spread_rows<1>(av, pa); else if (t == 2) spread_rows<2>(av, pa); else spread_rows<3>(av, pa);
      if (t < 3) {
#pragma unroll
        for (int j = 0; j < 8; ++j) {
#pragma unroll
          for (int k = 0; k < 4; ++k) bb[j][k] = __builtin_amdgcn_perm(vr[8 * t + 2 * k + 1][j >> 1], vr[8 * t + 2 * k][j >> 1], (j & 1) ? 0x07060302u : 0x05040100u);
          acc[j >> 2] = mfma(av[j & 3], bb[j], acc[j >> 2]);
        }
        SB();
      } else {
        const unsigned e0 = __builtin_amdgcn_perm(av[1][3], av[0][3], 0x05040100u), e1 = __builtin_amdgcn_perm(av[3][3], av[2][3], 0x05040100u);
        const unsigned f0 = __builtin_amdgcn_perm(av[1][3], av[0][3], 0x07060302u), f1 = __builtin_amdgcn_perm(av[3][3], av[2][3], 0x07060302u);
        u32x4 s0 = {e0, e1, 0u, 0u}, s1 = {0u, 0u, e0, e1}, s2 = {f0, f1, 0u, 0u}, s3 = {0u, 0u, f0, f1};
        asm volatile("" : "+v"(s0), "+v"(s1), "+v"(s2), "+v"(s3));
#pragma unroll
        for (int k = 0; k < 3; ++k) {
#pragma unroll
          for (int j = 0; j < 8; ++j) bb[j][k] = __builtin_amdgcn_perm(vr[24 + 2 * k + 1][j >> 1], vr[24 + 2 * k][j >> 1], (j & 1) ? 0x07060302u : 0x05040100u);
          SB();
        }
#pragma unroll
        for (int j = 0; j < 8; ++j) { bb[j][3] = 0u; acc[j >> 2] = mfma(av[j & 3], bb[j], acc[j >> 2]); }
        SB();
        acc[0] = mfma(s0, vr[30], acc[0]); acc[1] = mfma(s1, vr[30], acc[1]);
        SB();
        acc[0] = mfma(s2, vr[31], acc[0]); acc[1] = mfma(s3, vr[31], acc[1]);
        SB();
      }
    }
    *(LAS f32x4*)(L + offr) = acc[0];
    *(LAS f32x4*)(L + offr + 1024) = acc[1];
    wg_barrier();
    if (w < 2) {
      f32x4 rd[8];
#pragma unroll
      for (int ww = 0; ww < 8; ++ww) rd[ww] = *(LAS f32x4*)(L + L_RED + ww * 2048 + tid * 16);
      SB();
      f32x4 v = rd[0] * al[0];
#pragma unroll
      for (int ww = 1; ww < 8; ++ww) v = __builtin_elementwise_fma(rd[ww], al[ww], v);
      const unsigned h0 = no_sent(pack_bf16(v.x, v.y)), h1 = no_sent(pack_bf16(v.z, v.w));
      const u32x4 pk = {h0, h1, dppu<XOR1>(h0), dppu<XOR1>(h1)};
      if (!(lane & 1)) __builtin_amdgcn_raw_buffer_store_b128(pk, rws, off1, 0, AUX_SC1);
    }
    u32x4 wa[4];
    __builtin_amdgcn_s_sleep(H1S0 - 4);
    lds_wait_ge<1>(L + F_WO, 1u);
#pragma unroll
    for (int ks = 0; ks < 4; ++ks) wa[ks] = tr_frag(L + L_IMG + (w & 3) * 4096 + (lg >> 1) * 512 + (lg & 1) * 128 + ln * 8 + ks * 1024);
    int oiw = L_OI + w * 272 + ln * 16 + lg * 4, oir = L_OI + (ln & 7) * 272 + lg * 16;
    asm volatile("" : "+v"(wa[0]), "+v"(wa[1]), "+v"(wa[2]), "+v"(wa[3]), "+v"(oiw), "+v"(oir));
    {
      u32x4 p = {SENT, SENT, SENT, SENT};
      poll1(mk_srd(a_ws, WS_END), offp, p);
      const float e0 = __uint_as_float(p.x << 16), e1 = __uint_as_float(p.x & 0xFFFF0000u), e2 = __uint_as_float(p.y << 16), e3 = __uint_as_float(p.y & 0xFFFF0000u);
      const float e4 = __uint_as_float(p.z << 16), e5 = __uint_as_float(p.z & 0xFFFF0000u), e6 = __uint_as_float(p.w << 16), e7 = __uint_as_float(p.w & 0xFFFF0000u);
      SB();
      const float a0 = tsum16(e0, e2), a1 = tsum16(e1, e3), a2 = tsum16(e4, e6), a3 = tsum16(e5, e7);
      *(LAS unsigned*)(L + oiw) = pack_bf16(tsum32(a0, a2), tsum32(a1, a3));
    }
    wg_barrier();
    if (w < 4) {
      u32x4 ob[4];
#pragma unroll
      for (int ks = 0; ks < 4; ++ks) ob[ks] = *(LAS u32x4*)(L + oir + ks * 64);
      SB();
      f32x4 d = {0.f, 0.f, 0.f, 0.f};
#pragma unroll
      for (int ks = 0; ks < 4; ++ks) d = mfma(wa[ks], ob[ks], d);
      if (ln < 8) __builtin_amdgcn_raw_buffer_store_b128(u32x4{no_sent(__float_as_uint(d.x)), no_sent(__float_as_uint(d.y)), no_sent(__float_as_uint(d.z)), no_sent(__float_as_uint(d.w))}, rws, offd, 0, AUX_SC1);
    }
  }
}

extern "C" void kernel_launch(void* const* d_in, const int* in_sizes, int n_in, void* d_out, int out_size, void* d_ws, size_t ws_size,
                              hipStream_t stream) {
  gqa_decode_kernel<<<256, NTHR, 0, stream>>>(d_in[1], d_in[2], d_ws, d_in[3], d_in[0], d_in[4], d_out);
}
```

```cpp
#include <hip/hip_runtime.h>
#include <hip/hip_bf16.h>

#define LAS __attribute__((address_space(3)))
typedef unsigned u32x4 __attribute__((ext_vector_type(4)));
typedef unsigned u32x2 __attribute__((ext_vector_type(2)));
typedef float f32x4 __attribute__((ext_vector_type(4)));
typedef short s16x4 __attribute__((ext_vector_type(4)));
typedef __bf16 bf16x8 __attribute__((ext_vector_type(8)));
typedef __bf16 bf16x2 __attribute__((ext_vector_type(2)));
typedef float f32x2 __attribute__((ext_vector_type(2)));
typedef unsigned long long u64;

constexpr int NTHR = 576;
constexpr int TOFF = 90, TAU = 289;
constexpr int DINIT = 4;
constexpr int DLO = 1, DMID = 3, DHI = 6;
constexpr int ELO = -40, EHI = 30;
constexpr int NTH = 16, NBOOST = 28;
constexpr int NCAL = 12, CADJ = 55;
constexpr int SPILL = 4, PARK = 36 + SPILL;
constexpr int TRIGQ = 20, TRIGS = 54;
constexpr int H1S0 = 22, H2S0 = 21;
constexpr int AUX_NT = 2, AUX_SC1 = 16;
constexpr unsigned SENT = 0xFFFFFFFFu;
constexpr unsigned SPIN_CAP = 1u << 16;
constexpr int WS_HI = 1024;
constexpr int WS_T0 = 4096;
constexpr int WS_QG = 8192;
constexpr int WS_P1 = WS_QG + 524288;
constexpr int WS_ST = WS_P1 + 262144;
constexpr int WS_P2 = WS_ST + 8192;
constexpr int WS_END = WS_P2 + 524288;
constexpr int L_IMG = 0;
constexpr int L_RED = 16384;
constexpr int L_Q = 32768;
constexpr int L_ML = L_Q + 1024;
constexpr int L_OI = L_ML + 256;
constexpr int L_AL = L_OI + 8 * 272;
constexpr int L_FLAG = L_AL + 128;
constexpr int L_X = L_FLAG + 48;
constexpr int L_SP = L_X + 8 * 272;
constexpr int L_END = L_SP + 8 * SPILL * 1024;
constexpr int F_Q = L_FLAG, F_ML = L_FLAG + 4, F_POS = L_FLAG + 8, F_LAST = L_FLAG + 12, F_WQ = L_FLAG + 16, F_AL = L_FLAG + 20, F_WO = L_FLAG + 24,
              F_REF = L_FLAG + 32, F_CAL = L_FLAG + 40;

constexpr float C2 = 0.08838834764831845f * 1.4426950408889634f;

__device__ __forceinline__ f32x4 mfma(u32x4 a, u32x4 b, f32x4 c) {
  return __builtin_amdgcn_mfma_f32_16x16x32_bf16(__builtin_bit_cast(bf16x8, a), __builtin_bit_cast(bf16x8, b), c, 0, 0, 0);
}
template <int CTRL> __device__ __forceinline__ float dppf(float x) {
  return __builtin_bit_cast(float, __builtin_amdgcn_mov_dpp(__builtin_bit_cast(int, x), CTRL, 0xf, 0xf, true));
}
template <int CTRL> __device__ __forceinline__ unsigned dppu(unsigned x) {
  return (unsigned)__builtin_amdgcn_mov_dpp((int)x, CTRL, 0xf, 0xf, true);
}
constexpr int ROR4 = 0x124, ROR8 = 0x128, ROR12 = 0x12C, QID = 0xE4, XOR1 = 0xB1, XOR2 = 0x4E;
template <int BANKS> __device__ __forceinline__ float bank_set(float old, float y) {
  return __builtin_bit_cast(float, __builtin_amdgcn_update_dpp(__builtin_bit_cast(int, old), __builtin_bit_cast(int, y), QID, 0xf, BANKS, false));
}
template <int SRC, int DST> __device__ __forceinline__ unsigned bank_move(unsigned old, unsigned y) {
  return (unsigned)__builtin_amdgcn_update_dpp((int)old, (int)y, SRC == DST ? QID : 0x120 + 4 * ((DST - SRC) & 3), 0xf, 1 << DST, false);
}
template <int SRC> __device__ __forceinline__ void spread_rows(u32x4 (&av)[4], u32x4 pa) {
#pragma unroll
  for (int k = 0; k < 4; ++k) {
    av[0][k] = bank_move<SRC, 0>(av[0][k], pa[k]); av[1][k] = bank_move<SRC, 1>(av[1][k], pa[k]);
    av[2][k] = bank_move<SRC, 2>(av[2][k], pa[k]); av[3][k] = bank_move<SRC, 3>(av[3][k], pa[k]);
  }
}
__device__ __forceinline__ float x16max(float x) {
  auto s = __builtin_amdgcn_permlane16_swap(__float_as_uint(x), __float_as_uint(x), false, false);
  return fmaxf(__uint_as_float(s[0]), __uint_as_float(s[1]));
}
__device__ __forceinline__ float x32max(float x) {
  auto s = __builtin_amdgcn_permlane32_swap(__float_as_uint(x), __float_as_uint(x), false, false);
  return fmaxf(__uint_as_float(s[0]), __uint_as_float(s[1]));
}
__device__ __forceinline__ float tsum16(float a, float b) {
  auto s = __builtin_amdgcn_permlane16_swap(__float_as_uint(a), __float_as_uint(b), false, false);
  return __uint_as_float(s[0]) + __uint_as_float(s[1]);
}
__device__ __forceinline__ float tsum32(float a, float b) {
  auto s = __builtin_amdgcn_permlane32_swap(__float_as_uint(a), __float_as_uint(b), false, false);
  return __uint_as_float(s[0]) + __uint_as_float(s[1]);
}
__device__ __forceinline__ unsigned pack_bf16(float lo, float hi) {
  f32x2 v = {lo, hi};
  return __builtin_bit_cast(unsigned, __builtin_convertvector(v, bf16x2));
}
__device__ __forceinline__ unsigned no_sent(unsigned u) { return min(u, SENT - 1u); }
__device__ __forceinline__ bool has_sent(u32x4 u) { return max(max(u.x, u.y), max(u.z, u.w)) == SENT; }
__device__ __forceinline__ void wg_barrier() { asm volatile("s_waitcnt lgkmcnt(0)\n\ts_barrier" ::: "memory"); }
__device__ __forceinline__ void lds_done() { asm volatile("s_waitcnt lgkmcnt(0)" ::: "memory"); }
__device__ __forceinline__ __amdgpu_buffer_rsrc_t mk_rsrc(u64 p, int bytes) { return __builtin_amdgcn_make_buffer_rsrc((void*)p, 0, bytes, 0x00020000); }
__device__ __forceinline__ u32x4 mk_srd(u64 p, int bytes) { return u32x4{(unsigned)p, (unsigned)(p >> 32) & 0xFFFFu, (unsigned)bytes, 0x00020000u}; }
__device__ __forceinline__ void poll1(u32x4 srd, int off, u32x4& r) {
  u64 m = ~0ull;
#pragma unroll 1
  for (unsigned spins = 0; spins < SPIN_CAP; ++spins) {
    asm volatile("s_mov_b64 exec, %2\n\tbuffer_load_dwordx4 %0, %1, %3, 0 offen sc1\n\ts_mov_b64 exec, -1\n\ts_waitcnt vmcnt(0)"
                 : "+v"(r) : "v"(off), "s"(m), "s"(srd) : "memory");
    m = __builtin_amdgcn_ballot_w64(has_sent(r));
    if (m == 0ull) break;
  }
}
__device__ __forceinline__ void poll2(u32x4 srd, int off, u32x4& r0, u32x4& r1) {
  u64 m0 = ~0ull, m1 = ~0ull;
#pragma unroll 1
  for (unsigned spins = 0; spins < SPIN_CAP; ++spins) {
    asm volatile("s_mov_b64 exec, %3\n\tbuffer_load_dwordx4 %0, %2, %5, 0 offen sc1\n\ts_mov_b64 exec, %4\n\tbuffer_load_dwordx4 %1, %2, %5, 0 offen offset:1024 sc1\n\t"
                 "s_mov_b64 exec, -1\n\ts_waitcnt vmcnt(0)"
                 : "+v"(r0), "+v"(r1) : "v"(off), "s"(m0 | 1ull), "s"(m1 | 1ull), "s"(srd) : "memory");
    m0 = __builtin_amdgcn_ballot_w64(has_sent(r0));
    m1 = __builtin_amdgcn_ballot_w64(has_sent(r1));
    if ((m0 | m1) == 0ull) break;
  }
}
__device__ __forceinline__ u32x4 tr_frag(LAS unsigned char* p) {
  const u32x2 lo = __builtin_bit_cast(u32x2, __builtin_amdgcn_ds_read_tr16_b64_v4i16((LAS s16x4*)p));
  const u32x2 hi = __builtin_bit_cast(u32x2, __builtin_amdgcn_ds_read_tr16_b64_v4i16((LAS s16x4*)(p + 256)));
  return u32x4{lo.x, lo.y, hi.x, hi.y};
}
__device__ __forceinline__ int img_row(int k) { return (k & ~12) * 32 + ((k >> 2) & 1) * 256 + ((k >> 3) & 1) * 128; }
template <int SLP> __device__ __forceinline__ void lds_wait_ge(LAS unsigned char* p, unsigned want) {
#pragma unroll 1
  for (unsigned spins = 0; spins < (SPIN_CAP << 6); ++spins) {
    if ((unsigned)__builtin_amdgcn_readfirstlane(*(volatile LAS unsigned*)p) >= want) break;
    __builtin_amdgcn_s_sleep(SLP);
  }
  asm volatile("" ::: "memory");
}
__device__ __forceinline__ void lds_count(LAS unsigned char* p) { __hip_atomic_fetch_add((LAS unsigned*)p, 1u, __ATOMIC_RELAXED, __HIP_MEMORY_SCOPE_WORKGROUP); }
typedef __attribute__((address_space(4))) const u64 karg_t;
__device__ __forceinline__ karg_t* kargs() { karg_t* p = (karg_t*)__builtin_amdgcn_kernarg_segment_ptr(); asm volatile("" : "+s"(p)); return p; }
#define SB() __builtin_amdgcn_sched_barrier(0)

__global__ void __launch_bounds__(NTHR, 3)
gqa_decode_kernel(const void* kc_, const void* vc_, void* ws_, const void* wq_, const void* x_, const void* wo_, void* out_) {
  karg_t* ka = kargs();
  const u64 a_k = ka[0], a_v = ka[1], a_ws = ka[2], a_wq = ka[3], a_x = ka[4];
  const unsigned t0 = (unsigned)__builtin_amdgcn_s_memrealtime();
  SB();
  __shared__ __attribute__((aligned(16))) unsigned char smem[L_END];
  LAS unsigned char* L = (LAS unsigned char*)smem;
  const int tid = threadIdx.x, lane = tid & 63, w = __builtin_amdgcn_readfirstlane(tid >> 6);
  const int g = blockIdx.x & 7, i = blockIdx.x >> 3;
  const int lg = lane >> 4, ln = lane & 15;
  const int rb = i >> 3, cb = i & 7;
  const int gq = i & 7, kq = i >> 3, cq = (g + gq) & 7;
  const int img_w = L_IMG + ((lane & 7) >> 1) * 4096 + img_row(lane >> 3) + (lane & 1) * 16;
  int koff = ((lane >> 2) & 3) * 256 + lg * 64 + (lane & 3) * 16;
  int qoff = (lane >> 3) * 8192 + (lane & 7) * 16, xoff = kq * 256 + ln * 16;
  asm volatile("" : "+v"(koff), "+v"(qoff), "+v"(xoff));
  if (tid < 12) *(LAS unsigned*)(L + L_FLAG + tid * 4) = tid == 8 ? t0 : 0u;
  wg_barrier();

  if (__builtin_expect(w >= 8, 0)) {
    __builtin_amdgcn_s_setprio(3);
    const __amdgpu_buffer_rsrc_t rws = mk_rsrc(a_ws, WS_END);
    u32x4 wv[16];
    const u32x2 ep = __builtin_amdgcn_raw_buffer_load_b64(rws, 0, 0, AUX_SC1);
    const int sl = cq * 4 + kq;
    {
      const u32x4 sv = {SENT, SENT, SENT, SENT};
      __builtin_amdgcn_raw_buffer_store_b128(sv, rws, WS_P1 + gq * 32768 + sl * 1024 + lane * 16, 0, AUX_SC1);
      if (lane < 2) __builtin_amdgcn_raw_buffer_store_b128(sv, rws, WS_ST + gq * 1024 + sl * 32 + lane * 16, 0, AUX_SC1);
      const int o2 = WS_P2 + lg * 65536 + (cb * 4 + ((lane >> 2) & 3)) * 2048 + (g * 4 + rb) * 64 + (lane & 3) * 16;
      __builtin_amdgcn_raw_buffer_store_b128(sv, rws, o2, 0, AUX_SC1);
      __builtin_amdgcn_raw_buffer_store_b128(sv, rws, o2 + 4 * 65536, 0, AUX_SC1);
    }
    SB();
    lds_wait_ge<1>(L + F_WQ, 8u);
    const u64 tag = (((u64)ep.y << 32) | ep.x) + 1ull;
    const u32x2 tagv = {(unsigned)tag, (unsigned)(tag >> 32)};
    {
      LAS unsigned char* ip = L + L_IMG + (lg >> 1) * 512 + (lg & 1) * 128 + ln * 8;
      LAS unsigned char* xp = L + L_X + (ln & 7) * 272 + lg * 16;
      f32x4 d[4];
#pragma unroll
      for (int ct = 0; ct < 4; ++ct) d[ct] = f32x4{0.f, 0.f, 0.f, 0.f};
#pragma unroll
      for (int ks = 0; ks < 4; ++ks) {
        const u32x4 xb = *(LAS u32x4*)(xp + ks * 64);
#pragma unroll
        for (int ct = 0; ct < 4; ++ct) d[ct] = mfma(tr_frag(ip + ct * 4096 + ks * 1024), xb, d[ct]);
      }
      const int off = WS_QG + gq * 65536 + (ln & 7) * 8192 + kq * 2048 + cq * 256 + (ln >> 3) * 128 + lg * 16;
      __builtin_amdgcn_raw_buffer_store_b128(__builtin_bit_cast(u32x4, ln < 8 ? d[0] : d[2]), rws, off, 0, AUX_SC1);
      __builtin_amdgcn_raw_buffer_store_b128(__builtin_bit_cast(u32x4, ln < 8 ? d[1] : d[3]), rws, off + 64, 0, AUX_SC1);
      asm volatile("s_waitcnt vmcnt(0)" ::: "memory");
      if (lane == 0) __builtin_amdgcn_raw_buffer_store_b64(tagv, rws, WS_HI + gq * 256 + sl * 8, 0, AUX_SC1);
    }
    lds_wait_ge<4>(L + F_CAL, 1u);
    if (lane == 0) __builtin_amdgcn_raw_buffer_store_b64(u32x2{(*(volatile LAS unsigned*)(L + F_CAL + 4) << 16) | (t0 & 0xFFFFu), tagv.x}, rws, WS_T0 + gq * 256 + sl * 8, 0, AUX_SC1);
    const __amdgpu_buffer_rsrc_t ro = mk_rsrc(kargs()[5] + g * 524288 + cb * 128, 524288 - 1024 + 128), rout = mk_rsrc(kargs()[6], 8192);
    {
      const unsigned c0 = __builtin_amdgcn_raw_buffer_load_b32(mk_rsrc(a_v + ((size_t)((i >> 2) * 8 + g) * 4096 + (i & 3) * 1024) * 256, 262144), (lane & 3) * 65536, 0, AUX_NT);
      const unsigned c1 = __builtin_amdgcn_raw_buffer_load_b32(ro, (lane & 7) * 65536 + rb * 2048, 0, AUX_NT);
      asm volatile("" :: "v"(c0), "v"(c1));
    }
    lds_wait_ge<4>(L + F_POS, TRIGQ);
    {
#pragma unroll 1
      for (unsigned spins = 0; spins < SPIN_CAP; ++spins) {
        asm volatile("" ::: "memory");
        const u32x2 hi = __builtin_amdgcn_raw_buffer_load_b64(rws, WS_HI + g * 256 + (lane & 31) * 8, 0, AUX_SC1);
        if (!__any(((hi.x ^ tagv.x) | (hi.y ^ tagv.y)) != 0u)) break;
        __builtin_amdgcn_s_sleep(8);
      }
      asm volatile("" ::: "memory");
      const int off = WS_QG + g * 65536 + (i >> 2) * 8192 + lane * 16;
#pragma unroll
      for (int t = 0; t < 8; ++t) wv[t] = __builtin_amdgcn_raw_buffer_load_b128(rws, off + (t & 1) * 1024, (t >> 1) * 2048, AUX_SC1);
      const u32x2 o = __builtin_amdgcn_raw_buffer_load_b64(rws, WS_T0 + g * 256 + (lane & 31) * 8, 0, AUX_SC1);
#pragma unroll
      for (int hh = 0; hh < 2; ++hh) {
        const f32x4 q4 = (__builtin_bit_cast(f32x4, wv[hh]) + __builtin_bit_cast(f32x4, wv[2 + hh])) + (__builtin_bit_cast(f32x4, wv[4 + hh]) + __builtin_bit_cast(f32x4, wv[6 + hh]));
        *(LAS u32x2*)(L + L_Q + hh * 512 + lane * 8) = u32x2{pack_bf16(q4.x, q4.y), pack_bf16(q4.z, q4.w)};
      }
      const bool ok = o.y == tagv.x;
      int dt = ok ? (int)(short)((lane < 32 ? o.x : o.x >> 16) - t0) : 0;
      dt += (int)dppu<XOR1>(dt); dt += (int)dppu<XOR2>(dt); dt += (int)dppu<ROR4>(dt); dt += (int)dppu<ROR8>(dt);
      const int cn = __builtin_popcount((unsigned)__builtin_amdgcn_ballot_w64(ok));
      const int s0 = __builtin_amdgcn_readlane(dt, 0) + __builtin_amdgcn_readlane(dt, 16), s1 = __builtin_amdgcn_readlane(dt, 32) + __builtin_amdgcn_readlane(dt, 48);
      const float mean = (float)max(s0, s1 + CADJ * cn) * __builtin_amdgcn_rcpf((float)cn);
      if (lane == 0 && cn >= 16) *(volatile LAS unsigned*)(L + F_REF) = t0 + (unsigned)(int)mean;
      lds_done();
      *(volatile LAS unsigned*)(L + F_Q) = 1u;
    }
    lds_wait_ge<4>(L + F_ML, 8u);
    const f32x4 m0 = *(LAS f32x4*)(L + L_ML + (lane & 3) * 32), m1 = *(LAS f32x4*)(L + L_ML + (lane & 3) * 32 + 16);
    {
      const f32x4 l0 = *(LAS f32x4*)(L + L_ML + 128 + (lane & 3) * 32), l1 = *(LAS f32x4*)(L + L_ML + 128 + (lane & 3) * 32 + 16);
      const float M = fmaxf(fmaxf(fmaxf(m0.x, m0.y), fmaxf(m0.z, m0.w)), fmaxf(fmaxf(m1.x, m1.y), fmaxf(m1.z, m1.w)));
      float Ls = 0.f;
#pragma unroll
      for (int k = 0; k < 4; ++k) Ls += l0[k] * __builtin_amdgcn_exp2f(m0[k] - M) + l1[k] * __builtin_amdgcn_exp2f(m1[k] - M);
      if (lane < 4) __builtin_amdgcn_raw_buffer_store_b64(u32x2{no_sent(__float_as_uint(M)), no_sent(__float_as_uint(Ls))}, rws, WS_ST + g * 1024 + i * 32 + lane * 8, 0, AUX_SC1);
    }
    lds_wait_ge<4>(L + F_POS, TRIGS);
    {
      u32x2 st;
#pragma unroll 1
      for (unsigned spins = 0; spins < SPIN_CAP; ++spins) {
        asm volatile("" ::: "memory");
        st = __builtin_amdgcn_raw_buffer_load_b64(rws, WS_ST + g * 1024 + (i >> 2) * 128 + ln * 8, 0, AUX_SC1);
        if (!__any(max(st.x, st.y) == SENT)) break;
        __builtin_amdgcn_s_sleep(8);
      }
      const float Ms = __uint_as_float(st.x);
      float Mg = fmaxf(Ms, dppf<ROR4>(Ms)); Mg = fmaxf(Mg, dppf<ROR8>(Mg));
      float Lg = __uint_as_float(st.y) * __builtin_amdgcn_exp2f(Ms - Mg);
      Lg += dppf<ROR4>(Lg); Lg += dppf<ROR8>(Lg);
      const float inv = 1.0f / Lg;
      if (lane < 4) {
#pragma unroll
        for (int k = 0; k < 4; ++k) {
          *(LAS float*)(L + L_AL + k * 16 + lane * 4) = __builtin_amdgcn_exp2f(m0[k] - Mg) * inv;
          *(LAS float*)(L + L_AL + 64 + k * 16 + lane * 4) = __builtin_amdgcn_exp2f(m1[k] - Mg) * inv;
        }
      }
      lds_done();
      *(volatile LAS unsigned*)(L + F_AL) = 1u;
    }
    lds_wait_ge<2>(L + F_LAST, 8u);
    {
      const int kk = lane >> 3;
      const int woff = (kk & 3) * 131072 + (kk >> 2) * 8192 + rb * 2048 + (lane & 7) * 16;
#pragma unroll
      for (int t = 0; t < 16; ++t) wv[t] = __builtin_amdgcn_raw_buffer_load_b128(ro, woff, (t & 7) * 16384 + (t >> 3) * 1024, AUX_NT);
    }
    int off2 = WS_P2 + (i >> 2) * 65536 + (4 * g + (i & 3)) * 2048 + lane * 16, offo = (i >> 2) * 1024 + (4 * g + (i & 3)) * 32 + ln * 8 + lg * 2;
    asm volatile("" : "+v"(off2), "+v"(offo));
    SB();
    __builtin_amdgcn_s_setprio(0);
    wg_barrier();
    __builtin_amdgcn_s_sleep(6);
#pragma unroll
    for (int t = 0; t < 16; ++t) *(LAS u32x4*)(L + img_w + img_row(8 * t)) = wv[t];
    lds_done();
    *(volatile LAS unsigned*)(L + F_WO) = 1u;
    wg_barrier();
    {
      u32x4 p0 = {SENT, SENT, SENT, SENT}, p1 = p0;
      __builtin_amdgcn_s_sleep(H2S0);
      poll2(mk_srd(a_ws, WS_END), off2, p0, p1);
      const f32x4 o = __builtin_bit_cast(f32x4, p0) + __builtin_bit_cast(f32x4, p1);
      float v = tsum32(tsum16(o.x, o.y), tsum16(o.z, o.w));
      v += dppf<ROR4>(v); v += dppf<ROR8>(v);
      if (ln < 4) __builtin_amdgcn_raw_buffer_store_b16((unsigned short)pack_bf16(v, v), rout, offo, 0, 0);
      if (blockIdx.x == 0 && lane == 0) __builtin_amdgcn_raw_buffer_store_b64(tagv, rws, 0, 0, AUX_SC1);
    }
  } else {
    const size_t base = ((size_t)((i >> 2) * 8 + g) * 4096 + (i & 3) * 1024) * 256;
#define CHUNK(c) ((c) * 32768 + ((w + (c)) & 7) * 4096)
    const int voff = lane * 16;
    const __amdgpu_buffer_rsrc_t rk = mk_rsrc(a_k + base, 262144), rv = mk_rsrc(a_v + base, 262144), rws = mk_rsrc(a_ws, WS_END);
    u32x4 kr[32], vr[32];
    const __amdgpu_buffer_rsrc_t rq = mk_rsrc(a_wq, 4194304), rx = mk_rsrc(a_x, 8192);
    const int qs = kq * 1048576 + gq * 1024 + cq * 128 + w * 131072;
    const u32x4 wq0 = __builtin_amdgcn_raw_buffer_load_b128(rq, qoff, qs, AUX_NT);
    const u32x4 wq1 = __builtin_amdgcn_raw_buffer_load_b128(rq, qoff, qs + 65536, AUX_NT);
    const u32x4 xq = __builtin_amdgcn_raw_buffer_load_b128(rx, xoff, w * 1024, 0);
#pragma unroll
    for (int n = 0; n < DINIT; ++n) kr[n] = __builtin_amdgcn_raw_buffer_load_b128(rk, koff + (n & 3) * 1024, CHUNK(n >> 2), AUX_NT);
    SB();
    *(LAS u32x4*)(L + img_w + w * 512) = wq0;
    *(LAS u32x4*)(L + img_w + w * 512 + 128) = wq1;
    *(LAS u32x4*)(L + L_X + w * 272 + ln * 16) = xq;
    lds_done();
    if (lane == 0) lds_count(L + F_WQ);
    SB();
    unsigned tref = t0;
    int lag = 0;
#define ISSUE(n) do { \
      __builtin_amdgcn_s_waitcnt(0x0F70 | (DHI - 1)); \
      if ((n) < NBOOST || lag < EHI) { __builtin_amdgcn_s_waitcnt(0x0F70 | (DMID - 1)); if ((n) >= NTH && lag < ELO) __builtin_amdgcn_s_waitcnt(0x0F70 | (DLO - 1)); } \
      if ((n) < 32) kr[(n) & 31] = __builtin_amdgcn_raw_buffer_load_b128(rk, koff + ((n) & 3) * 1024, CHUNK(((n) & 31) >> 2), AUX_NT); \
      else vr[(n) & 31] = __builtin_amdgcn_raw_buffer_load_b128(rv, voff + ((n) & 3) * 1024, CHUNK(((n) & 31) >> 2), AUX_NT); \
      SB(); \
      if (((n) & 1) == 0 && (n) >= NCAL) { \
        if ((n) >= 24 && (n) <= PARK + 4) tref = __builtin_amdgcn_readfirstlane(*(volatile LAS unsigned*)(L + F_REF)); \
        lag = (int)((unsigned)__builtin_amdgcn_s_memrealtime() - tref) - (TOFF + ((n) * TAU) / 10); \
        if ((n) == NCAL && w == 3) *(volatile LAS u32x2*)(L + F_CAL) = u32x2{1u, tref + (unsigned)lag}; } \
      if ((n) == TRIGQ || (n) == TRIGS) *(volatile LAS unsigned*)(L + F_POS) = (n); \
      if ((n) == 63 && lane == 0) lds_count(L + F_LAST); \
      SB(); } while (0)
#pragma unroll
    for (int n = DINIT; n < PARK; ++n) {
      ISSUE(n);
      if (n >= 12 && n < 12 + SPILL) { *(LAS u32x4*)(L + L_SP + (w * SPILL + (n - 12)) * 1024 + voff) = kr[n - 12]; SB(); }
    }
    lds_wait_ge<1>(L + F_Q, 1u);
    const u32x4 qf = *(LAS u32x4*)(L + L_Q + (lane & 3) * 256 + (4 * lg + ((lane >> 2) & 3)) * 16);
    float sc[8];
    constexpr int NQ = PARK + 4;
#pragma unroll
    for (int uu = 0; uu < 32; uu += 4) {
      const int u0 = (uu + SPILL) & 31, t = u0 >> 3;
      f32x4 d[4];
      float y[4];
#pragma unroll
      for (int k = 0; k < 4; ++k) d[k] = mfma(u0 + k < SPILL ? *(LAS u32x4*)(L + L_SP + (w * SPILL + u0 + k) * 1024 + voff) : kr[u0 + k], qf, f32x4{0.f, 0.f, 0.f, 0.f});
      SB();
#pragma unroll
      for (int k = 0; k < 4; ++k) y[k] = d[k][t] + dppf<ROR12>(d[k][(t + 1) & 3]);
      SB();
#pragma unroll
      for (int k = 0; k < 4; ++k) y[k] += dppf<ROR8>(d[k][(t + 2) & 3]);
      SB();
#pragma unroll
      for (int k = 0; k < 4; ++k) y[k] += dppf<ROR4>(d[k][(t + 3) & 3]);
      SB();
#pragma unroll
      for (int k = 0; k < 4; ++k) {
        const int T = (u0 + k) & 7;
        sc[T] = uu < 8 ? y[k] : t == 0 ? bank_set<1>(sc[T], y[k]) : t == 1 ? bank_set<2>(sc[T], y[k]) : t == 2 ? bank_set<4>(sc[T], y[k]) : bank_set<8>(sc[T], y[k]);
      }
      SB();
      if ((uu & 4) == 0) ISSUE(PARK + (uu >> 3));
    }
    float m = fmaxf(fmaxf(fmaxf(sc[0], sc[1]), fmaxf(sc[2], sc[3])), fmaxf(fmaxf(sc[4], sc[5]), fmaxf(sc[6], sc[7])));
    m = fmaxf(m, dppf<ROR4>(m)); m = fmaxf(m, dppf<ROR8>(m)); m = x32max(x16max(m));
    m *= C2;
    float ls = 0.f;
#pragma unroll
    for (int T = 0; T < 8; ++T) { sc[T] = __builtin_amdgcn_exp2f(fmaf(sc[T], C2, -m)); ls += sc[T]; }
    ls += dppf<ROR4>(ls); ls += dppf<ROR8>(ls); ls = tsum16(ls, ls); ls = tsum32(ls, ls);
    if (lane < 4) { *(LAS float*)(L + L_ML + lane * 32 + w * 4) = m; *(LAS float*)(L + L_ML + 128 + lane * 32 + w * 4) = ls; }
    lds_done();
    if (lane == 0) lds_count(L + F_ML);
    u32x4 pa;
#pragma unroll
    for (int k = 0; k < 4; ++k) pa[k] = pack_bf16(sc[2 * k], sc[2 * k + 1]);
    f32x4 acc[2] = {{0.f, 0.f, 0.f, 0.f}, {0.f, 0.f, 0.f, 0.f}}, al[8];
    u32x4 av[4] = {{0u, 0u, 0u, 0u}, {0u, 0u, 0u, 0u}, {0u, 0u, 0u, 0u}, {0u, 0u, 0u, 0u}};
    int off1 = 0, offp = 0, offd = 0, offr = 0;
    SB();
#pragma unroll
    for (int t = 0; t < 4; ++t) {
#pragma unroll
      for (int n = NQ; n < 64; ++n) if ((n > 31 + 8 * t + DHI && n <= 39 + 8 * t + DHI) || (t == 0 && n <= 31 + DHI)) ISSUE(n);
      if (t == 3) {
        off1 = WS_P1 + g * 32768 + (tid >> 5) * 8192 + i * 256 + (tid & 31) * 8;
        offp = WS_P1 + g * 32768 + rb * 8192 + w * 1024 + lane * 16;
        offd = WS_P2 + ln * 65536 + (cb * 4 + w) * 2048 + (g * 4 + rb) * 64 + lg * 16;
        offr = L_RED + w * 2048 + voff;
        asm volatile("" : "+v"(off1), "+v"(offp), "+v"(offd), "+v"(offr));
        if (w < 2) {
          lds_wait_ge<1>(L + F_AL, 1u);
#pragma unroll
          for (int ww = 0; ww < 8; ++ww) al[ww] = *(LAS f32x4*)(L + L_AL + ww * 16);
          asm volatile("" : "+v"(al[0]), "+v"(al[1]), "+v"(al[2]), "+v"(al[3]), "+v"(al[4]), "+v"(al[5]), "+v"(al[6]), "+v"(al[7]));
        }
      }
      u32x4 bb[8];
      if (t == 0) spread_rows<0>(av, pa); else if (t == 1) spread_rows<1>(av, pa); else if (t == 2) spread_rows<2>(av, pa); else spread_rows<3>(av, pa);
      if (t < 3) {
#pragma unroll
        for (int j = 0; j < 8; ++j) {
#pragma unroll
          for (int k = 0; k < 4; ++k) bb[j][k] = __builtin_amdgcn_perm(vr[8 * t + 2 * k + 1][j >> 1], vr[8 * t + 2 * k][j >> 1], (j & 1) ? 0x07060302u : 0x05040100u);
          acc[j >> 2] = mfma(av[j & 3], bb[j], acc[j >> 2]);
        }
        SB();
      } else {
        const unsigned e0 = __builtin_amdgcn_perm(av[1][3], av[0][3], 0x05040100u), e1 = __builtin_amdgcn_perm(av[3][3], av[2][3], 0x05040100u);
        const unsigned f0 = __builtin_amdgcn_perm(av[1][3], av[0][3], 0x07060302u), f1 = __builtin_amdgcn_perm(av[3][3], av[2][3], 0x07060302u);
        u32x4 s0 = {e0, e1, 0u, 0u}, s1 = {0u, 0u, e0, e1}, s2 = {f0, f1, 0u, 0u}, s3 = {0u, 0u, f0, f1};
        asm volatile("" : "+v"(s0), "+v"(s1), "+v"(s2), "+v"(s3));
#pragma unroll
        for (int k = 0; k < 3; ++k) {
#pragma unroll
          for (int j = 0; j < 8; ++j) bb[j][k] = __builtin_amdgcn_perm(vr[24 + 2 * k + 1][j >> 1], vr[24 + 2 * k][j >> 1], (j & 1) ? 0x07060302u : 0x05040100u);
          SB();
        }
#pragma unroll
        for (int j = 0; j < 8; ++j) { bb[j][3] = 0u; acc[j >> 2] = mfma(av[j & 3], bb[j], acc[j >> 2]); }
        SB();
        acc[0] = mfma(s0, vr[30], acc[0]); acc[1] = mfma(s1, vr[30], acc[1]);
        SB();
        acc[0] = mfma(s2, vr[31], acc[0]); acc[1] = mfma(s3, vr[31], acc[1]);
        SB();
      }
    }
    *(LAS f32x4*)(L + offr) = acc[0];
    *(LAS f32x4*)(L + offr + 1024) = acc[1];
    wg_barrier();
    if (w < 2) {
      f32x4 rd[8];
#pragma unroll
      for (int ww = 0; ww < 8; ++ww) rd[ww] = *(LAS f32x4*)(L + L_RED + ww * 2048 + tid * 16);
      SB();
      f32x4 v = rd[0] * al[0];
#pragma unroll
      for (int ww = 1; ww < 8; ++ww) v = __builtin_elementwise_fma(rd[ww], al[ww], v);
      const unsigned h0 = no_sent(pack_bf16(v.x, v.y)), h1 = no_sent(pack_bf16(v.z, v.w));
      const u32x4 pk = {h0, h1, dppu<XOR1>(h0), dppu<XOR1>(h1)};
      if (!(lane & 1)) __builtin_amdgcn_raw_buffer_store_b128(pk, rws, off1, 0, AUX_SC1);
    }
    u32x4 wa[4];
    __builtin_amdgcn_s_sleep(H1S0 - 4);
    lds_wait_ge<1>(L + F_WO, 1u);
#pragma unroll
    for (int ks = 0; ks < 4; ++ks) wa[ks] = tr_frag(L + L_IMG + (w & 3) * 4096 + (lg >> 1) * 512 + (lg & 1) * 128 + ln * 8 + ks * 1024);
    int oiw = L_OI + w * 272 + ln * 16 + lg * 4, oir = L_OI + (ln & 7) * 272 + lg * 16;
    asm volatile("" : "+v"(wa[0]), "+v"(wa[1]), "+v"(wa[2]), "+v"(wa[3]), "+v"(oiw), "+v"(oir));
    {
      u32x4 p = {SENT, SENT, SENT, SENT};
      poll1(mk_srd(a_ws, WS_END), offp, p);
      const float e0 = __uint_as_float(p.x << 16), e1 = __uint_as_float(p.x & 0xFFFF0000u), e2 = __uint_as_float(p.y << 16), e3 = __uint_as_float(p.y & 0xFFFF0000u);
      const float e4 = __uint_as_float(p.z << 16), e5 = __uint_as_float(p.z & 0xFFFF0000u), e6 = __uint_as_float(p.w << 16), e7 = __uint_as_float(p.w & 0xFFFF0000u);
      SB();
      const float a0 = tsum16(e0, e2), a1 = tsum16(e1, e3), a2 = tsum16(e4, e6), a3 = tsum16(e5, e7);
      *(LAS unsigned*)(L + oiw) = pack_bf16(tsum32(a0, a2), tsum32(a1, a3));
    }
    wg_barrier();
    if (w < 4) {
      u32x4 ob[4];
#pragma unroll
      for (int ks = 0; ks < 4; ++ks) ob[ks] = *(LAS u32x4*)(L + oir + ks * 64);
      SB();
      f32x4 d = {0.f, 0.f, 0.f, 0.f};
#pragma unroll
      for (int ks = 0; ks < 4; ++ks) d = mfma(wa[ks], ob[ks], d);
      if (ln < 8) __builtin_amdgcn_raw_buffer_store_b128(u32x4{no_sent(__float_as_uint(d.x)), no_sent(__float_as_uint(d.y)), no_sent(__float_as_uint(d.z)), no_sent(__float_as_uint(d.w))}, rws, offd, 0, AUX_SC1);
    }
  }
}

extern "C" void kernel_launch(void* const* d_in, const int* in_sizes, int n_in, void* d_out, int out_size, void* d_ws, size_t ws_size,
                              hipStream_t stream) {
  gqa_decode_kernel<<<256, NTHR, 0, stream>>>(d_in[1], d_in[2], d_ws, d_in[3], d_in[0], d_in[4], d_out);
}
```

```cpp
#include <hip/hip_runtime.h>
#include <hip/hip_bf16.h>

#define LAS __attribute__((address_space(3)))
typedef unsigned u32x4 __attribute__((ext_vector_type(4)));
typedef unsigned u32x2 __attribute__((ext_vector_type(2)));
typedef float f32x4 __attribute__((ext_vector_type(4)));
typedef short s16x4 __attribute__((ext_vector_type(4)));
typedef __bf16 bf16x8 __attribute__((ext_vector_type(8)));
typedef __bf16 bf16x2 __attribute__((ext_vector_type(2)));
typedef float f32x2 __attribute__((ext_vector_type(2)));
typedef unsigned long long u64;

constexpr int NTHR = 576;
constexpr int TOFF = 90, TAU = 289;
constexpr int DINIT = 4;
constexpr int DLO = 1, DMID = 3, DHI = 6;
constexpr int ELO = -40, EHI = 30;
constexpr int NTH = 16, NBOOST = 28;
constexpr int NCAL = 12, CADJ = 55;
constexpr int SPILL = 4, PARK = 36 + SPILL;
constexpr int TRIGQ = 20, TRIGS = 54;
constexpr int H1S0 = 22, H2S0 = 21;
constexpr int AUX_NT = 2, AUX_SC1 = 16;
constexpr unsigned SENT = 0xFFFFFFFFu;
constexpr unsigned SPIN_CAP = 1u << 16;
constexpr int WS_HI = 1024;
constexpr int WS_T0 = 4096;
constexpr int WS_QG = 8192;
constexpr int WS_P1 = WS_QG + 524288;
constexpr int WS_ST = WS_P1 + 262144;
constexpr int WS_P2 = WS_ST + 8192;
constexpr int WS_END = WS_P2 + 524288;
constexpr int L_IMG = 0;
constexpr int L_RED = 16384;
constexpr int L_Q = 32768;
constexpr int L_ML = L_Q + 1024;
constexpr int L_OI = L_ML + 256;
constexpr int L_AL = L_OI + 8 * 272;
constexpr int L_FLAG = L_AL + 128;
constexpr int L_X = L_FLAG + 48;
constexpr int L_SP = L_X + 8 * 272;
constexpr int L_END = L_SP + 8 * SPILL * 1024;
constexpr int F_Q = L_FLAG, F_ML = L_FLAG + 4, F_POS = L_FLAG + 8, F_LAST = L_FLAG + 12, F_WQ = L_FLAG + 16, F_AL = L_FLAG + 20, F_WO = L_FLAG + 24,
              F_REF = L_FLAG + 32, F_CAL = L_FLAG + 40;

constexpr float C2 = 0.08838834764831845f * 1.4426950408889634f;

__device__ __forceinline__ f32x4 mfma(u32x4 a, u32x4 b, f32x4 c) {
  return __builtin_amdgcn_mfma_f32_16x16x32_bf16(__builtin_bit_cast(bf16x8, a), __builtin_bit_cast(bf16x8, b), c, 0, 0, 0);
}
template <int CTRL> __device__ __forceinline__ float dppf(float x) {
  return __builtin_bit_cast(float, __builtin_amdgcn_mov_dpp(__builtin_bit_cast(int, x), CTRL, 0xf, 0xf, true));
}
template <int CTRL> __device__ __forceinline__ unsigned dppu(unsigned x) {
  return (unsigned)__builtin_amdgcn_mov_dpp((int)x, CTRL, 0xf, 0xf, true);
}
constexpr int ROR4 = 0x124, ROR8 = 0x128, ROR12 = 0x12C, QID = 0xE4, XOR1 = 0xB1, XOR2 = 0x4E;
template <int BANKS> __device__ __forceinline__ float bank_set(float old, float y) {
  return __builtin_bit_cast(float, __builtin_amdgcn_update_dpp(__builtin_bit_cast(int, old), __builtin_bit_cast(int, y), QID, 0xf, BANKS, false));
}
template <int SRC, int DST> __device__ __forceinline__ unsigned bank_move(unsigned old, unsigned y) {
  return (unsigned)__builtin_amdgcn_update_dpp((int)old, (int)y, SRC == DST ? QID : 0x120 + 4 * ((DST - SRC) & 3), 0xf, 1 << DST, false);
}
template <int SRC> __device__ __forceinline__ void spread_rows(u32x4 (&av)[4], u32x4 pa) {
#pragma unroll
  for (int k = 0; k < 4; ++k) {
    av[0][k] = bank_move<SRC, 0>(av[0][k], pa[k]); av[1][k] = bank_move<SRC, 1>(av[1][k], pa[k]);
    av[2][k] = bank_move<SRC, 2>(av[2][k], pa[k]); av[3][k] = bank_move<SRC, 3>(av[3][k], pa[k]);
  }
}
__device__ __forceinline__ float x16max(float x) {
  auto s = __builtin_amdgcn_permlane16_swap(__float_as_uint(x), __float_as_uint(x), false, false);
  return fmaxf(__uint_as_float(s[0]), __uint_as_float(s[1]));
}
__device__ __forceinline__ float x32max(float x) {
  auto s = __builtin_amdgcn_permlane32_swap(__float_as_uint(x), __float_as_uint(x), false, false);
  return fmaxf(__uint_as_float(s[0]), __uint_as_float(s[1]));
}
__device__ __forceinline__ float tsum16(float a, float b) {
  auto s = __builtin_amdgcn_permlane16_swap(__float_as_uint(a), __float_as_uint(b), false, false);
  return __uint_as_float(s[0]) + __uint_as_float(s[1]);
}
__device__ __forceinline__ float tsum32(float a, float b) {
  auto s = __builtin_amdgcn_permlane32_swap(__float_as_uint(a), __float_as_uint(b), false, false);
  return __uint_as_float(s[0]) + __uint_as_float(s[1]);
}
__device__ __forceinline__ unsigned pack_bf16(float lo, float hi) {
  f32x2 v = {lo, hi};
  return __builtin_bit_cast(unsigned, __builtin_convertvector(v, bf16x2));
}
__device__ __forceinline__ unsigned no_sent(unsigned u) { return min(u, SENT - 1u); }
__device__ __forceinline__ bool has_sent(u32x4 u) { return max(max(u.x, u.y), max(u.z, u.w)) == SENT; }
__device__ __forceinline__ void wg_barrier() { asm volatile("s_waitcnt lgkmcnt(0)\n\ts_barrier" ::: "memory"); }
__device__ __forceinline__ void lds_done() { asm volatile("s_waitcnt lgkmcnt(0)" ::: "memory"); }
__device__ __forceinline__ __amdgpu_buffer_rsrc_t mk_rsrc(u64 p, int bytes) { return __builtin_amdgcn_make_buffer_rsrc((void*)p, 0, bytes, 0x00020000); }
__device__ __forceinline__ u32x4 mk_srd(u64 p, int bytes) { return u32x4{(unsigned)p, (unsigned)(p >> 32) & 0xFFFFu, (unsigned)bytes, 0x00020000u}; }
__device__ __forceinline__ void poll1(u32x4 srd, int off, u32x4& r) {
  u64 m = ~0ull;
#pragma unroll 1
  for (unsigned spins = 0; spins < SPIN_CAP; ++spins) {
    asm volatile("s_mov_b64 exec, %2\n\tbuffer_load_dwordx4 %0, %1, %3, 0 offen sc1\n\ts_mov_b64 exec, -1\n\ts_waitcnt vmcnt(0)"
                 : "+v"(r) : "v"(off), "s"(m), "s"(srd) : "memory");
    m = __builtin_amdgcn_ballot_w64(has_sent(r));
    if (m == 0ull) break;
  }
}
__device__ __forceinline__ void poll2(u32x4 srd, int off, u32x4& r0, u32x4& r1) {
  u64 m0 = ~0ull, m1 = ~0ull;
#pragma unroll 1
  for (unsigned spins = 0; spins < SPIN_CAP; ++spins) {
    asm volatile("s_mov_b64 exec, %3\n\tbuffer_load_dwordx4 %0, %2, %5, 0 offen sc1\n\ts_mov_b64 exec, %4\n\tbuffer_load_dwordx4 %1, %2, %5, 0 offen offset:1024 sc1\n\t"
                 "s_mov_b64 exec, -1\n\ts_waitcnt vmcnt(0)"
                 : "+v"(r0), "+v"(r1) : "v"(off), "s"(m0 | 1ull), "s"(m1 | 1ull), "s"(srd) : "memory");
    m0 = __builtin_amdgcn_ballot_w64(has_sent(r0));
    m1 = __builtin_amdgcn_ballot_w64(has_sent(r1));
    if ((m0 | m1) == 0ull) break;
  }
}
__device__ __forceinline__ u32x4 tr_frag(LAS unsigned char* p) {
  const u32x2 lo = __builtin_bit_cast(u32x2, __builtin_amdgcn_ds_read_tr16_b64_v4i16((LAS s16x4*)p));
  const u32x2 hi = __builtin_bit_cast(u32x2, __builtin_amdgcn_ds_read_tr16_b64_v4i16((LAS s16x4*)(p + 256)));
  return u32x4{lo.x, lo.y, hi.x, hi.y};
}
__device__ __forceinline__ int img_row(int k) { return (k & ~12) * 32 + ((k >> 2) & 1) * 256 + ((k >> 3) & 1) * 128; }
template <int SLP> __device__ __forceinline__ void lds_wait_ge(LAS unsigned char* p, unsigned want) {
#pragma unroll 1
  for (unsigned spins = 0; spins < (SPIN_CAP << 6); ++spins) {
    if ((unsigned)__builtin_amdgcn_readfirstlane(*(volatile LAS unsigned*)p) >= want) break;
    __builtin_amdgcn_s_sleep(SLP);
  }
  asm volatile("" ::: "memory");
}
__device__ __forceinline__ void lds_count(LAS unsigned char* p) { __hip_atomic_fetch_add((LAS unsigned*)p, 1u, __ATOMIC_RELAXED, __HIP_MEMORY_SCOPE_WORKGROUP); }
typedef __attribute__((address_space(4))) const u64 karg_t;
__device__ __forceinline__ karg_t* kargs() { karg_t* p = (karg_t*)__builtin_amdgcn_kernarg_segment_ptr(); asm volatile("" : "+s"(p)); return p; }
#define SB() __builtin_amdgcn_sched_barrier(0)
#define RELANE() int tid = threadIdx.x; asm volatile("" : "+v"(tid)); const int lane = tid & 63, lg = lane >> 4, ln = lane & 15

__global__ void __launch_bounds__(NTHR, 3)
gqa_decode_kernel(const void* kc_, const void* vc_, void* ws_, const void* wq_, const void* x_, const void* wo_, void* out_) {
  karg_t* ka = kargs();
  const u64 a_k = ka[0], a_v = ka[1], a_ws = ka[2], a_wq = ka[3], a_x = ka[4];
  const u64 t0w = __builtin_amdgcn_s_memrealtime();
  const unsigned t0 = (unsigned)t0w;
  SB();
  __shared__ __attribute__((aligned(16))) unsigned char smem[L_END];
  LAS unsigned char* L = (LAS unsigned char*)smem;
  const int tid = threadIdx.x, lane = tid & 63, w = __builtin_amdgcn_readfirstlane(tid >> 6);
  const int g = blockIdx.x & 7, i = blockIdx.x >> 3;
  const int lg = lane >> 4, ln = lane & 15;
  const int rb = i >> 3, cb = i & 7;
  const int gq = i & 7, kq = i >> 3, cq = (g + gq) & 7;
  const int img_w = L_IMG + ((lane & 7) >> 1) * 4096 + img_row(lane >> 3) + (lane & 1) * 16;
  if (tid < 12) *(LAS unsigned*)(L + L_FLAG + tid * 4) = 0u;
  int koff = ((lane >> 2) & 3) * 256 + lg * 64 + (lane & 3) * 16;
  int qoff = (lane >> 3) * 8192 + (lane & 7) * 16, xoff = kq * 256 + ln * 16;
  unsigned base = (((i >> 2) * 8 + g) * 4096 + (i & 3) * 1024) * 256;
  int qs = kq * 1048576 + gq * 1024 + cq * 128 + w * 131072, qs1 = qs + 65536, xs = w * 1024, c0 = w * 4096;
  asm volatile("" : "+v"(koff), "+v"(qoff), "+v"(xoff));
  asm volatile("" : "+s"(base), "+s"(qs), "+s"(qs1), "+s"(xs), "+s"(c0) : "s"(t0w));
  wg_barrier();

  if (__builtin_expect(w >= 8, 0)) {
    RELANE();
    const int img_w = L_IMG + ((lane & 7) >> 1) * 4096 + img_row(lane >> 3) + (lane & 1) * 16;
    __builtin_amdgcn_s_setprio(3);
    const __amdgpu_buffer_rsrc_t rws = mk_rsrc(a_ws, WS_END);
    u32x4 wv[16];
    const u32x2 ep = __builtin_amdgcn_raw_buffer_load_b64(rws, 0, 0, AUX_SC1);
    const int sl = cq * 4 + kq;
    {
      const u32x4 sv = {SENT, SENT, SENT, SENT};
      __builtin_amdgcn_raw_buffer_store_b128(sv, rws, WS_P1 + gq * 32768 + sl * 1024 + lane * 16, 0, AUX_SC1);
      if (lane < 2) __builtin_amdgcn_raw_buffer_store_b128(sv, rws, WS_ST + gq * 1024 + sl * 32 + lane * 16, 0, AUX_SC1);
      const int o2 = WS_P2 + lg * 65536 + (cb * 4 + ((lane >> 2) & 3)) * 2048 + (g * 4 + rb) * 64 + (lane & 3) * 16;
      __builtin_amdgcn_raw_buffer_store_b128(sv, rws, o2, 0, AUX_SC1);
      __builtin_amdgcn_raw_buffer_store_b128(sv, rws, o2 + 4 * 65536, 0, AUX_SC1);
    }
    SB();
    lds_wait_ge<1>(L + F_WQ, 8u);
    const u64 tag = (((u64)ep.y << 32) | ep.x) + 1ull;
    const u32x2 tagv = {(unsigned)tag, (unsigned)(tag >> 32)};
    {
      LAS unsigned char* ip = L + L_IMG + (lg >> 1) * 512 + (lg & 1) * 128 + ln * 8;
      LAS unsigned char* xp = L + L_X + (ln & 7) * 272 + lg * 16;
      f32x4 d[4];
#pragma unroll
      for (int ct = 0; ct < 4; ++ct) d[ct] = f32x4{0.f, 0.f, 0.f, 0.f};
#pragma unroll
      for (int ks = 0; ks < 4; ++ks) {
        const u32x4 xb = *(LAS u32x4*)(xp + ks * 64);
#pragma unroll
        for (int ct = 0; ct < 4; ++ct) d[ct] = mfma(tr_frag(ip + ct * 4096 + ks * 1024), xb, d[ct]);
      }
      const int off = WS_QG + gq * 65536 + (ln & 7) * 8192 + kq * 2048 + cq * 256 + (ln >> 3) * 128 + lg * 16;
      __builtin_amdgcn_raw_buffer_store_b128(__builtin_bit_cast(u32x4, ln < 8 ? d[0] : d[2]), rws, off, 0, AUX_SC1);
      __builtin_amdgcn_raw_buffer_store_b128(__builtin_bit_cast(u32x4, ln < 8 ? d[1] : d[3]), rws, off + 64, 0, AUX_SC1);
      asm volatile("s_waitcnt vmcnt(0)" ::: "memory");
      if (lane == 0) __builtin_amdgcn_raw_buffer_store_b64(tagv, rws, WS_HI + gq * 256 + sl * 8, 0, AUX_SC1);
    }
    lds_wait_ge<4>(L + F_CAL, 1u);
    if (lane == 0) __builtin_amdgcn_raw_buffer_store_b64(u32x2{(*(volatile LAS unsigned*)(L + F_CAL + 4) << 16) | (t0 & 0xFFFFu), tagv.x}, rws, WS_T0 + gq * 256 + sl * 8, 0, AUX_SC1);
    const __amdgpu_buffer_rsrc_t ro = mk_rsrc(kargs()[5] + g * 524288 + cb * 128, 524288 - 1024 + 128), rout = mk_rsrc(kargs()[6], 8192);
    lds_wait_ge<4>(L + F_POS, TRIGQ);
    {
#pragma unroll 1
      for (unsigned spins = 0; spins < SPIN_CAP; ++spins) {
        asm volatile("" ::: "memory");
        const u32x2 hi = __builtin_amdgcn_raw_buffer_load_b64(rws, WS_HI + g * 256 + (lane & 31) * 8, 0, AUX_SC1);
        if (!__any(((hi.x ^ tagv.x) | (hi.y ^ tagv.y)) != 0u)) break;
        __builtin_amdgcn_s_sleep(8);
      }
      asm volatile("" ::: "memory");
      const int off = WS_QG + g * 65536 + (i >> 2) * 8192 + lane * 16;
#pragma unroll
      for (int t = 0; t < 8; ++t) wv[t] = __builtin_amdgcn_raw_buffer_load_b128(rws, off + (t & 1) * 1024, (t >> 1) * 2048, AUX_SC1);
      const u32x2 o = __builtin_amdgcn_raw_buffer_load_b64(rws, WS_T0 + g * 256 + (lane & 31) * 8, 0, AUX_SC1);
#pragma unroll
      for (int hh = 0; hh < 2; ++hh) {
        const f32x4 q4 = (__builtin_bit_cast(f32x4, wv[hh]) + __builtin_bit_cast(f32x4, wv[2 + hh])) + (__builtin_bit_cast(f32x4, wv[4 + hh]) + __builtin_bit_cast(f32x4, wv[6 + hh]));
        *(LAS u32x2*)(L + L_Q + hh * 512 + lane * 8) = u32x2{pack_bf16(q4.x, q4.y), pack_bf16(q4.z, q4.w)};
      }
      const bool ok = o.y == tagv.x;
      int dt = ok ? (int)(short)((lane < 32 ? o.x : o.x >> 16) - t0) : 0;
      dt += (int)dppu<XOR1>(dt); dt += (int)dppu<XOR2>(dt); dt += (int)dppu<ROR4>(dt); dt += (int)dppu<ROR8>(dt);
      const int cn = __builtin_popcount((unsigned)__builtin_amdgcn_ballot_w64(ok));
      const int s0 = __builtin_amdgcn_readlane(dt, 0) + __builtin_amdgcn_readlane(dt, 16), s1 = __builtin_amdgcn_readlane(dt, 32) + __builtin_amdgcn_readlane(dt, 48);
      const float mean = (float)max(s0, s1 + CADJ * cn) * __builtin_amdgcn_rcpf((float)cn);
      if (lane == 0 && cn >= 16) *(volatile LAS unsigned*)(L + F_REF) = t0 + (unsigned)(int)mean;
      lds_done();
      *(volatile LAS unsigned*)(L + F_Q) = 1u;
    }
    lds_wait_ge<4>(L + F_ML, 8u);
    const f32x4 m0 = *(LAS f32x4*)(L + L_ML + (lane & 3) * 32), m1 = *(LAS f32x4*)(L + L_ML + (lane & 3) * 32 + 16);
    {
      const f32x4 l0 = *(LAS f32x4*)(L + L_ML + 128 + (lane & 3) * 32), l1 = *(LAS f32x4*)(L + L_ML + 128 + (lane & 3) * 32 + 16);
      const float M = fmaxf(fmaxf(fmaxf(m0.x, m0.y), fmaxf(m0.z, m0.w)), fmaxf(fmaxf(m1.x, m1.y), fmaxf(m1.z, m1.w)));
      float Ls = 0.f;
#pragma unroll
      for (int k = 0; k < 4; ++k) Ls += l0[k] * __builtin_amdgcn_exp2f(m0[k] - M) + l1[k] * __builtin_amdgcn_exp2f(m1[k] - M);
      if (lane < 4) __builtin_amdgcn_raw_buffer_store_b64(u32x2{no_sent(__float_as_uint(M)), no_sent(__float_as_uint(Ls))}, rws, WS_ST + g * 1024 + i * 32 + lane * 8, 0, AUX_SC1);
    }
    lds_wait_ge<4>(L + F_POS, TRIGS);
    {
      u32x2 st;
#pragma unroll 1
      for (unsigned spins = 0; spins < SPIN_CAP; ++spins) {
        asm volatile("" ::: "memory");
        st = __builtin_amdgcn_raw_buffer_load_b64(rws, WS_ST + g * 1024 + (i >> 2) * 128 + ln * 8, 0, AUX_SC1);
        if (!__any(max(st.x, st.y) == SENT)) break;
        __builtin_amdgcn_s_sleep(8);
      }
      const float Ms = __uint_as_float(st.x);
      float Mg = fmaxf(Ms, dppf<ROR4>(Ms)); Mg = fmaxf(Mg, dppf<ROR8>(Mg));
      float Lg = __uint_as_float(st.y) * __builtin_amdgcn_exp2f(Ms - Mg);
      Lg += dppf<ROR4>(Lg); Lg += dppf<ROR8>(Lg);
      const float inv = 1.0f / Lg;
      if (lane < 4) {
#pragma unroll
        for (int k = 0; k < 4; ++k) {
          *(LAS float*)(L + L_AL + k * 16 + lane * 4) = __builtin_amdgcn_exp2f(m0[k] - Mg) * inv;
          *(LAS float*)(L + L_AL + 64 + k * 16 + lane * 4) = __builtin_amdgcn_exp2f(m1[k] - Mg) * inv;
        }
      }
      lds_done();
      *(volatile LAS unsigned*)(L + F_AL) = 1u;
    }
    lds_wait_ge<2>(L + F_LAST, 8u);
    {
      const int kk = lane >> 3;
      const int woff = (kk & 3) * 131072 + (kk >> 2) * 8192 + rb * 2048 + (lane & 7) * 16;
#pragma unroll
      for (int t = 0; t < 16; ++t) wv[t] = __builtin_amdgcn_raw_buffer_load_b128(ro, woff, (t & 7) * 16384 + (t >> 3) * 1024, AUX_NT);
    }
    int off2 = WS_P2 + (i >> 2) * 65536 + (4 * g + (i & 3)) * 2048 + lane * 16, offo = (i >> 2) * 1024 + (4 * g + (i & 3)) * 32 + ln * 8 + lg * 2;
    asm volatile("" : "+v"(off2), "+v"(offo));
    SB();
    __builtin_amdgcn_s_setprio(0);
    wg_barrier();
    __builtin_amdgcn_s_sleep(6);
#pragma unroll
    for (int t = 0; t < 16; ++t) *(LAS u32x4*)(L + img_w + img_row(8 * t)) = wv[t];
    lds_done();
    *(volatile LAS unsigned*)(L + F_WO) = 1u;
    wg_barrier();
    {
      u32x4 p0 = {SENT, SENT, SENT, SENT}, p1 = p0;
      __builtin_amdgcn_s_sleep(H2S0);
      poll2(mk_srd(a_ws, WS_END), off2, p0, p1);
      const f32x4 o = __builtin_bit_cast(f32x4, p0) + __builtin_bit_cast(f32x4, p1);
      float v = tsum32(tsum16(o.x, o.y), tsum16(o.z, o.w));
      v += dppf<ROR4>(v); v += dppf<ROR8>(v);
      if (ln < 4) __builtin_amdgcn_raw_buffer_store_b16((unsigned short)pack_bf16(v, v), rout, offo, 0, 0);
      if (blockIdx.x == 0 && lane == 0) __builtin_amdgcn_raw_buffer_store_b64(tagv, rws, 0, 0, AUX_SC1);
    }
  } else {
#define CHUNK(c) ((c) * 32768 + ((w + (c)) & 7) * 4096)
    const int voff = lane * 16;
    const __amdgpu_buffer_rsrc_t rk = mk_rsrc(a_k + base, 262144), rv = mk_rsrc(a_v + base, 262144), rws = mk_rsrc(a_ws, WS_END);
    u32x4 kr[32], vr[32];
    const __amdgpu_buffer_rsrc_t rq = mk_rsrc(a_wq, 4194304), rx = mk_rsrc(a_x, 8192);
    const u32x4 wq0 = __builtin_amdgcn_raw_buffer_load_b128(rq, qoff, qs, AUX_NT);
    const u32x4 wq1 = __builtin_amdgcn_raw_buffer_load_b128(rq, qoff, qs1, AUX_NT);
    const u32x4 xq = __builtin_amdgcn_raw_buffer_load_b128(rx, xoff, xs, 0);
#pragma unroll
    for (int n = 0; n < DINIT; ++n) kr[n] = __builtin_amdgcn_raw_buffer_load_b128(rk, koff + n * 1024, c0, AUX_NT);
    SB();
    *(LAS u32x4*)(L + img_w + w * 512) = wq0;
    *(LAS u32x4*)(L + img_w + w * 512 + 128) = wq1;
    *(LAS u32x4*)(L + L_X + w * 272 + ln * 16) = xq;
    lds_done();
    if (lane == 0) lds_count(L + F_WQ);
    SB();
    unsigned tref = t0;
    int lag = 0;
#define ISSUE(n) do { \
      __builtin_amdgcn_s_waitcnt(0x0F70 | (DHI - 1)); \
      if ((n) < NBOOST || lag < EHI) { __builtin_amdgcn_s_waitcnt(0x0F70 | (DMID - 1)); if ((n) >= NTH && lag < ELO) __builtin_amdgcn_s_waitcnt(0x0F70 | (DLO - 1)); } \
      if ((n) < 32) kr[(n) & 31] = __builtin_amdgcn_raw_buffer_load_b128(rk, koff + ((n) & 3) * 1024, CHUNK(((n) & 31) >> 2), AUX_NT); \
      else vr[(n) & 31] = __builtin_amdgcn_raw_buffer_load_b128(rv, voff + ((n) & 3) * 1024, CHUNK(((n) & 31) >> 2), AUX_NT); \
      SB(); \
      if (((n) & 1) == 0 && (n) >= NCAL) { \
        if ((n) >= 24 && (n) <= PARK + 4) { const unsigned r_ = __builtin_amdgcn_readfirstlane(*(volatile LAS unsigned*)(L + F_REF)); tref = r_ ? r_ : tref; } \
        lag = (int)((unsigned)__builtin_amdgcn_s_memrealtime() - tref) - (TOFF + ((n) * TAU) / 10); \
        if ((n) == NCAL && w == 3) *(volatile LAS u32x2*)(L + F_CAL) = u32x2{1u, tref + (unsigned)lag}; } \
      if ((n) == TRIGQ || (n) == TRIGS) *(volatile LAS unsigned*)(L + F_POS) = (n); \
      if ((n) == 63 && lane == 0) lds_count(L + F_LAST); \
      SB(); } while (0)
#pragma unroll
    for (int n = DINIT; n < PARK; ++n) {
      ISSUE(n);
      if (n >= 12 && n < 12 + SPILL) { *(LAS u32x4*)(L + L_SP + (w * SPILL + (n - 12)) * 1024 + voff) = kr[n - 12]; SB(); }
    }
    {
    RELANE();
    lds_wait_ge<1>(L + F_Q, 1u);
    const u32x4 qf = *(LAS u32x4*)(L + L_Q + (lane & 3) * 256 + (4 * lg + ((lane >> 2) & 3)) * 16);
    float sc[8];
    constexpr int NQ = PARK + 4;
#pragma unroll
    for (int uu = 0; uu < 32; uu += 4) {
      const int u0 = (uu + SPILL) & 31, t = u0 >> 3;
      f32x4 d[4];
      float y[4];
#pragma unroll
      for (int k = 0; k < 4; ++k) d[k] = mfma(u0 + k < SPILL ? *(LAS u32x4*)(L + L_SP + (w * SPILL + u0 + k) * 1024 + voff) : kr[u0 + k], qf, f32x4{0.f, 0.f, 0.f, 0.f});
      SB();
#pragma unroll
      for (int k = 0; k < 4; ++k) y[k] = d[k][t] + dppf<ROR12>(d[k][(t + 1) & 3]);
      SB();
#pragma unroll
      for (int k = 0; k < 4; ++k) y[k] += dppf<ROR8>(d[k][(t + 2) & 3]);
      SB();
#pragma unroll
      for (int k = 0; k < 4; ++k) y[k] += dppf<ROR4>(d[k][(t + 3) & 3]);
      SB();
#pragma unroll
      for (int k = 0; k < 4; ++k) {
        const int T = (u0 + k) & 7;
        sc[T] = uu < 8 ? y[k] : t == 0 ? bank_set<1>(sc[T], y[k]) : t == 1 ? bank_set<2>(sc[T], y[k]) : t == 2 ? bank_set<4>(sc[T], y[k]) : bank_set<8>(sc[T], y[k]);
      }
      SB();
      if ((uu & 4) == 0) ISSUE(PARK + (uu >> 3));
    }
    float m = fmaxf(fmaxf(fmaxf(sc[0], sc[1]), fmaxf(sc[2], sc[3])), fmaxf(fmaxf(sc[4], sc[5]), fmaxf(sc[6], sc[7])));
    m = fmaxf(m, dppf<ROR4>(m)); m = fmaxf(m, dppf<ROR8>(m)); m = x32max(x16max(m));
    m *= C2;
    float ls = 0.f;
#pragma unroll
    for (int T = 0; T < 8; ++T) { sc[T] = __builtin_amdgcn_exp2f(fmaf(sc[T], C2, -m)); ls += sc[T]; }
    ls += dppf<ROR4>(ls); ls += dppf<ROR8>(ls); ls = tsum16(ls, ls); ls = tsum32(ls, ls);
    if (lane < 4) { *(LAS float*)(L + L_ML + lane * 32 + w * 4) = m; *(LAS float*)(L + L_ML + 128 + lane * 32 + w * 4) = ls; }
    lds_done();
    if (lane == 0) lds_count(L + F_ML);
    u32x4 pa;
#pragma unroll
    for (int k = 0; k < 4; ++k) pa[k] = pack_bf16(sc[2 * k], sc[2 * k + 1]);
    f32x4 acc[2] = {{0.f, 0.f, 0.f, 0.f}, {0.f, 0.f, 0.f, 0.f}}, al[8];
    u32x4 av[4] = {{0u, 0u, 0u, 0u}, {0u, 0u, 0u, 0u}, {0u, 0u, 0u, 0u}, {0u, 0u, 0u, 0u}};
    int off1 = 0, offp = 0, offd = 0, offr = 0;
    SB();
#pragma unroll
    for (int t = 0; t < 4; ++t) {
#pragma unroll
      for (int n = NQ; n < 64; ++n) if ((n > 31 + 8 * t + DHI && n <= 39 + 8 * t + DHI) || (t == 0 && n <= 31 + DHI)) ISSUE(n);
      if (t == 3) {
        off1 = WS_P1 + g * 32768 + (tid >> 5) * 8192 + i * 256 + (tid & 31) * 8;
        offp = WS_P1 + g * 32768 + rb * 8192 + w * 1024 + lane * 16;
        offd = WS_P2 + ln * 65536 + (cb * 4 + w) * 2048 + (g * 4 + rb) * 64 + lg * 16;
        offr = L_RED + w * 2048 + voff;
        asm volatile("" : "+v"(off1), "+v"(offp), "+v"(offd), "+v"(offr));
        if (w < 2) {
          lds_wait_ge<1>(L + F_AL, 1u);
#pragma unroll
          for (int ww = 0; ww < 8; ++ww) al[ww] = *(LAS f32x4*)(L + L_AL + ww * 16);
          asm volatile("" : "+v"(al[0]), "+v"(al[1]), "+v"(al[2]), "+v"(al[3]), "+v"(al[4]), "+v"(al[5]), "+v"(al[6]), "+v"(al[7]));
        }
      }
      u32x4 bb[8];
      if (t == 0) spread_rows<0>(av, pa); else if (t == 1) spread_rows<1>(av, pa); else if (t == 2) spread_rows<2>(av, pa); else spread_rows<3>(av, pa);
      if (t < 3) {
#pragma unroll
        for (int j = 0; j < 8; ++j) {
#pragma unroll
          for (int k = 0; k < 4; ++k) bb[j][k] = __builtin_amdgcn_perm(vr[8 * t + 2 * k + 1][j >> 1], vr[8 * t + 2 * k][j >> 1], (j & 1) ? 0x07060302u : 0x05040100u);
          acc[j >> 2] = mfma(av[j & 3], bb[j], acc[j >> 2]);
        }
        SB();
      } else {
        const unsigned e0 = __builtin_amdgcn_perm(av[1][3], av[0][3], 0x05040100u), e1 = __builtin_amdgcn_perm(av[3][3], av[2][3], 0x05040100u);
        const unsigned f0 = __builtin_amdgcn_perm(av[1][3], av[0][3], 0x07060302u), f1 = __builtin_amdgcn_perm(av[3][3], av[2][3], 0x07060302u);
        u32x4 s0 = {e0, e1, 0u, 0u}, s1 = {0u, 0u, e0, e1}, s2 = {f0, f1, 0u, 0u}, s3 = {0u, 0u, f0, f1};
        asm volatile("" : "+v"(s0), "+v"(s1), "+v"(s2), "+v"(s3));
#pragma unroll
        for (int k = 0; k < 3; ++k) {
#pragma unroll
          for (int j = 0; j < 8; ++j) bb[j][k] = __builtin_amdgcn_perm(vr[24 + 2 * k + 1][j >> 1], vr[24 + 2 * k][j >> 1], (j & 1) ? 0x07060302u : 0x05040100u);
          SB();
        }
#pragma unroll
        for (int j = 0; j < 8; ++j) { bb[j][3] = 0u; acc[j >> 2] = mfma(av[j & 3], bb[j], acc[j >> 2]); }
        SB();
        acc[0] = mfma(s0, vr[30], acc[0]); acc[1] = mfma(s1, vr[30], acc[1]);
        SB();
        acc[0] = mfma(s2, vr[31], acc[0]); acc[1] = mfma(s3, vr[31], acc[1]);
        SB();
      }
    }
    *(LAS f32x4*)(L + offr) = acc[0];
    *(LAS f32x4*)(L + offr + 1024) = acc[1];
    wg_barrier();
    if (w < 2) {
      f32x4 rd[8];
#pragma unroll
      for (int ww = 0; ww < 8; ++ww) rd[ww] = *(LAS f32x4*)(L + L_RED + ww * 2048 + tid * 16);
      SB();
      f32x4 v = rd[0] * al[0];
#pragma unroll
      for (int ww = 1; ww < 8; ++ww) v = __builtin_elementwise_fma(rd[ww], al[ww], v);
      const unsigned h0 = no_sent(pack_bf16(v.x, v.y)), h1 = no_sent(pack_bf16(v.z, v.w));
      const u32x4 pk = {h0, h1, dppu<XOR1>(h0), dppu<XOR1>(h1)};
      if (!(lane & 1)) __builtin_amdgcn_raw_buffer_store_b128(pk, rws, off1, 0, AUX_SC1);
    }
    u32x4 wa[4];
    __builtin_amdgcn_s_sleep(H1S0 - 4);
    lds_wait_ge<1>(L + F_WO, 1u);
#pragma unroll
    for (int ks = 0; ks < 4; ++ks) wa[ks] = tr_frag(L + L_IMG + (w & 3) * 4096 + (lg >> 1) * 512 + (lg & 1) * 128 + ln * 8 + ks * 1024);
    int oiw = L_OI + w * 272 + ln * 16 + lg * 4, oir = L_OI + (ln & 7) * 272 + lg * 16;
    asm volatile("" : "+v"(wa[0]), "+v"(wa[1]), "+v"(wa[2]), "+v"(wa[3]), "+v"(oiw), "+v"(oir));
    {
      u32x4 p = {SENT, SENT, SENT, SENT};
      poll1(mk_srd(a_ws, WS_END), offp, p);
      const float e0 = __uint_as_float(p.x << 16), e1 = __uint_as_float(p.x & 0xFFFF0000u), e2 = __uint_as_float(p.y << 16), e3 = __uint_as_float(p.y & 0xFFFF0000u);
      const float e4 = __uint_as_float(p.z << 16), e5 = __uint_as_float(p.z & 0xFFFF0000u), e6 = __uint_as_float(p.w << 16), e7 = __uint_as_float(p.w & 0xFFFF0000u);
      SB();
      const float a0 = tsum16(e0, e2), a1 = tsum16(e1, e3), a2 = tsum16(e4, e6), a3 = tsum16(e5, e7);
      *(LAS unsigned*)(L + oiw) = pack_bf16(tsum32(a0, a2), tsum32(a1, a3));
    }
    wg_barrier();
    if (w < 4) {
      u32x4 ob[4];
#pragma unroll
      for (int ks = 0; ks < 4; ++ks) ob[ks] = *(LAS u32x4*)(L + oir + ks * 64);
      SB();
      f32x4 d = {0.f, 0.f, 0.f, 0.f};
#pragma unroll
      for (int ks = 0; ks < 4; ++ks) d = mfma(wa[ks], ob[ks], d);
      if (ln < 8) __builtin_amdgcn_raw_buffer_store_b128(u32x4{no_sent(__float_as_uint(d.x)), no_sent(__float_as_uint(d.y)), no_sent(__float_as_uint(d.z)), no_sent(__float_as_uint(d.w))}, rws, offd, 0, AUX_SC1);
    }
    }
  }
}

extern "C" void kernel_launch(void* const* d_in, const int* in_sizes, int n_in, void* d_out, int out_size, void* d_ws, size_t ws_size,
                              hipStream_t stream) {
  gqa_decode_kernel<<<256, NTHR, 0, stream>>>(d_in[1], d_in[2], d_ws, d_in[3], d_in[0], d_in[4], d_out);
}
```

```cpp
#define PK 1
#include <hip/hip_runtime.h>
#include <hip/hip_bf16.h>

#define LAS __attribute__((address_space(3)))
typedef unsigned u32x4 __attribute__((ext_vector_type(4)));
typedef unsigned u32x2 __attribute__((ext_vector_type(2)));
typedef float f32x4 __attribute__((ext_vector_type(4)));
typedef short s16x4 __attribute__((ext_vector_type(4)));
typedef __bf16 bf16x8 __attribute__((ext_vector_type(8)));
typedef __bf16 bf16x2 __attribute__((ext_vector_type(2)));
typedef float f32x2 __attribute__((ext_vector_type(2)));
typedef unsigned long long u64;

constexpr int NTHR = 576;
constexpr int TOFF = 90, TAU = 289;
constexpr int DINIT = 4;
constexpr int DLO = 1, DMID = 3, DHI = 6;
constexpr int ELO = -40, EHI = 30;
constexpr int NTH = 16, NBOOST = 28;
constexpr int NCAL = 12, CADJ = 55;
constexpr int SPILL = 4, PARK = 36 + SPILL;
constexpr int TRIGQ = 20, TRIGS = 54;
constexpr int H1S0 = 22, H2S0 = 21;
constexpr int AUX_NT = 2, AUX_SC1 = 16;
constexpr unsigned SENT = 0xFFFFFFFFu;
constexpr unsigned SPIN_CAP = 1u << 16;
constexpr int WS_HI = 1024;
constexpr int WS_T0 = 4096;
constexpr int WS_QG = 8192;
constexpr int WS_P1 = WS_QG + 524288;
constexpr int WS_ST = WS_P1 + 262144;
constexpr int WS_P2 = WS_ST + 8192;
constexpr int WS_END = WS_P2 + 524288;
constexpr int L_IMG = 0;
constexpr int L_RED = 16384;
constexpr int L_Q = 32768;
constexpr int L_ML = L_Q + 1024;
constexpr int L_OI = L_ML + 256;
constexpr int L_AL = L_OI + 8 * 272;
constexpr int L_FLAG = L_AL + 128;
constexpr int L_X = L_FLAG + 48;
constexpr int L_SP = L_X + 8 * 272;
constexpr int L_END = L_SP + 8 * SPILL * 1024;
constexpr int F_Q = L_FLAG, F_ML = L_FLAG + 4, F_POS = L_FLAG + 8, F_LAST = L_FLAG + 12, F_WQ = L_FLAG + 16, F_AL = L_FLAG + 20, F_WO = L_FLAG + 24,
              F_REF = L_FLAG + 32, F_CAL = L_FLAG + 40;

constexpr float C2 = 0.08838834764831845f * 1.4426950408889634f;

__device__ __forceinline__ f32x4 mfma(u32x4 a, u32x4 b, f32x4 c) {
  return __builtin_amdgcn_mfma_f32_16x16x32_bf16(__builtin_bit_cast(bf16x8, a), __builtin_bit_cast(bf16x8, b), c, 0, 0, 0);
}
template <int CTRL> __device__ __forceinline__ float dppf(float x) {
  return __builtin_bit_cast(float, __builtin_amdgcn_mov_dpp(__builtin_bit_cast(int, x), CTRL, 0xf, 0xf, true));
}
template <int CTRL> __device__ __forceinline__ unsigned dppu(unsigned x) {
  return (unsigned)__builtin_amdgcn_mov_dpp((int)x, CTRL, 0xf, 0xf, true);
}
constexpr int ROR4 = 0x124, ROR8 = 0x128, ROR12 = 0x12C, QID = 0xE4, XOR1 = 0xB1, XOR2 = 0x4E;
template <int BANKS> __device__ __forceinline__ float bank_set(float old, float y) {
  return __builtin_bit_cast(float, __builtin_amdgcn_update_dpp(__builtin_bit_cast(int, old), __builtin_bit_cast(int, y), QID, 0xf, BANKS, false));
}
template <int SRC, int DST> __device__ __forceinline__ unsigned bank_move(unsigned old, unsigned y) {
  return (unsigned)__builtin_amdgcn_update_dpp((int)old, (int)y, SRC == DST ? QID : 0x120 + 4 * ((DST - SRC) & 3), 0xf, 1 << DST, false);
}
template <int SRC> __device__ __forceinline__ void spread_rows(u32x4 (&av)[4], u32x4 pa) {
#pragma unroll
  for (int k = 0; k < 4; ++k) {
    av[0][k] = bank_move<SRC, 0>(av[0][k], pa[k]); av[1][k] = bank_move<SRC, 1>(av[1][k], pa[k]);
    av[2][k] = bank_move<SRC, 2>(av[2][k], pa[k]); av[3][k] = bank_move<SRC, 3>(av[3][k], pa[k]);
  }
}
__device__ __forceinline__ float x16max(float x) {
  auto s = __builtin_amdgcn_permlane16_swap(__float_as_uint(x), __float_as_uint(x), false, false);
  return fmaxf(__uint_as_float(s[0]), __uint_as_float(s[1]));
}
__device__ __forceinline__ float x32max(float x) {
  auto s = __builtin_amdgcn_permlane32_swap(__float_as_uint(x), __float_as_uint(x), false, false);
  return fmaxf(__uint_as_float(s[0]), __uint_as_float(s[1]));
}
__device__ __forceinline__ float tsum16(float a, float b) {
  auto s = __builtin_amdgcn_permlane16_swap(__float_as_uint(a), __float_as_uint(b), false, false);
  return __uint_as_float(s[0]) + __uint_as_float(s[1]);
}
__device__ __forceinline__ float tsum32(float a, float b) {
  auto s = __builtin_amdgcn_permlane32_swap(__float_as_uint(a), __float_as_uint(b), false, false);
  return __uint_as_float(s[0]) + __uint_as_float(s[1]);
}
__device__ __forceinline__ unsigned pack_bf16(float lo, float hi) {
  f32x2 v = {lo, hi};
  return __builtin_bit_cast(unsigned, __builtin_convertvector(v, bf16x2));
}
__device__ __forceinline__ unsigned no_sent(unsigned u) { return min(u, SENT - 1u); }
__device__ __forceinline__ bool has_sent(u32x4 u) { return max(max(u.x, u.y), max(u.z, u.w)) == SENT; }
__device__ __forceinline__ void wg_barrier() { asm volatile("s_waitcnt lgkmcnt(0)\n\ts_barrier" ::: "memory"); }
__device__ __forceinline__ void lds_done() { asm volatile("s_waitcnt lgkmcnt(0)" ::: "memory"); }
__device__ __forceinline__ __amdgpu_buffer_rsrc_t mk_rsrc(u64 p, int bytes) { return __builtin_amdgcn_make_buffer_rsrc((void*)p, 0, bytes, 0x00020000); }
__device__ __forceinline__ u32x4 mk_srd(u64 p, int bytes) { return u32x4{(unsigned)p, (unsigned)(p >> 32) & 0xFFFFu, (unsigned)bytes, 0x00020000u}; }
__device__ __forceinline__ void poll1(u32x4 srd, int off, u32x4& r) {
  u64 m = ~0ull;
#pragma unroll 1
  for (unsigned spins = 0; spins < SPIN_CAP; ++spins) {
    asm volatile("s_mov_b64 exec, %2\n\tbuffer_load_dwordx4 %0, %1, %3, 0 offen sc1\n\ts_mov_b64 exec, -1\n\ts_waitcnt vmcnt(0)"
                 : "+v"(r) : "v"(off), "s"(m), "s"(srd) : "memory");
    m = __builtin_amdgcn_ballot_w64(has_sent(r));
    if (m == 0ull) break;
  }
}
__device__ __forceinline__ void poll2(u32x4 srd, int off, u32x4& r0, u32x4& r1) {
  u64 m0 = ~0ull, m1 = ~0ull;
#pragma unroll 1
  for (unsigned spins = 0; spins < SPIN_CAP; ++spins) {
    asm volatile("s_mov_b64 exec, %3\n\tbuffer_load_dwordx4 %0, %2, %5, 0 offen sc1\n\ts_mov_b64 exec, %4\n\tbuffer_load_dwordx4 %1, %2, %5, 0 offen offset:1024 sc1\n\t"
                 "s_mov_b64 exec, -1\n\ts_waitcnt vmcnt(0)"
                 : "+v"(r0), "+v"(r1) : "v"(off), "s"(m0 | 1ull), "s"(m1 | 1ull), "s"(srd) : "memory");
    m0 = __builtin_amdgcn_ballot_w64(has_sent(r0));
    m1 = __builtin_amdgcn_ballot_w64(has_sent(r1));
    if ((m0 | m1) == 0ull) break;
  }
}
__device__ __forceinline__ u32x4 tr_frag(LAS unsigned char* p) {
  const u32x2 lo = __builtin_bit_cast(u32x2, __builtin_amdgcn_ds_read_tr16_b64_v4i16((LAS s16x4*)p));
  const u32x2 hi = __builtin_bit_cast(u32x2, __builtin_amdgcn_ds_read_tr16_b64_v4i16((LAS s16x4*)(p + 256)));
  return u32x4{lo.x, lo.y, hi.x, hi.y};
}
__device__ __forceinline__ int img_row(int k) { return (k & ~12) * 32 + ((k >> 2) & 1) * 256 + ((k >> 3) & 1) * 128; }
template <int SLP> __device__ __forceinline__ void lds_wait_ge(LAS unsigned char* p, unsigned want) {
#pragma unroll 1
  for (unsigned spins = 0; spins < (SPIN_CAP << 6); ++spins) {
    if ((unsigned)__builtin_amdgcn_readfirstlane(*(volatile LAS unsigned*)p) >= want) break;
    __builtin_amdgcn_s_sleep(SLP);
  }
  asm volatile("" ::: "memory");
}
__device__ __forceinline__ void lds_count(LAS unsigned char* p) { __hip_atomic_fetch_add((LAS unsigned*)p, 1u, __ATOMIC_RELAXED, __HIP_MEMORY_SCOPE_WORKGROUP); }
typedef __attribute__((address_space(4))) const u64 karg_t;
__device__ __forceinline__ karg_t* kargs() { karg_t* p = (karg_t*)__builtin_amdgcn_kernarg_segment_ptr(); asm volatile("" : "+s"(p)); return p; }
#define SB() __builtin_amdgcn_sched_barrier(0)
#define RELANE() int tid = threadIdx.x; asm volatile("" : "+v"(tid)); const int lane = tid & 63, lg = lane >> 4, ln = lane & 15

__global__ void __launch_bounds__(NTHR, 3)
gqa_decode_kernel(const void* kc_, const void* vc_, void* ws_, const void* wq_, const void* x_, const void* wo_, void* out_) {
  karg_t* ka = kargs();
  const u64 a_k = ka[0], a_v = ka[1], a_ws = ka[2], a_wq = ka[3], a_x = ka[4];
  const u64 t0w = __builtin_amdgcn_s_memrealtime();
  const unsigned t0 = (unsigned)t0w;
  SB();
  __shared__ __attribute__((aligned(16))) unsigned char smem[L_END];
  LAS unsigned char* L = (LAS unsigned char*)smem;
  const int tid = threadIdx.x, lane = tid & 63, w = __builtin_amdgcn_readfirstlane(tid >> 6);
  const int g = blockIdx.x & 7, i = blockIdx.x >> 3;
  const int lg = lane >> 4, ln = lane & 15;
  const int rb = i >> 3, cb = i & 7;
  const int gq = i & 7, kq = i >> 3, cq = (g + gq) & 7;
  const int img_w = L_IMG + ((lane & 7) >> 1) * 4096 + img_row(lane >> 3) + (lane & 1) * 16;
  if (tid < 12) *(LAS unsigned*)(L + L_FLAG + tid * 4) = 0u;
  int koff = ((lane >> 2) & 3) * 256 + lg * 64 + (lane & 3) * 16;
  int qoff = (lane >> 3) * 8192 + (lane & 7) * 16, xoff = kq * 256 + ln * 16;
  unsigned base = (((i >> 2) * 8 + g) * 4096 + (i & 3) * 1024) * 256;
  int qs = kq * 1048576 + gq * 1024 + cq * 128 + w * 131072, qs1 = qs + 65536, xs = w * 1024, c0 = w * 4096;
  asm volatile("" : "+v"(koff), "+v"(qoff), "+v"(xoff));
  asm volatile("" : "+s"(base), "+s"(qs), "+s"(qs1), "+s"(xs), "+s"(c0) : "s"(t0w));
  wg_barrier();

  if (__builtin_expect(w >= 8, 0)) {
    RELANE();
    const int img_w = L_IMG + ((lane & 7) >> 1) * 4096 + img_row(lane >> 3) + (lane & 1) * 16;
    int pv_ = 0;
    __builtin_amdgcn_s_setprio(3);
    const __amdgpu_buffer_rsrc_t rws = mk_rsrc(a_ws, WS_END);
    u32x4 wv[16];
    const u32x2 ep = __builtin_amdgcn_raw_buffer_load_b64(rws, 0, 0, AUX_SC1);
    const int sl = cq * 4 + kq;
    {
      const u32x4 sv = {SENT, SENT, SENT, SENT};
      __builtin_amdgcn_raw_buffer_store_b128(sv, rws, WS_P1 + gq * 32768 + sl * 1024 + lane * 16, 0, AUX_SC1);
      if (lane < 2) __builtin_amdgcn_raw_buffer_store_b128(sv, rws, WS_ST + gq * 1024 + sl * 32 + lane * 16, 0, AUX_SC1);
      const int o2 = WS_P2 + lg * 65536 + (cb * 4 + ((lane >> 2) & 3)) * 2048 + (g * 4 + rb) * 64 + (lane & 3) * 16;
      __builtin_amdgcn_raw_buffer_store_b128(sv, rws, o2, 0, AUX_SC1);
      __builtin_amdgcn_raw_buffer_store_b128(sv, rws, o2 + 4 * 65536, 0, AUX_SC1);
    }
    SB();
    lds_wait_ge<1>(L + F_WQ, 8u);
    const u64 tag = (((u64)ep.y << 32) | ep.x) + 1ull;
    const u32x2 tagv = {(unsigned)tag, (unsigned)(tag >> 32)};
    {
      LAS unsigned char* ip = L + L_IMG + (lg >> 1) * 512 + (lg & 1) * 128 + ln * 8;
      LAS unsigned char* xp = L + L_X + (ln & 7) * 272 + lg * 16;
      f32x4 d[4];
#pragma unroll
      for (int ct = 0; ct < 4; ++ct) d[ct] = f32x4{0.f, 0.f, 0.f, 0.f};
#pragma unroll
      for (int ks = 0; ks < 4; ++ks) {
        const u32x4 xb = *(LAS u32x4*)(xp + ks * 64);
#pragma unroll
        for (int ct = 0; ct < 4; ++ct) d[ct] = mfma(tr_frag(ip + ct * 4096 + ks * 1024), xb, d[ct]);
      }
      const int off = WS_QG + gq * 65536 + (ln & 7) * 8192 + kq * 2048 + cq * 256 + (ln >> 3) * 128 + lg * 16;
      __builtin_amdgcn_raw_buffer_store_b128(__builtin_bit_cast(u32x4, ln < 8 ? d[0] : d[2]), rws, off, 0, AUX_SC1);
      __builtin_amdgcn_raw_buffer_store_b128(__builtin_bit_cast(u32x4, ln < 8 ? d[1] : d[3]), rws, off + 64, 0, AUX_SC1);
      asm volatile("s_waitcnt vmcnt(0)" ::: "memory");
      if (lane == 0) __builtin_amdgcn_raw_buffer_store_b64(tagv, rws, WS_HI + gq * 256 + sl * 8, 0, AUX_SC1);
    }
    lds_wait_ge<4>(L + F_CAL, 1u);
    if (lane == 0) __builtin_amdgcn_raw_buffer_store_b64(u32x2{(*(volatile LAS unsigned*)(L + F_CAL + 4) << 16) | (t0 & 0xFFFFu), tagv.x}, rws, WS_T0 + gq * 256 + sl * 8, 0, AUX_SC1);
    const __amdgpu_buffer_rsrc_t ro = mk_rsrc(kargs()[5] + g * 524288 + cb * 128, 524288 - 1024 + 128), rout = mk_rsrc(kargs()[6], 8192);
    lds_wait_ge<4>(L + F_POS, TRIGQ);
    {
#pragma unroll 1
      for (unsigned spins = 0; spins < SPIN_CAP; ++spins) {
        asm volatile("" ::: "memory");
        const u32x2 hi = __builtin_amdgcn_raw_buffer_load_b64(rws, WS_HI + g * 256 + (lane & 31) * 8, 0, AUX_SC1);
        if (!__any(((hi.x ^ tagv.x) | (hi.y ^ tagv.y)) != 0u)) break;
        __builtin_amdgcn_s_sleep(8);
      }
      asm volatile("" ::: "memory");
      const int off = WS_QG + g * 65536 + (i >> 2) * 8192 + lane * 16;
#pragma unroll
      for (int t = 0; t < 8; ++t) wv[t] = __builtin_amdgcn_raw_buffer_load_b128(rws, off + (t & 1) * 1024, (t >> 1) * 2048, AUX_SC1);
      const u32x2 o = __builtin_amdgcn_raw_buffer_load_b64(rws, WS_T0 + g * 256 + (lane & 31) * 8, 0, AUX_SC1);
#pragma unroll
      for (int hh = 0; hh < 2; ++hh) {
        const f32x4 q4 = (__builtin_bit_cast(f32x4, wv[hh]) + __builtin_bit_cast(f32x4, wv[2 + hh])) + (__builtin_bit_cast(f32x4, wv[4 + hh]) + __builtin_bit_cast(f32x4, wv[6 + hh]));
        *(LAS u32x2*)(L + L_Q + hh * 512 + lane * 8) = u32x2{pack_bf16(q4.x, q4.y), pack_bf16(q4.z, q4.w)};
      }
      const bool ok = o.y == tagv.x;
      int dt = ok ? (int)(short)((lane < 32 ? o.x : o.x >> 16) - t0) : 0;
      dt += (int)dppu<XOR1>(dt); dt += (int)dppu<XOR2>(dt); dt += (int)dppu<ROR4>(dt); dt += (int)dppu<ROR8>(dt);
      const int cn = __builtin_popcount((unsigned)__builtin_amdgcn_ballot_w64(ok));
      const int s0 = __builtin_amdgcn_readlane(dt, 0) + __builtin_amdgcn_readlane(dt, 16), s1 = __builtin_amdgcn_readlane(dt, 32) + __builtin_amdgcn_readlane(dt, 48);
      const float mean = (float)max(s0, s1 + CADJ * cn) * __builtin_amdgcn_rcpf((float)cn);
      if (PK == 1) pv_ = 16 * max(0, (s1 - s0) / max(cn, 1) + 150);
      if (PK == 6) pv_ = 16 * max(0, (int)mean);
      if (lane == 0 && cn >= 16) *(volatile LAS unsigned*)(L + F_REF) = t0 + (unsigned)(int)mean;
      lds_done();
      *(volatile LAS unsigned*)(L + F_Q) = 1u;
    }
    lds_wait_ge<4>(L + F_ML, 8u);
    const f32x4 m0 = *(LAS f32x4*)(L + L_ML + (lane & 3) * 32), m1 = *(LAS f32x4*)(L + L_ML + (lane & 3) * 32 + 16);
    {
      const f32x4 l0 = *(LAS f32x4*)(L + L_ML + 128 + (lane & 3) * 32), l1 = *(LAS f32x4*)(L + L_ML + 128 + (lane & 3) * 32 + 16);
      const float M = fmaxf(fmaxf(fmaxf(m0.x, m0.y), fmaxf(m0.z, m0.w)), fmaxf(fmaxf(m1.x, m1.y), fmaxf(m1.z, m1.w)));
      float Ls = 0.f;
#pragma unroll
      for (int k = 0; k < 4; ++k) Ls += l0[k] * __builtin_amdgcn_exp2f(m0[k] - M) + l1[k] * __builtin_amdgcn_exp2f(m1[k] - M);
      if (lane < 4) __builtin_amdgcn_raw_buffer_store_b64(u32x2{no_sent(__float_as_uint(M)), no_sent(__float_as_uint(Ls))}, rws, WS_ST + g * 1024 + i * 32 + lane * 8, 0, AUX_SC1);
    }
    lds_wait_ge<4>(L + F_POS, TRIGS);
    {
      u32x2 st;
#pragma unroll 1
      for (unsigned spins = 0; spins < SPIN_CAP; ++spins) {
        asm volatile("" ::: "memory");
        st = __builtin_amdgcn_raw_buffer_load_b64(rws, WS_ST + g * 1024 + (i >> 2) * 128 + ln * 8, 0, AUX_SC1);
        if (!__any(max(st.x, st.y) == SENT)) break;
        __builtin_amdgcn_s_sleep(8);
      }
      const float Ms = __uint_as_float(st.x);
      float Mg = fmaxf(Ms, dppf<ROR4>(Ms)); Mg = fmaxf(Mg, dppf<ROR8>(Mg));
      float Lg = __uint_as_float(st.y) * __builtin_amdgcn_exp2f(Ms - Mg);
      Lg += dppf<ROR4>(Lg); Lg += dppf<ROR8>(Lg);
      const float inv = 1.0f / Lg;
      if (lane < 4) {
#pragma unroll
        for (int k = 0; k < 4; ++k) {
          *(LAS float*)(L + L_AL + k * 16 + lane * 4) = __builtin_amdgcn_exp2f(m0[k] - Mg) * inv;
          *(LAS float*)(L + L_AL + 64 + k * 16 + lane * 4) = __builtin_amdgcn_exp2f(m1[k] - Mg) * inv;
        }
      }
      lds_done();
      *(volatile LAS unsigned*)(L + F_AL) = 1u;
    }
    lds_wait_ge<2>(L + F_LAST, 8u);
    {
      const int kk = lane >> 3;
      const int woff = (kk & 3) * 131072 + (kk >> 2) * 8192 + rb * 2048 + (lane & 7) * 16;
#pragma unroll
      for (int t = 0; t < 16; ++t) wv[t] = __builtin_amdgcn_raw_buffer_load_b128(ro, woff, (t & 7) * 16384 + (t >> 3) * 1024, AUX_NT);
    }
    int off2 = WS_P2 + (i >> 2) * 65536 + (4 * g + (i & 3)) * 2048 + lane * 16, offo = (i >> 2) * 1024 + (4 * g + (i & 3)) * 32 + ln * 8 + lg * 2;
    asm volatile("" : "+v"(off2), "+v"(offo));
    SB();
    __builtin_amdgcn_s_setprio(0);
    wg_barrier();
    __builtin_amdgcn_s_sleep(6);
#pragma unroll
    for (int t = 0; t < 16; ++t) *(LAS u32x4*)(L + img_w + img_row(8 * t)) = wv[t];
    lds_done();
    *(volatile LAS unsigned*)(L + F_WO) = 1u;
    wg_barrier();
    {
      u32x4 p0 = {SENT, SENT, SENT, SENT}, p1 = p0;
      __builtin_amdgcn_s_sleep(H2S0);
      poll2(mk_srd(a_ws, WS_END), off2, p0, p1);
      const f32x4 o = __builtin_bit_cast(f32x4, p0) + __builtin_bit_cast(f32x4, p1);
      float v = tsum32(tsum16(o.x, o.y), tsum16(o.z, o.w));
      v += dppf<ROR4>(v); v += dppf<ROR8>(v);
      if (ln < 4) __builtin_amdgcn_raw_buffer_store_b16((unsigned short)pack_bf16(v, v), rout, offo, 0, 0);
      if (blockIdx.x == 0 && lane == 0) __builtin_amdgcn_raw_buffer_store_b64(tagv, rws, 0, 0, AUX_SC1);
      { const unsigned te_ = (unsigned)__builtin_amdgcn_s_memrealtime();
        if (PK >= 2 && PK <= 4) pv_ = 16 * max(0, __builtin_amdgcn_readfirstlane(*(volatile LAS int*)(L + L_FLAG + 28)) + 100);
        if (PK == 5) pv_ = 16 * (int)(te_ - (unsigned)__builtin_amdgcn_readfirstlane(*(volatile LAS unsigned*)(L + F_REF)) - (TOFF + 63 * TAU / 10));
        while ((int)((unsigned)__builtin_amdgcn_s_memrealtime() - te_) < pv_) __builtin_amdgcn_s_sleep(1); }
    }
  } else {
#define CHUNK(c) ((c) * 32768 + ((w + (c)) & 7) * 4096)
    const int voff = lane * 16;
    const __amdgpu_buffer_rsrc_t rk = mk_rsrc(a_k + base, 262144), rv = mk_rsrc(a_v + base, 262144), rws = mk_rsrc(a_ws, WS_END);
    u32x4 kr[32], vr[32];
    const __amdgpu_buffer_rsrc_t rq = mk_rsrc(a_wq, 4194304), rx = mk_rsrc(a_x, 8192);
    const u32x4 wq0 = __builtin_amdgcn_raw_buffer_load_b128(rq, qoff, qs, AUX_NT);
    const u32x4 wq1 = __builtin_amdgcn_raw_buffer_load_b128(rq, qoff, qs1, AUX_NT);
    const u32x4 xq = __builtin_amdgcn_raw_buffer_load_b128(rx, xoff, xs, 0);
#pragma unroll
    for (int n = 0; n < DINIT; ++n) kr[n] = __builtin_amdgcn_raw_buffer_load_b128(rk, koff + n * 1024, c0, AUX_NT);
    SB();
    *(LAS u32x4*)(L + img_w + w * 512) = wq0;
    *(LAS u32x4*)(L + img_w + w * 512 + 128) = wq1;
    *(LAS u32x4*)(L + L_X + w * 272 + ln * 16) = xq;
    lds_done();
    if (lane == 0) lds_count(L + F_WQ);
    SB();
    unsigned tref = t0;
    int lag = 0;
#define ISSUE(n) do { \
      __builtin_amdgcn_s_waitcnt(0x0F70 | (DHI - 1)); \
      if ((n) < NBOOST || lag < EHI) { __builtin_amdgcn_s_waitcnt(0x0F70 | (DMID - 1)); if ((n) >= NTH && lag < ELO) __builtin_amdgcn_s_waitcnt(0x0F70 | (DLO - 1)); } \
      if ((n) < 32) kr[(n) & 31] = __builtin_amdgcn_raw_buffer_load_b128(rk, koff + ((n) & 3) * 1024, CHUNK(((n) & 31) >> 2), AUX_NT); \
      else vr[(n) & 31] = __builtin_amdgcn_raw_buffer_load_b128(rv, voff + ((n) & 3) * 1024, CHUNK(((n) & 31) >> 2), AUX_NT); \
      SB(); \
      if (((n) & 1) == 0 && (n) >= NCAL) { \
        if ((n) >= 24 && (n) <= PARK + 4) { const unsigned r_ = __builtin_amdgcn_readfirstlane(*(volatile LAS unsigned*)(L + F_REF)); tref = r_ ? r_ : tref; } \
        lag = (int)((unsigned)__builtin_amdgcn_s_memrealtime() - tref) - (TOFF + ((n) * TAU) / 10); \
        if ((n) == (PK == 2 ? 34 : PK == 3 ? 48 : 62) && w == 3) *(volatile LAS int*)(L + L_FLAG + 28) = lag; \
        if ((n) == NCAL && w == 3) *(volatile LAS u32x2*)(L + F_CAL) = u32x2{1u, tref + (unsigned)lag}; } \
      if ((n) == TRIGQ || (n) == TRIGS) *(volatile LAS unsigned*)(L + F_POS) = (n); \
      if ((n) == 63 && lane == 0) lds_count(L + F_LAST); \
      SB(); } while (0)
#pragma unroll
    for (int n = DINIT; n < PARK; ++n) {
      ISSUE(n);
      if (n >= 12 && n < 12 + SPILL) { *(LAS u32x4*)(L + L_SP + (w * SPILL + (n - 12)) * 1024 + voff) = kr[n - 12]; SB(); }
    }
    {
    RELANE();
    lds_wait_ge<1>(L + F_Q, 1u);
    const u32x4 qf = *(LAS u32x4*)(L + L_Q + (lane & 3) * 256 + (4 * lg + ((lane >> 2) & 3)) * 16);
    float sc[8];
    constexpr int NQ = PARK + 4;
#pragma unroll
    for (int uu = 0; uu < 32; uu += 4) {
      const int u0 = (uu + SPILL) & 31, t = u0 >> 3;
      f32x4 d[4];
      float y[4];
#pragma unroll
      for (int k = 0; k < 4; ++k) d[k] = mfma(u0 + k < SPILL ? *(LAS u32x4*)(L + L_SP + (w * SPILL + u0 + k) * 1024 + voff) : kr[u0 + k], qf, f32x4{0.f, 0.f, 0.f, 0.f});
      SB();
#pragma unroll
      for (int k = 0; k < 4; ++k) y[k] = d[k][t] + dppf<ROR12>(d[k][(t + 1) & 3]);
      SB();
#pragma unroll
      for (int k = 0; k < 4; ++k) y[k] += dppf<ROR8>(d[k][(t + 2) & 3]);
      SB();
#pragma unroll
      for (int k = 0; k < 4; ++k) y[k] += dppf<ROR4>(d[k][(t + 3) & 3]);
      SB();
#pragma unroll
      for (int k = 0; k < 4; ++k) {
        const int T = (u0 + k) & 7;
        sc[T] = uu < 8 ? y[k] : t == 0 ? bank_set<1>(sc[T], y[k]) : t == 1 ? bank_set<2>(sc[T], y[k]) : t == 2 ? bank_set<4>(sc[T], y[k]) : bank_set<8>(sc[T], y[k]);
      }
      SB();
      if ((uu & 4) == 0) ISSUE(PARK + (uu >> 3));
    }
    float m = fmaxf(fmaxf(fmaxf(sc[0], sc[1]), fmaxf(sc[2], sc[3])), fmaxf(fmaxf(sc[4], sc[5]), fmaxf(sc[6], sc[7])));
    m = fmaxf(m, dppf<ROR4>(m)); m = fmaxf(m, dppf<ROR8>(m)); m = x32max(x16max(m));
    m *= C2;
    float ls = 0.f;
#pragma unroll
    for (int T = 0; T < 8; ++T) { sc[T] = __builtin_amdgcn_exp2f(fmaf(sc[T], C2, -m)); ls += sc[T]; }
    ls += dppf<ROR4>(ls); ls += dppf<ROR8>(ls); ls = tsum16(ls, ls); ls = tsum32(ls, ls);
    if (lane < 4) { *(LAS float*)(L + L_ML + lane * 32 + w * 4) = m; *(LAS float*)(L + L_ML + 128 + lane * 32 + w * 4) = ls; }
    lds_done();
    if (lane == 0) lds_count(L + F_ML);
    u32x4 pa;
#pragma unroll
    for (int k = 0; k < 4; ++k) pa[k] = pack_bf16(sc[2 * k], sc[2 * k + 1]);
    f32x4 acc[2] = {{0.f, 0.f, 0.f, 0.f}, {0.f, 0.f, 0.f, 0.f}}, al[8];
    u32x4 av[4] = {{0u, 0u, 0u, 0u}, {0u, 0u, 0u, 0u}, {0u, 0u, 0u, 0u}, {0u, 0u, 0u, 0u}};
    int off1 = 0, offp = 0, offd = 0, offr = 0;
    SB();
#pragma unroll
    for (int t = 0; t < 4; ++t) {
#pragma unroll
      for (int n = NQ; n < 64; ++n) if ((n > 31 + 8 * t + DHI && n <= 39 + 8 * t + DHI) || (t == 0 && n <= 31 + DHI)) ISSUE(n);
      if (t == 3) {
        off1 = WS_P1 + g * 32768 + (tid >> 5) * 8192 + i * 256 + (tid & 31) * 8;
        offp = WS_P1 + g * 32768 + rb * 8192 + w * 1024 + lane * 16;
        offd = WS_P2 + ln * 65536 + (cb * 4 + w) * 2048 + (g * 4 + rb) * 64 + lg * 16;
        offr = L_RED + w * 2048 + voff;
        asm volatile("" : "+v"(off1), "+v"(offp), "+v"(offd), "+v"(offr));
        if (w < 2) {
          lds_wait_ge<1>(L + F_AL, 1u);
#pragma unroll
          for (int ww = 0; ww < 8; ++ww) al[ww] = *(LAS f32x4*)(L + L_AL + ww * 16);
          asm volatile("" : "+v"(al[0]), "+v"(al[1]), "+v"(al[2]), "+v"(al[3]), "+v"(al[4]), "+v"(al[5]), "+v"(al[6]), "+v"(al[7]));
        }
      }
      u32x4 bb[8];
      if (t == 0) spread_rows<0>(av, pa); else if (t == 1) spread_rows<1>(av, pa); else if (t == 2) spread_rows<2>(av, pa); else spread_rows<3>(av, pa);
      if (t < 3) {
#pragma unroll
        for (int j = 0; j < 8; ++j) {
#pragma unroll
          for (int k = 0; k < 4; ++k) bb[j][k] = __builtin_amdgcn_perm(vr[8 * t + 2 * k + 1][j >> 1], vr[8 * t + 2 * k][j >> 1], (j & 1) ? 0x07060302u : 0x05040100u);
          acc[j >> 2] = mfma(av[j & 3], bb[j], acc[j >> 2]);
        }
        SB();
      } else {
        const unsigned e0 = __builtin_amdgcn_perm(av[1][3], av[0][3], 0x05040100u), e1 = __builtin_amdgcn_perm(av[3][3], av[2][3], 0x05040100u);
        const unsigned f0 = __builtin_amdgcn_perm(av[1][3], av[0][3], 0x07060302u), f1 = __builtin_amdgcn_perm(av[3][3], av[2][3], 0x07060302u);
        u32x4 s0 = {e0, e1, 0u, 0u}, s1 = {0u, 0u, e0, e1}, s2 = {f0, f1, 0u, 0u}, s3 = {0u, 0u, f0, f1};
        asm volatile("" : "+v"(s0), "+v"(s1), "+v"(s2), "+v"(s3));
#pragma unroll
        for (int k = 0; k < 3; ++k) {
#pragma unroll
          for (int j = 0; j < 8; ++j) bb[j][k] = __builtin_amdgcn_perm(vr[24 + 2 * k + 1][j >> 1], vr[24 + 2 * k][j >> 1], (j & 1) ? 0x07060302u : 0x05040100u);
          SB();
        }
#pragma unroll
        for (int j = 0; j < 8; ++j) { bb[j][3] = 0u; acc[j >> 2] = mfma(av[j & 3], bb[j], acc[j >> 2]); }
        SB();
        acc[0] = mfma(s0, vr[30], acc[0]); acc[1] = mfma(s1, vr[30], acc[1]);
        SB();
        acc[0] = mfma(s2, vr[31], acc[0]); acc[1] = mfma(s3, vr[31], acc[1]);
        SB();
      }
    }
    *(LAS f32x4*)(L + offr) = acc[0];
    *(LAS f32x4*)(L + offr + 1024) = acc[1];
    wg_barrier();
    if (w < 2) {
      f32x4 rd[8];
#pragma unroll
      for (int ww = 0; ww < 8; ++ww) rd[ww] = *(LAS f32x4*)(L + L_RED + ww * 2048 + tid * 16);
      SB();
      f32x4 v = rd[0] * al[0];
#pragma unroll
      for (int ww = 1; ww < 8; ++ww) v = __builtin_elementwise_fma(rd[ww], al[ww], v);
      const unsigned h0 = no_sent(pack_bf16(v.x, v.y)), h1 = no_sent(pack_bf16(v.z, v.w));
      const u32x4 pk = {h0, h1, dppu<XOR1>(h0), dppu<XOR1>(h1)};
      if (!(lane & 1)) __builtin_amdgcn_raw_buffer_store_b128(pk, rws, off1, 0, AUX_SC1);
    }
    u32x4 wa[4];
    __builtin_amdgcn_s_sleep(H1S0 - 4);
    lds_wait_ge<1>(L + F_WO, 1u);
#pragma unroll
    for (int ks = 0; ks < 4; ++ks) wa[ks] = tr_frag(L + L_IMG + (w & 3) * 4096 + (lg >> 1) * 512 + (lg & 1) * 128 + ln * 8 + ks * 1024);
    int oiw = L_OI + w * 272 + ln * 16 + lg * 4, oir = L_OI + (ln & 7) * 272 + lg * 16;
    asm volatile("" : "+v"(wa[0]), "+v"(wa[1]), "+v"(wa[2]), "+v"(wa[3]), "+v"(oiw), "+v"(oir));
    {
      u32x4 p = {SENT, SENT, SENT, SENT};
      poll1(mk_srd(a_ws, WS_END), offp, p);
      const float e0 = __uint_as_float(p.x << 16), e1 = __uint_as_float(p.x & 0xFFFF0000u), e2 = __uint_as_float(p.y << 16), e3 = __uint_as_float(p.y & 0xFFFF0000u);
      const float e4 = __uint_as_float(p.z << 16), e5 = __uint_as_float(p.z & 0xFFFF0000u), e6 = __uint_as_float(p.w << 16), e7 = __uint_as_float(p.w & 0xFFFF0000u);
      SB();
      const float a0 = tsum16(e0, e2), a1 = tsum16(e1, e3), a2 = tsum16(e4, e6), a3 = tsum16(e5, e7);
      *(LAS unsigned*)(L + oiw) = pack_bf16(tsum32(a0, a2), tsum32(a1, a3));
    }
    wg_barrier();
    if (w < 4) {
      u32x4 ob[4];
#pragma unroll
      for (int ks = 0; ks < 4; ++ks) ob[ks] = *(LAS u32x4*)(L + oir + ks * 64);
      SB();
      f32x4 d = {0.f, 0.f, 0.f, 0.f};
#pragma unroll
      for (int ks = 0; ks < 4; ++ks) d = mfma(wa[ks], ob[ks], d);
      if (ln < 8) __builtin_amdgcn_raw_buffer_store_b128(u32x4{no_sent(__float_as_uint(d.x)), no_sent(__float_as_uint(d.y)), no_sent(__float_as_uint(d.z)), no_sent(__float_as_uint(d.w))}, rws, offd, 0, AUX_SC1);
    }
    }
  }
}

extern "C" void kernel_launch(void* const* d_in, const int* in_sizes, int n_in, void* d_out, int out_size, void* d_ws, size_t ws_size,
                              hipStream_t stream) {
  gqa_decode_kernel<<<256, NTHR, 0, stream>>>(d_in[1], d_in[2], d_ws, d_in[3], d_in[0], d_in[4], d_out);
}
```

```cpp
#include <hip/hip_runtime.h>
#include <hip/hip_bf16.h>

#define LAS __attribute__((address_space(3)))
typedef unsigned u32x4 __attribute__((ext_vector_type(4)));
typedef unsigned u32x2 __attribute__((ext_vector_type(2)));
typedef float f32x4 __attribute__((ext_vector_type(4)));
typedef short s16x4 __attribute__((ext_vector_type(4)));
typedef __bf16 bf16x8 __attribute__((ext_vector_type(8)));
typedef __bf16 bf16x2 __attribute__((ext_vector_type(2)));
typedef float f32x2 __attribute__((ext_vector_type(2)));
typedef unsigned long long u64;

constexpr int NTHR = 576;
constexpr int TOFF = 90, TAU = 287;
constexpr int DINIT = 4;
constexpr int DLO = 1, DMID = 3, DHI = 6;
constexpr int ELO = -40, EHI = 30;
constexpr int NTH = 16, NBOOST = 28;
constexpr int NCAL = 12, CADJ = 55;
constexpr int SPILL = 4, PARK = 36 + SPILL;
constexpr int TRIGQ = 20, TRIGS = 54;
constexpr int H1S0 = 22, H2S0 = 21;
constexpr int AUX_NT = 2, AUX_SC1 = 16;
constexpr unsigned SENT = 0xFFFFFFFFu;
constexpr unsigned SPIN_CAP = 1u << 16;
constexpr int WS_HI = 1024;
constexpr int WS_T0 = 4096;
constexpr int WS_QG = 8192;
constexpr int WS_P1 = WS_QG + 524288;
constexpr int WS_ST = WS_P1 + 262144;
constexpr int WS_P2 = WS_ST + 8192;
constexpr int WS_END = WS_P2 + 524288;
constexpr int L_IMG = 0;
constexpr int L_RED = 16384;
constexpr int L_Q = 32768;
constexpr int L_ML = L_Q + 1024;
constexpr int L_OI = L_ML + 256;
constexpr int L_AL = L_OI + 8 * 272;
constexpr int L_FLAG = L_AL + 128;
constexpr int L_X = L_FLAG + 48;
constexpr int L_SP = L_X + 8 * 272;
constexpr int L_END = L_SP + 8 * SPILL * 1024;
constexpr int F_Q = L_FLAG, F_ML = L_FLAG + 4, F_POS = L_FLAG + 8, F_LAST = L_FLAG + 12, F_WQ = L_FLAG + 16, F_AL = L_FLAG + 20, F_WO = L_FLAG + 24,
              F_REF = L_FLAG + 32, F_CAL = L_FLAG + 40;

constexpr float C2 = 0.08838834764831845f * 1.4426950408889634f;

__device__ __forceinline__ f32x4 mfma(u32x4 a, u32x4 b, f32x4 c) {
  return __builtin_amdgcn_mfma_f32_16x16x32_bf16(__builtin_bit_cast(bf16x8, a), __builtin_bit_cast(bf16x8, b), c, 0, 0, 0);
}
template <int CTRL> __device__ __forceinline__ float dppf(float x) {
  return __builtin_bit_cast(float, __builtin_amdgcn_mov_dpp(__builtin_bit_cast(int, x), CTRL, 0xf, 0xf, true));
}
template <int CTRL> __device__ __forceinline__ unsigned dppu(unsigned x) {
  return (unsigned)__builtin_amdgcn_mov_dpp((int)x, CTRL, 0xf, 0xf, true);
}
constexpr int ROR4 = 0x124, ROR8 = 0x128, ROR12 = 0x12C, QID = 0xE4, XOR1 = 0xB1, XOR2 = 0x4E;
template <int BANKS> __device__ __forceinline__ float bank_set(float old, float y) {
  return __builtin_bit_cast(float, __builtin_amdgcn_update_dpp(__builtin_bit_cast(int, old), __builtin_bit_cast(int, y), QID, 0xf, BANKS, false));
}
template <int SRC, int DST> __device__ __forceinline__ unsigned bank_move(unsigned old, unsigned y) {
  return (unsigned)__builtin_amdgcn_update_dpp((int)old, (int)y, SRC == DST ? QID : 0x120 + 4 * ((DST - SRC) & 3), 0xf, 1 << DST, false);
}
template <int SRC> __device__ __forceinline__ void spread_rows(u32x4 (&av)[4], u32x4 pa) {
#pragma unroll
  for (int k = 0; k < 4; ++k) {
    av[0][k] = bank_move<SRC, 0>(av[0][k], pa[k]); av[1][k] = bank_move<SRC, 1>(av[1][k], pa[k]);
    av[2][k] = bank_move<SRC, 2>(av[2][k], pa[k]); av[3][k] = bank_move<SRC, 3>(av[3][k], pa[k]);
  }
}
__device__ __forceinline__ float x16max(float x) {
  auto s = __builtin_amdgcn_permlane16_swap(__float_as_uint(x), __float_as_uint(x), false, false);
  return fmaxf(__uint_as_float(s[0]), __uint_as_float(s[1]));
}
__device__ __forceinline__ float x32max(float x) {
  auto s = __builtin_amdgcn_permlane32_swap(__float_as_uint(x), __float_as_uint(x), false, false);
  return fmaxf(__uint_as_float(s[0]), __uint_as_float(s[1]));
}
__device__ __forceinline__ float tsum16(float a, float b) {
  auto s = __builtin_amdgcn_permlane16_swap(__float_as_uint(a), __float_as_uint(b), false, false);
  return __uint_as_float(s[0]) + __uint_as_float(s[1]);
}
__device__ __forceinline__ float tsum32(float a, float b) {
  auto s = __builtin_amdgcn_permlane32_swap(__float_as_uint(a), __float_as_uint(b), false, false);
  return __uint_as_float(s[0]) + __uint_as_float(s[1]);
}
__device__ __forceinline__ unsigned pack_bf16(float lo, float hi) {
  f32x2 v = {lo, hi};
  return __builtin_bit_cast(unsigned, __builtin_convertvector(v, bf16x2));
}
__device__ __forceinline__ unsigned no_sent(unsigned u) { return min(u, SENT - 1u); }
__device__ __forceinline__ bool has_sent(u32x4 u) { return max(max(u.x, u.y), max(u.z, u.w)) == SENT; }
__device__ __forceinline__ void wg_barrier() { asm volatile("s_waitcnt lgkmcnt(0)\n\ts_barrier" ::: "memory"); }
__device__ __forceinline__ void lds_done() { asm volatile("s_waitcnt lgkmcnt(0)" ::: "memory"); }
__device__ __forceinline__ __amdgpu_buffer_rsrc_t mk_rsrc(u64 p, int bytes) { return __builtin_amdgcn_make_buffer_rsrc((void*)p, 0, bytes, 0x00020000); }
__device__ __forceinline__ u32x4 mk_srd(u64 p, int bytes) { return u32x4{(unsigned)p, (unsigned)(p >> 32) & 0xFFFFu, (unsigned)bytes, 0x00020000u}; }
__device__ __forceinline__ void poll1(u32x4 srd, int off, u32x4& r) {
  u64 m = ~0ull;
#pragma unroll 1
  for (unsigned spins = 0; spins < SPIN_CAP; ++spins) {
    asm volatile("s_mov_b64 exec, %2\n\tbuffer_load_dwordx4 %0, %1, %3, 0 offen sc1\n\ts_mov_b64 exec, -1\n\ts_waitcnt vmcnt(0)"
                 : "+v"(r) : "v"(off), "s"(m), "s"(srd) : "memory");
    m = __builtin_amdgcn_ballot_w64(has_sent(r));
    if (m == 0ull) break;
  }
}
__device__ __forceinline__ void poll2(u32x4 srd, int off, u32x4& r0, u32x4& r1) {
  u64 m0 = ~0ull, m1 = ~0ull;
#pragma unroll 1
  for (unsigned spins = 0; spins < SPIN_CAP; ++spins) {
    asm volatile("s_mov_b64 exec, %3\n\tbuffer_load_dwordx4 %0, %2, %5, 0 offen sc1\n\ts_mov_b64 exec, %4\n\tbuffer_load_dwordx4 %1, %2, %5, 0 offen offset:1024 sc1\n\t"
                 "s_mov_b64 exec, -1\n\ts_waitcnt vmcnt(0)"
                 : "+v"(r0), "+v"(r1) : "v"(off), "s"(m0 | 1ull), "s"(m1 | 1ull), "s"(srd) : "memory");
    m0 = __builtin_amdgcn_ballot_w64(has_sent(r0));
    m1 = __builtin_amdgcn_ballot_w64(has_sent(r1));
    if ((m0 | m1) == 0ull) break;
  }
}
__device__ __forceinline__ u32x4 tr_frag(LAS unsigned char* p) {
  const u32x2 lo = __builtin_bit_cast(u32x2, __builtin_amdgcn_ds_read_tr16_b64_v4i16((LAS s16x4*)p));
  const u32x2 hi = __builtin_bit_cast(u32x2, __builtin_amdgcn_ds_read_tr16_b64_v4i16((LAS s16x4*)(p + 256)));
  return u32x4{lo.x, lo.y, hi.x, hi.y};
}
__device__ __forceinline__ int img_row(int k) { return (k & ~12) * 32 + ((k >> 2) & 1) * 256 + ((k >> 3) & 1) * 128; }
template <int SLP> __device__ __forceinline__ void lds_wait_ge(LAS unsigned char* p, unsigned want) {
#pragma unroll 1
  for (unsigned spins = 0; spins < (SPIN_CAP << 6); ++spins) {
    if ((unsigned)__builtin_amdgcn_readfirstlane(*(volatile LAS unsigned*)p) >= want) break;
    __builtin_amdgcn_s_sleep(SLP);
  }
  asm volatile("" ::: "memory");
}
__device__ __forceinline__ void lds_count(LAS unsigned char* p) { __hip_atomic_fetch_add((LAS unsigned*)p, 1u, __ATOMIC_RELAXED, __HIP_MEMORY_SCOPE_WORKGROUP); }
typedef __attribute__((address_space(4))) const u64 karg_t;
__device__ __forceinline__ karg_t* kargs() { karg_t* p = (karg_t*)__builtin_amdgcn_kernarg_segment_ptr(); asm volatile("" : "+s"(p)); return p; }
#define SB() __builtin_amdgcn_sched_barrier(0)
#define RELANE() int tid = threadIdx.x; asm volatile("" : "+v"(tid)); const int lane = tid & 63, lg = lane >> 4, ln = lane & 15

__global__ void __launch_bounds__(NTHR, 3)
gqa_decode_kernel(const void* kc_, const void* vc_, void* ws_, const void* wq_, const void* x_, const void* wo_, void* out_) {
  karg_t* ka = kargs();
  const u64 a_k = ka[0], a_v = ka[1], a_ws = ka[2], a_wq = ka[3], a_x = ka[4];
  const u64 t0w = __builtin_amdgcn_s_memrealtime();
  const unsigned t0 = (unsigned)t0w;
  SB();
  __shared__ __attribute__((aligned(16))) unsigned char smem[L_END];
  LAS unsigned char* L = (LAS unsigned char*)smem;
  const int tid = threadIdx.x, lane = tid & 63, w = __builtin_amdgcn_readfirstlane(tid >> 6);
  const int g = blockIdx.x & 7, i = blockIdx.x >> 3;
  const int lg = lane >> 4, ln = lane & 15;
  const int rb = i >> 3, cb = i & 7;
  const int gq = i & 7, kq = i >> 3, cq = (g + gq) & 7;
  const int img_w = L_IMG + ((lane & 7) >> 1) * 4096 + img_row(lane >> 3) + (lane & 1) * 16;
  if (tid < 12) *(LAS unsigned*)(L + L_FLAG + tid * 4) = 0u;
  int koff = ((lane >> 2) & 3) * 256 + lg * 64 + (lane & 3) * 16;
  int qoff = (lane >> 3) * 8192 + (lane & 7) * 16, xoff = kq * 256 + ln * 16;
  unsigned base = (((i >> 2) * 8 + g) * 4096 + (i & 3) * 1024) * 256;
  int qs = kq * 1048576 + gq * 1024 + cq * 128 + w * 131072, qs1 = qs + 65536, xs = w * 1024, c0 = w * 4096;
  asm volatile("" : "+v"(koff), "+v"(qoff), "+v"(xoff));
  asm volatile("" : "+s"(base), "+s"(qs), "+s"(qs1), "+s"(xs), "+s"(c0) : "s"(t0w));
  wg_barrier();

  if (__builtin_expect(w >= 8, 0)) {
    RELANE();
    const int img_w = L_IMG + ((lane & 7) >> 1) * 4096 + img_row(lane >> 3) + (lane & 1) * 16;
    __builtin_amdgcn_s_setprio(3);
    const __amdgpu_buffer_rsrc_t rws = mk_rsrc(a_ws, WS_END);
    u32x4 wv[16];
    const u32x2 ep = __builtin_amdgcn_raw_buffer_load_b64(rws, 0, 0, AUX_SC1);
    const int sl = cq * 4 + kq;
    {
      const u32x4 sv = {SENT, SENT, SENT, SENT};
      __builtin_amdgcn_raw_buffer_store_b128(sv, rws, WS_P1 + gq * 32768 + sl * 1024 + lane * 16, 0, AUX_SC1);
      if (lane < 2) __builtin_amdgcn_raw_buffer_store_b128(sv, rws, WS_ST + gq * 1024 + sl * 32 + lane * 16, 0, AUX_SC1);
      const int o2 = WS_P2 + lg * 65536 + (cb * 4 + ((lane >> 2) & 3)) * 2048 + (g * 4 + rb) * 64 + (lane & 3) * 16;
      __builtin_amdgcn_raw_buffer_store_b128(sv, rws, o2, 0, AUX_SC1);
      __builtin_amdgcn_raw_buffer_store_b128(sv, rws, o2 + 4 * 65536, 0, AUX_SC1);
    }
    SB();
    lds_wait_ge<1>(L + F_WQ, 8u);
    const u64 tag = (((u64)ep.y << 32) | ep.x) + 1ull;
    const u32x2 tagv = {(unsigned)tag, (unsigned)(tag >> 32)};
    {
      LAS unsigned char* ip = L + L_IMG + (lg >> 1) * 512 + (lg & 1) * 128 + ln * 8;
      LAS unsigned char* xp = L + L_X + (ln & 7) * 272 + lg * 16;
      f32x4 d[4];
#pragma unroll
      for (int ct = 0; ct < 4; ++ct) d[ct] = f32x4{0.f, 0.f, 0.f, 0.f};
#pragma unroll
      for (int ks = 0; ks < 4; ++ks) {
        const u32x4 xb = *(LAS u32x4*)(xp + ks * 64);
#pragma unroll
        for (int ct = 0; ct < 4; ++ct) d[ct] = mfma(tr_frag(ip + ct * 4096 + ks * 1024), xb, d[ct]);
      }
      const int off = WS_QG + gq * 65536 + (ln & 7) * 8192 + kq * 2048 + cq * 256 + (ln >> 3) * 128 + lg * 16;
      __builtin_amdgcn_raw_buffer_store_b128(__builtin_bit_cast(u32x4, ln < 8 ? d[0] : d[2]), rws, off, 0, AUX_SC1);
      __builtin_amdgcn_raw_buffer_store_b128(__builtin_bit_cast(u32x4, ln < 8 ? d[1] : d[3]), rws, off + 64, 0, AUX_SC1);
      asm volatile("s_waitcnt vmcnt(0)" ::: "memory");
      if (lane == 0) __builtin_amdgcn_raw_buffer_store_b64(tagv, rws, WS_HI + gq * 256 + sl * 8, 0, AUX_SC1);
    }
    lds_wait_ge<4>(L + F_CAL, 1u);
    if (lane == 0) __builtin_amdgcn_raw_buffer_store_b64(u32x2{(*(volatile LAS unsigned*)(L + F_CAL + 4) << 16) | (t0 & 0xFFFFu), tagv.x}, rws, WS_T0 + gq * 256 + sl * 8, 0, AUX_SC1);
    const __amdgpu_buffer_rsrc_t ro = mk_rsrc(kargs()[5] + g * 524288 + cb * 128, 524288 - 1024 + 128), rout = mk_rsrc(kargs()[6], 8192);
    lds_wait_ge<4>(L + F_POS, TRIGQ);
    {
#pragma unroll 1
      for (unsigned spins = 0; spins < SPIN_CAP; ++spins) {
        asm volatile("" ::: "memory");
        const u32x2 hi = __builtin_amdgcn_raw_buffer_load_b64(rws, WS_HI + g * 256 + (lane & 31) * 8, 0, AUX_SC1);
        if (!__any(((hi.x ^ tagv.x) | (hi.y ^ tagv.y)) != 0u)) break;
        __builtin_amdgcn_s_sleep(8);
      }
      asm volatile("" ::: "memory");
      const int off = WS_QG + g * 65536 + (i >> 2) * 8192 + lane * 16;
#pragma unroll
      for (int t = 0; t < 8; ++t) wv[t] = __builtin_amdgcn_raw_buffer_load_b128(rws, off + (t & 1) * 1024, (t >> 1) * 2048, AUX_SC1);
      const u32x2 o = __builtin_amdgcn_raw_buffer_load_b64(rws, WS_T0 + g * 256 + (lane & 31) * 8, 0, AUX_SC1);
#pragma unroll
      for (int hh = 0; hh < 2; ++hh) {
        const f32x4 q4 = (__builtin_bit_cast(f32x4, wv[hh]) + __builtin_bit_cast(f32x4, wv[2 + hh])) + (__builtin_bit_cast(f32x4, wv[4 + hh]) + __builtin_bit_cast(f32x4, wv[6 + hh]));
        *(LAS u32x2*)(L + L_Q + hh * 512 + lane * 8) = u32x2{pack_bf16(q4.x, q4.y), pack_bf16(q4.z, q4.w)};
      }
      const bool ok = o.y == tagv.x;
      int dt = ok ? (int)(short)((lane < 32 ? o.x : o.x >> 16) - t0) : 0;
      dt += (int)dppu<XOR1>(dt); dt += (int)dppu<XOR2>(dt); dt += (int)dppu<ROR4>(dt); dt += (int)dppu<ROR8>(dt);
      const int cn = __builtin_popcount((unsigned)__builtin_amdgcn_ballot_w64(ok));
      const int s0 = __builtin_amdgcn_readlane(dt, 0) + __builtin_amdgcn_readlane(dt, 16), s1 = __builtin_amdgcn_readlane(dt, 32) + __builtin_amdgcn_readlane(dt, 48);
      const float mean = (float)max(s0, s1 + CADJ * cn) * __builtin_amdgcn_rcpf((float)cn);
      if (lane == 0 && cn >= 16) *(volatile LAS unsigned*)(L + F_REF) = t0 + (unsigned)(int)mean;
      lds_done();
      *(volatile LAS unsigned*)(L + F_Q) = 1u;
    }
    lds_wait_ge<4>(L + F_ML, 8u);
    const f32x4 m0 = *(LAS f32x4*)(L + L_ML + (lane & 3) * 32), m1 = *(LAS f32x4*)(L + L_ML + (lane & 3) * 32 + 16);
    {
      const f32x4 l0 = *(LAS f32x4*)(L + L_ML + 128 + (lane & 3) * 32), l1 = *(LAS f32x4*)(L + L_ML + 128 + (lane & 3) * 32 + 16);
      const float M = fmaxf(fmaxf(fmaxf(m0.x, m0.y), fmaxf(m0.z, m0.w)), fmaxf(fmaxf(m1.x, m1.y), fmaxf(m1.z, m1.w)));
      float Ls = 0.f;
#pragma unroll
      for (int k = 0; k < 4; ++k) Ls += l0[k] * __builtin_amdgcn_exp2f(m0[k] - M) + l1[k] * __builtin_amdgcn_exp2f(m1[k] - M);
      if (lane < 4) __builtin_amdgcn_raw_buffer_store_b64(u32x2{no_sent(__float_as_uint(M)), no_sent(__float_as_uint(Ls))}, rws, WS_ST + g * 1024 + i * 32 + lane * 8, 0, AUX_SC1);
    }
    lds_wait_ge<4>(L + F_POS, TRIGS);
    {
      u32x2 st;
#pragma unroll 1
      for (unsigned spins = 0; spins < SPIN_CAP; ++spins) {
        asm volatile("" ::: "memory");
        st = __builtin_amdgcn_raw_buffer_load_b64(rws, WS_ST + g * 1024 + (i >> 2) * 128 + ln * 8, 0, AUX_SC1);
        if (!__any(max(st.x, st.y) == SENT)) break;
        __builtin_amdgcn_s_sleep(8);
      }
      const float Ms = __uint_as_float(st.x);
      float Mg = fmaxf(Ms, dppf<ROR4>(Ms)); Mg = fmaxf(Mg, dppf<ROR8>(Mg));
      float Lg = __uint_as_float(st.y) * __builtin_amdgcn_exp2f(Ms - Mg);
      Lg += dppf<ROR4>(Lg); Lg += dppf<ROR8>(Lg);
      const float inv = 1.0f / Lg;
      if (lane < 4) {
#pragma unroll
        for (int k = 0; k < 4; ++k) {
          *(LAS float*)(L + L_AL + k * 16 + lane * 4) = __builtin_amdgcn_exp2f(m0[k] - Mg) * inv;
          *(LAS float*)(L + L_AL + 64 + k * 16 + lane * 4) = __builtin_amdgcn_exp2f(m1[k] - Mg) * inv;
        }
      }
      lds_done();
      *(volatile LAS unsigned*)(L + F_AL) = 1u;
    }
    lds_wait_ge<2>(L + F_LAST, 8u);
    {
      const int kk = lane >> 3;
      const int woff = (kk & 3) * 131072 + (kk >> 2) * 8192 + rb * 2048 + (lane & 7) * 16;
#pragma unroll
      for (int t = 0; t < 16; ++t) wv[t] = __builtin_amdgcn_raw_buffer_load_b128(ro, woff, (t & 7) * 16384 + (t >> 3) * 1024, AUX_NT);
    }
    int off2 = WS_P2 + (i >> 2) * 65536 + (4 * g + (i & 3)) * 2048 + lane * 16, offo = (i >> 2) * 1024 + (4 * g + (i & 3)) * 32 + ln * 8 + lg * 2;
    asm volatile("" : "+v"(off2), "+v"(offo));
    SB();
    __builtin_amdgcn_s_setprio(0);
    wg_barrier();
    __builtin_amdgcn_s_sleep(6);
#pragma unroll
    for (int t = 0; t < 16; ++t) *(LAS u32x4*)(L + img_w + img_row(8 * t)) = wv[t];
    lds_done();
    *(volatile LAS unsigned*)(L + F_WO) = 1u;
    wg_barrier();
    {
      u32x4 p0 = {SENT, SENT, SENT, SENT}, p1 = p0;
      __builtin_amdgcn_s_sleep(H2S0);
      poll2(mk_srd(a_ws, WS_END), off2, p0, p1);
      const f32x4 o = __builtin_bit_cast(f32x4, p0) + __builtin_bit_cast(f32x4, p1);
      float v = tsum32(tsum16(o.x, o.y), tsum16(o.z, o.w));
      v += dppf<ROR4>(v); v += dppf<ROR8>(v);
      if (ln < 4) __builtin_amdgcn_raw_buffer_store_b16((unsigned short)pack_bf16(v, v), rout, offo, 0, 0);
      if (blockIdx.x == 0 && lane == 0) __builtin_amdgcn_raw_buffer_store_b64(tagv, rws, 0, 0, AUX_SC1);
    }
  } else {
#define SUB(n) ((((n) + w + i) & 3) * 1024)
#define CHUNK(c) ((c) * 32768 + ((w + (c)) & 7) * 4096)
    const int voff = lane * 16;
    const __amdgpu_buffer_rsrc_t rk = mk_rsrc(a_k + base, 262144), rv = mk_rsrc(a_v + base, 262144), rws = mk_rsrc(a_ws, WS_END);
    u32x4 kr[32], vr[32];
    const __amdgpu_buffer_rsrc_t rq = mk_rsrc(a_wq, 4194304), rx = mk_rsrc(a_x, 8192);
    const u32x4 wq0 = __builtin_amdgcn_raw_buffer_load_b128(rq, qoff, qs, AUX_NT);
    const u32x4 wq1 = __builtin_amdgcn_raw_buffer_load_b128(rq, qoff, qs1, AUX_NT);
    const u32x4 xq = __builtin_amdgcn_raw_buffer_load_b128(rx, xoff, xs, 0);
#pragma unroll
    for (int n = 0; n < DINIT; ++n) kr[n] = __builtin_amdgcn_raw_buffer_load_b128(rk, koff, c0 + SUB(n), AUX_NT);
    SB();
    *(LAS u32x4*)(L + img_w + w * 512) = wq0;
    *(LAS u32x4*)(L + img_w + w * 512 + 128) = wq1;
    *(LAS u32x4*)(L + L_X + w * 272 + ln * 16) = xq;
    lds_done();
    if (lane == 0) lds_count(L + F_WQ);
    SB();
    unsigned tref = t0;
    int lag = 0;
#define ISSUE(n) do { \
      __builtin_amdgcn_s_waitcnt(0x0F70 | (DHI - 1)); \
      if ((n) < NBOOST || lag < EHI) { __builtin_amdgcn_s_waitcnt(0x0F70 | (DMID - 1)); if ((n) >= NTH && lag < ELO) __builtin_amdgcn_s_waitcnt(0x0F70 | (DLO - 1)); } \
      if ((n) < 32) kr[(n) & 31] = __builtin_amdgcn_raw_buffer_load_b128(rk, koff, CHUNK(((n) & 31) >> 2) + SUB(n), AUX_NT); \
      else vr[(n) & 31] = __builtin_amdgcn_raw_buffer_load_b128(rv, voff, CHUNK(((n) & 31) >> 2) + SUB(n), AUX_NT); \
      SB(); \
      if (((n) & 1) == 0 && (n) >= NCAL) { \
        if ((n) >= 24 && (n) <= PARK + 4) { const unsigned r_ = __builtin_amdgcn_readfirstlane(*(volatile LAS unsigned*)(L + F_REF)); tref = r_ ? r_ : tref; } \
        lag = (int)((unsigned)__builtin_amdgcn_s_memrealtime() - tref) - (TOFF + ((n) * TAU) / 10); \
        if ((n) == NCAL && w == 3) *(volatile LAS u32x2*)(L + F_CAL) = u32x2{1u, tref + (unsigned)lag}; } \
      if ((n) == TRIGQ || (n) == TRIGS) *(volatile LAS unsigned*)(L + F_POS) = (n); \
      if ((n) == 63 && lane == 0) lds_count(L + F_LAST); \
      SB(); } while (0)
#pragma unroll
    for (int n = DINIT; n < PARK; ++n) {
      ISSUE(n);
      if (n >= 12 && n < 12 + SPILL) { *(LAS u32x4*)(L + L_SP + (w * SPILL + (n - 12)) * 1024 + voff) = kr[n - 12]; SB(); }
    }
    {
    RELANE();
    lds_wait_ge<1>(L + F_Q, 1u);
    const u32x4 qf = *(LAS u32x4*)(L + L_Q + (lane & 3) * 256 + (4 * lg + ((lane >> 2) & 3)) * 16);
    float sc[8];
    constexpr int NQ = PARK + 4;
#pragma unroll
    for (int uu = 0; uu < 32; uu += 4) {
      const int u0 = (uu + SPILL) & 31, t = u0 >> 3;
      f32x4 d[4];
      float y[4];
#pragma unroll
      for (int k = 0; k < 4; ++k) d[k] = mfma(u0 + k < SPILL ? *(LAS u32x4*)(L + L_SP + (w * SPILL + u0 + k) * 1024 + voff) : kr[u0 + k], qf, f32x4{0.f, 0.f, 0.f, 0.f});
      SB();
#pragma unroll
      for (int k = 0; k < 4; ++k) y[k] = d[k][t] + dppf<ROR12>(d[k][(t + 1) & 3]);
      SB();
#pragma unroll
      for (int k = 0; k < 4; ++k) y[k] += dppf<ROR8>(d[k][(t + 2) & 3]);
      SB();
#pragma unroll
      for (int k = 0; k < 4; ++k) y[k] += dppf<ROR4>(d[k][(t + 3) & 3]);
      SB();
#pragma unroll
      for (int k = 0; k < 4; ++k) {
        const int T = (u0 + k) & 7;
        sc[T] = uu < 8 ? y[k] : t == 0 ? bank_set<1>(sc[T], y[k]) : t == 1 ? bank_set<2>(sc[T], y[k]) : t == 2 ? bank_set<4>(sc[T], y[k]) : bank_set<8>(sc[T], y[k]);
      }
      SB();
      if ((uu & 4) == 0) ISSUE(PARK + (uu >> 3));
    }
    float m = fmaxf(fmaxf(fmaxf(sc[0], sc[1]), fmaxf(sc[2], sc[3])), fmaxf(fmaxf(sc[4], sc[5]), fmaxf(sc[6], sc[7])));
    m = fmaxf(m, dppf<ROR4>(m)); m = fmaxf(m, dppf<ROR8>(m)); m = x32max(x16max(m));
    m *= C2;
    float ls = 0.f;
#pragma unroll
    for (int T = 0; T < 8; ++T) { sc[T] = __builtin_amdgcn_exp2f(fmaf(sc[T], C2, -m)); ls += sc[T]; }
    ls += dppf<ROR4>(ls); ls += dppf<ROR8>(ls); ls = tsum16(ls, ls); ls = tsum32(ls, ls);
    if (lane < 4) { *(LAS float*)(L + L_ML + lane * 32 + w * 4) = m; *(LAS float*)(L + L_ML + 128 + lane * 32 + w * 4) = ls; }
    lds_done();
    if (lane == 0) lds_count(L + F_ML);
    u32x4 pa;
#pragma unroll
    for (int k = 0; k < 4; ++k) pa[k] = pack_bf16(sc[2 * k], sc[2 * k + 1]);
    f32x4 acc[2] = {{0.f, 0.f, 0.f, 0.f}, {0.f, 0.f, 0.f, 0.f}}, al[8];
    u32x4 av[4] = {{0u, 0u, 0u, 0u}, {0u, 0u, 0u, 0u}, {0u, 0u, 0u, 0u}, {0u, 0u, 0u, 0u}};
    int off1 = 0, offp = 0, offd = 0, offr = 0;
    SB();
#pragma unroll
    for (int t = 0; t < 4; ++t) {
#pragma unroll
      for (int n = NQ; n < 64; ++n) if ((n > 31 + 8 * t + DHI && n <= 39 + 8 * t + DHI) || (t == 0 && n <= 31 + DHI)) ISSUE(n);
      if (t == 3) {
        off1 = WS_P1 + g * 32768 + (tid >> 5) * 8192 + i * 256 + (tid & 31) * 8;
        offp = WS_P1 + g * 32768 + rb * 8192 + w * 1024 + lane * 16;
        offd = WS_P2 + ln * 65536 + (cb * 4 + w) * 2048 + (g * 4 + rb) * 64 + lg * 16;
        offr = L_RED + w * 2048 + voff;
        asm volatile("" : "+v"(off1), "+v"(offp), "+v"(offd), "+v"(offr));
        if (w < 2) {
          lds_wait_ge<1>(L + F_AL, 1u);
#pragma unroll
          for (int ww = 0; ww < 8; ++ww) al[ww] = *(LAS f32x4*)(L + L_AL + ww * 16);
          asm volatile("" : "+v"(al[0]), "+v"(al[1]), "+v"(al[2]), "+v"(al[3]), "+v"(al[4]), "+v"(al[5]), "+v"(al[6]), "+v"(al[7]));
        }
      }
      u32x4 bb[8];
      if (t == 0) spread_rows<0>(av, pa); else if (t == 1) spread_rows<1>(av, pa); else if (t == 2) spread_rows<2>(av, pa); else spread_rows<3>(av, pa);
      if (t < 3) {
#pragma unroll
        for (int j = 0; j < 8; ++j) {
#pragma unroll
          for (int k = 0; k < 4; ++k) bb[j][k] = __builtin_amdgcn_perm(vr[8 * t + 2 * k + 1][j >> 1], vr[8 * t + 2 * k][j >> 1], (j & 1) ? 0x07060302u : 0x05040100u);
          acc[j >> 2] = mfma(av[j & 3], bb[j], acc[j >> 2]);
        }
        SB();
      } else {
        const unsigned e0 = __builtin_amdgcn_perm(av[1][3], av[0][3], 0x05040100u), e1 = __builtin_amdgcn_perm(av[3][3], av[2][3], 0x05040100u);
        const unsigned f0 = __builtin_amdgcn_perm(av[1][3], av[0][3], 0x07060302u), f1 = __builtin_amdgcn_perm(av[3][3], av[2][3], 0x07060302u);
        u32x4 s0 = {e0, e1, 0u, 0u}, s1 = {0u, 0u, e0, e1}, s2 = {f0, f1, 0u, 0u}, s3 = {0u, 0u, f0, f1};
        asm volatile("" : "+v"(s0), "+v"(s1), "+v"(s2), "+v"(s3));
#pragma unroll
        for (int k = 0; k < 3; ++k) {
#pragma unroll
          for (int j = 0; j < 8; ++j) bb[j][k] = __builtin_amdgcn_perm(vr[24 + 2 * k + 1][j >> 1], vr[24 + 2 * k][j >> 1], (j & 1) ? 0x07060302u : 0x05040100u);
          SB();
        }
#pragma unroll
        for (int j = 0; j < 8; ++j) { bb[j][3] = 0u; acc[j >> 2] = mfma(av[j & 3], bb[j], acc[j >> 2]); }
        SB();
        acc[0] = mfma(s0, vr[30], acc[0]); acc[1] = mfma(s1, vr[30], acc[1]);
        SB();
        acc[0] = mfma(s2, vr[31], acc[0]); acc[1] = mfma(s3, vr[31], acc[1]);
        SB();
      }
    }
    *(LAS f32x4*)(L + offr) = acc[0];
    *(LAS f32x4*)(L + offr + 1024) = acc[1];
    wg_barrier();
    if (w < 2) {
      f32x4 rd[8];
#pragma unroll
      for (int ww = 0; ww < 8; ++ww) rd[ww] = *(LAS f32x4*)(L + L_RED + ww * 2048 + tid * 16);
      SB();
      f32x4 v = rd[0] * al[0];
#pragma unroll
      for (int ww = 1; ww < 8; ++ww) v = __builtin_elementwise_fma(rd[ww], al[ww], v);
      const unsigned h0 = no_sent(pack_bf16(v.x, v.y)), h1 = no_sent(pack_bf16(v.z, v.w));
      const u32x4 pk = {h0, h1, dppu<XOR1>(h0), dppu<XOR1>(h1)};
      if (!(lane & 1)) __builtin_amdgcn_raw_buffer_store_b128(pk, rws, off1, 0, AUX_SC1);
    }
    u32x4 wa[4];
    __builtin_amdgcn_s_sleep(H1S0 - 4);
    lds_wait_ge<1>(L + F_WO, 1u);
#pragma unroll
    for (int ks = 0; ks < 4; ++ks) wa[ks] = tr_frag(L + L_IMG + (w & 3) * 4096 + (lg >> 1) * 512 + (lg & 1) * 128 + ln * 8 + ks * 1024);
    int oiw = L_OI + w * 272 + ln * 16 + lg * 4, oir = L_OI + (ln & 7) * 272 + lg * 16;
    asm volatile("" : "+v"(wa[0]), "+v"(wa[1]), "+v"(wa[2]), "+v"(wa[3]), "+v"(oiw), "+v"(oir));
    {
      u32x4 p = {SENT, SENT, SENT, SENT};
      poll1(mk_srd(a_ws, WS_END), offp, p);
      const float e0 = __uint_as_float(p.x << 16), e1 = __uint_as_float(p.x & 0xFFFF0000u), e2 = __uint_as_float(p.y << 16), e3 = __uint_as_float(p.y & 0xFFFF0000u);
      const float e4 = __uint_as_float(p.z << 16), e5 = __uint_as_float(p.z & 0xFFFF0000u), e6 = __uint_as_float(p.w << 16), e7 = __uint_as_float(p.w & 0xFFFF0000u);
      SB();
      const float a0 = tsum16(e0, e2), a1 = tsum16(e1, e3), a2 = tsum16(e4, e6), a3 = tsum16(e5, e7);
      *(LAS unsigned*)(L + oiw) = pack_bf16(tsum32(a0, a2), tsum32(a1, a3));
    }
    wg_barrier();
    if (w < 4) {
      u32x4 ob[4];
#pragma unroll
      for (int ks = 0; ks < 4; ++ks) ob[ks] = *(LAS u32x4*)(L + oir + ks * 64);
      SB();
      f32x4 d = {0.f, 0.f, 0.f, 0.f};
#pragma unroll
      for (int ks = 0; ks < 4; ++ks) d = mfma(wa[ks], ob[ks], d);
      if (ln < 8) __builtin_amdgcn_raw_buffer_store_b128(u32x4{no_sent(__float_as_uint(d.x)), no_sent(__float_as_uint(d.y)), no_sent(__float_as_uint(d.z)), no_sent(__float_as_uint(d.w))}, rws, offd, 0, AUX_SC1);
    }
    }
  }
}

extern "C" void kernel_launch(void* const* d_in, const int* in_sizes, int n_in, void* d_out, int out_size, void* d_ws, size_t ws_size,
                              hipStream_t stream) {
  gqa_decode_kernel<<<256, NTHR, 0, stream>>>(d_in[1], d_in[2], d_ws, d_in[3], d_in[0], d_in[4], d_out);
}
```
